```python
import torch

def _softmax(x, axis=-1):
    return torch.softmax(x, dim=axis)

def _gelu(x):
    return torch.nn.functional.gelu(x, approximate='tanh')

def _silu(x):
    return torch.nn.functional.silu(x)

def _sigmoid(x):
    return torch.sigmoid(x)

def _layernorm(x, axis=-1, eps=1e-05):
    m = x.mean(dim=axis, keepdim=True)
    v = x.var(dim=axis, keepdim=True, unbiased=False)
    return (x - m) / torch.sqrt(v + eps)

def _scatter_add(tgt, idx, src, axis=0):
    return tgt.clone().index_add_(axis, idx, src)

def _segment_sum(src, seg, n):
    out = torch.zeros((n,) + src.shape[1:], dtype=src.dtype)
    return out.index_add_(0, seg, src)

def _pad(x, pw):
    flat = tuple((v for pair in reversed(pw) for v in pair))
    return torch.nn.functional.pad(x, flat)
kernel_name = 'adaLN_cond__ssm_conv_s4__mixer_gmlp__prompt'

def setup_inputs():

    _w = lambda a: (lambda t: t.to(torch.bfloat16) if t.is_floating_point() else t)(torch.as_tensor(a))

    def _A_setup():
        B, L, D, N = (1, 2048, 640, 128)
        A_re = -(lambda _x: torch.exp(torch.as_tensor(_x)))((lambda *s: torch.randn(*s, dtype=torch.bfloat16) * 3.2)(D, N))
        A_im = torch.pi * (lambda *s: torch.randn(*s, dtype=torch.bfloat16) * 0.1)(D, N)
        B_re = (lambda *s: torch.randn(*s, dtype=torch.bfloat16) * 0.2)(D, N)
        B_im = (lambda *s: torch.randn(*s, dtype=torch.bfloat16) * 0.2)(D, N)
        C_re = (lambda *s: torch.randn(*s, dtype=torch.bfloat16) * 0.2)(D, N)
        C_im = (lambda *s: torch.randn(*s, dtype=torch.bfloat16) * 0.2)(D, N)
        log_dt = (lambda *s: torch.randn(*s, dtype=torch.bfloat16) * 0.1)(D) * 0.5 - 4.0
        Dskip = (lambda *s: torch.randn(*s, dtype=torch.bfloat16) * 0.1)(D)
        return {'u': _w((lambda *s: torch.randn(*s, dtype=torch.bfloat16) * 0.05)(B, L, D)), 'A_re': _w(A_re), 'A_im': _w(A_im), 'B_re': _w(B_re), 'B_im': _w(B_im), 'C_re': _w(C_re), 'C_im': _w(C_im), 'log_dt': _w(log_dt), 'Dskip': _w(Dskip)}
    _A_in = _A_setup()

    def _B_setup():
        B, S, D, F = (1, 2048, 640, 1728)
        Fh = F // 2
        return {'x': _w((lambda *s: torch.randn(*s, dtype=torch.bfloat16) * 0.05)(B, S, D)), 'w_in': _w((lambda *s: torch.randn(*s, dtype=torch.bfloat16) * 0.2)(D, 2 * Fh)), 'w_sp': _w(0.01 * (lambda *s: torch.randn(*s, dtype=torch.bfloat16) * 0.2)(S, S)), 'b_sp': _w(1.0 + 0.01 * (lambda *s: torch.randn(*s, dtype=torch.bfloat16) * 0.05)(S)), 'w_out': _w((lambda *s: torch.randn(*s, dtype=torch.bfloat16) * 0.05)(Fh, D))}
    _B_in = _B_setup()
    return {'A_u': _A_in['u'], 'A_A_re': _A_in['A_re'], 'A_A_im': _A_in['A_im'], 'A_B_re': _A_in['B_re'], 'A_B_im': _A_in['B_im'], 'A_C_re': _A_in['C_re'], 'A_C_im': _A_in['C_im'], 'A_log_dt': _A_in['log_dt'], 'A_Dskip': _A_in['Dskip'], 'B_x': _B_in['x'], 'B_w_in': _B_in['w_in'], 'B_w_sp': _B_in['w_sp'], 'B_b_sp': _B_in['b_sp'], 'B_w_out': _B_in['w_out'], 'c': _w((lambda *s: torch.randn(*s, dtype=torch.bfloat16) * 0.1)(1, 640) * 8.0), 'w_mod': _w((lambda *s: torch.randn(*s, dtype=torch.bfloat16) * 0.1)(640, 6 * 640) * 640 ** (-0.5) * 8.0)}

def reference(A_u, A_A_re, A_A_im, A_B_re, A_B_im, A_C_re, A_C_im, A_log_dt, A_Dskip, B_x, B_w_in, B_w_sp, B_b_sp, B_w_out, c, w_mod):

    def _A_ref(u, A_re, A_im, B_re, B_im, C_re, C_im, log_dt, Dskip):
        Bsz, L, D = u.shape
        dt = torch.exp(log_dt)[:, None]
        A = A_re + 1j * A_im
        Bc = B_re + 1j * B_im
        Cc = C_re + 1j * C_im
        logdA = dt * A
        dB = dt * Bc
        l = (lambda x, dt: x.to(dt))(torch.arange(L), A_re.dtype)
        P = torch.exp(logdA[:, :, None] * l[None, None, :])
        K = torch.einsum('dn,dnl,dn->dl', Cc, P, dB).real
        L2 = 2 * L
        Kp = _pad(K, ((0, 0), (0, L)))
        up = (lambda x, *d: x.permute(*d))(u, 0, 2, 1)
        up = _pad(up, ((0, 0), (0, 0), (0, L)))
        Kf = (lambda x, axes=(-2, -1): torch.fft.rfft2(x.float(), dim=axes))(Kp[:, None, :], axes=(-2, -1))
        Uf = (lambda x, axes=(-2, -1): torch.fft.rfft2(x.float(), dim=axes))(up[:, :, None, :], axes=(-2, -1))
        Yf = Uf * Kf[None, :, :, :]
        y = (lambda x, s, axes=(-2, -1): torch.fft.irfft2(x, s=s, dim=axes).to(torch.bfloat16))(Yf, (1, L2), axes=(-2, -1))[:, :, 0, :L]
        y = (lambda x, *d: x.permute(*d))(y, 0, 2, 1) + u * Dskip[None, None, :]
        return y

    def _B_ref(x, w_in, w_sp, b_sp, w_out):
        B, S, D = x.shape
        F = w_in.shape[-1]
        Fh = F // 2
        z = _gelu(torch.einsum('bsd,df->bsf', x, w_in))
        z1 = z[..., :Fh].clone()
        z2 = _layernorm(z[..., Fh:], axis=-1)
        g = torch.einsum('ts,bsf->btf', w_sp, z2) + b_sp[None, :, None]
        return torch.einsum('bsf,fd->bsd', z1 * g, w_out)
    x = A_u
    mod = (_silu(c) @ w_mod).reshape(c.shape[0], 6, 1, -1)
    sa, ca, ga, sb, cb, gb = (mod[:, 0].clone(), mod[:, 1].clone(), mod[:, 2].clone(), mod[:, 3].clone(), mod[:, 4].clone(), mod[:, 5].clone())
    h = _layernorm(x, axis=-1) * (1 + ca) + sa
    x = x + ga * _A_ref(h, A_A_re, A_A_im, A_B_re, A_B_im, A_C_re, A_C_im, A_log_dt, A_Dskip)
    h = _layernorm(x, axis=-1) * (1 + cb) + sb
    x = x + gb * _B_ref(h, B_w_in, B_w_sp, B_b_sp, B_w_out)
    return x
```

```cpp
#include <hip/hip_runtime.h>
#include <hip/hip_bf16.h>
#include <stdint.h>

typedef unsigned v4u __attribute__((ext_vector_type(4)));
typedef unsigned v2u __attribute__((ext_vector_type(2)));
typedef float f32x4 __attribute__((ext_vector_type(4)));
typedef float f32x2 __attribute__((ext_vector_type(2)));
typedef short bf16x8 __attribute__((ext_vector_type(8)));
typedef _Float16 f16x8 __attribute__((ext_vector_type(8)));
typedef _Float16 f16x2 __attribute__((ext_vector_type(2)));
typedef __bf16 bf16x2_t __attribute__((ext_vector_type(2)));
typedef unsigned short u16;
#define GAS __attribute__((address_space(1)))
#define LAS __attribute__((address_space(3)))
#define RLX_AGENT __ATOMIC_RELAXED, __HIP_MEMORY_SCOPE_AGENT
#define DEVI __device__ __forceinline__

constexpr int T = 2048, D = 640, NST = 128, FH = 864, F2 = 1728;
constexpr int KL = 2080;
constexpr int KIMG_B = 2 * KL * 2;
constexpr size_t O_MODP = 0;
constexpr size_t O_STAT1 = O_MODP + 4 * 3840 * 4;
constexpr size_t O_ZST = O_STAT1 + 2048 * 8;
constexpr size_t O_U = O_ZST + 32 * 8 * 64 * 8;
constexpr size_t O_XG = (O_U + 1728 * 8 + 4095) / 4096 * 4096;
constexpr size_t O_X1G = O_XG + (size_t)T * D * 2;
constexpr size_t O_KIMG = O_X1G + (size_t)T * D * 2;
constexpr size_t O_WINF = (O_KIMG + (size_t)D * KIMG_B + 4095) / 4096 * 4096;
constexpr size_t O_WSPF = O_WINF + (size_t)108 * 20 * 1024;
constexpr size_t O_WOUTF = O_WSPF + (size_t)128 * 64 * 1024;
constexpr size_t O_Z2N = O_WOUTF + (size_t)40 * 27 * 1024;
constexpr size_t O_Z1 = O_Z2N + (size_t)54 * 64 * 1024;
constexpr size_t O_P = O_Z1 + (size_t)54 * 128 * 512;
constexpr size_t O_END = O_P + (size_t)128 * 27 * 1024;

DEVI float bf2f(unsigned b) { return __builtin_bit_cast(float, b << 16); }
DEVI float bflo(unsigned u) { return __builtin_bit_cast(float, u << 16); }
DEVI float bfhi(unsigned u) { return __builtin_bit_cast(float, u & 0xffff0000u); }
DEVI unsigned pk_bf16(float a, float b) {
  f32x2 v = {a, b};
  bf16x2_t r = __builtin_convertvector(v, bf16x2_t);
  return __builtin_bit_cast(unsigned, r);
}
DEVI float rbf(float a) { return bflo(pk_bf16(a, 0.f)); }
DEVI unsigned pk_f16(float a, float b) {
  f32x2 v = {a, b};
  f16x2 r = __builtin_convertvector(v, f16x2);
  return __builtin_bit_cast(unsigned, r);
}
DEVI f32x4 mfma_bf(v4u a, v4u b, f32x4 c) {
  return __builtin_amdgcn_mfma_f32_16x16x32_bf16(__builtin_bit_cast(bf16x8, a), __builtin_bit_cast(bf16x8, b), c, 0, 0, 0);
}
DEVI f32x4 mfma_h(v4u a, v4u b, f32x4 c) {
  return __builtin_amdgcn_mfma_f32_16x16x32_f16(__builtin_bit_cast(f16x8, a), __builtin_bit_cast(f16x8, b), c, 0, 0, 0);
}
DEVI float modval(const float* modp, int idx) {
  return rbf((modp[idx] + modp[3840 + idx]) + (modp[2 * 3840 + idx] + modp[3 * 3840 + idx]));
}
DEVI float gelu_t(float x) {
  float u = x * (1.0f + 0.044715f * x * x) * (2.0f * 0.7978845608028654f * 1.4426950408889634f);
  float e = __builtin_amdgcn_exp2f(-u);
  return x * __builtin_amdgcn_rcpf(1.0f + e);
}

constexpr size_t O_STAMP = 200u << 20;
#define STAMP(kid, idx) do {} while (0)
struct Cx { float re, im; };
DEVI Cx cmul(Cx a, Cx b) { return Cx{a.re * b.re - a.im * b.im, a.re * b.im + a.im * b.re}; }
DEVI Cx cexpl(float are2, float arev, float l) {
  float m = __builtin_amdgcn_exp2f(are2 * l);
  float ph = __builtin_amdgcn_fractf(arev * l);
  return Cx{m * __builtin_amdgcn_cosf(ph), m * __builtin_amdgcn_sinf(ph)};
}

struct Params {
  const u16 *u, *a_re, *a_im, *b_re, *b_im, *c_re, *c_im, *log_dt, *dskip, *w_in, *w_sp, *b_sp, *w_out, *cvec, *w_mod;
  u16* out;
  unsigned char* ws;
};

constexpr int K1_KGEN = 640, K1_MOD = 240, K1_STAT = 128, K1_WSP = 128;
constexpr int RS = 132;

DEVI void kgen_block(const Params& p, unsigned* lds, int d) {
  const int tid = threadIdx.x, n = tid & 127, half = tid >> 7, lane = tid & 63, w = tid >> 6;
  unsigned* rowsL = lds;
  unsigned* colsL = lds + 16 * RS;
  float* scl = (float*)(lds + 64 * RS);
  const float dt = rbf(__expf(bf2f(p.log_dt[d])));
  const float are = dt * bf2f(p.a_re[d * NST + n]), aim = dt * bf2f(p.a_im[d * NST + n]);
  const float are2 = are * 1.4426950408889634f, arev = aim * 0.15915494309189535f;
  if (half == 0) {
    Cx W, Wo;
    {
      const int o = d * NST + n, o2 = d * NST + (n ^ 64);
      Cx b{dt * bf2f(p.b_re[o]), dt * bf2f(p.b_im[o])}, c{bf2f(p.c_re[o]), bf2f(p.c_im[o])};
      W = cmul(c, b);
      Cx b2{dt * bf2f(p.b_re[o2]), dt * bf2f(p.b_im[o2])}, c2{bf2f(p.c_re[o2]), bf2f(p.c_im[o2])};
      Wo = cmul(c2, b2);
    }
    float mx = fmaxf(fmaxf(fabsf(W.re), fabsf(W.im)), fmaxf(fabsf(Wo.re), fabsf(Wo.im)));
#pragma unroll
    for (int o = 32; o >= 1; o >>= 1) mx = fmaxf(mx, __shfl_xor(mx, o));
    unsigned eb = (__builtin_bit_cast(unsigned, mx) >> 23) & 0xffu;
    eb = eb < 2u ? 2u : (eb > 250u ? 250u : eb);
    const float s = __builtin_bit_cast(float, (eb + 1u) << 23), is = __builtin_bit_cast(float, (253u - eb) << 23);
    if (tid == 0) scl[0] = s;
    Cx r{W.re * is, W.im * is};
    const Cx e8 = cexpl(are2, arev, 8.f);
#pragma unroll
    for (int b = 0; b < 16; ++b) {
      rowsL[b * RS + n] = pk_f16(r.re, r.im);
      r = cmul(r, e8);
    }
    const Cx e128 = cexpl(are2, arev, 128.f);
    Cx e = cexpl(are2, arev, 1024.f);
#pragma unroll
    for (int c = 0; c < 9; ++c) {
      colsL[(32 + c) * RS + n] = pk_f16(e.re, -e.im);
      e = cmul(e, e128);
    }
  } else {
    Cx er[8];
    er[0] = Cx{1.f, 0.f};
    er[1] = cexpl(are2, arev, 1.f);
    er[2] = cmul(er[1], er[1]); er[3] = cmul(er[2], er[1]); er[4] = cmul(er[2], er[2]);
    er[5] = cmul(er[4], er[1]); er[6] = cmul(er[3], er[3]); er[7] = cmul(er[4], er[3]);
    const Cx e128 = cexpl(are2, arev, 128.f);
#pragma unroll
    for (int r = 0; r < 8; ++r) {
      colsL[r * RS + n] = pk_f16(er[r].re, -er[r].im);
      Cx v = cmul(e128, er[r]);
      colsL[(8 + r) * RS + n] = pk_f16(v.re, -v.im);
    }
    Cx e = cmul(e128, e128);
#pragma unroll
    for (int a = 0; a < 2; ++a) {
#pragma unroll
      for (int r = 0; r < 4; ++r) {
        Cx v = cmul(e, er[2 * r]);
        colsL[(16 + 4 * a + r) * RS + n] = pk_f16(v.re, -v.im);
      }
      e = cmul(e, e128);
    }
#pragma unroll
    for (int a = 0; a < 4; ++a) {
      colsL[(24 + 2 * a) * RS + n] = pk_f16(e.re, -e.im);
      Cx v = cmul(e, er[4]);
      colsL[(25 + 2 * a) * RS + n] = pk_f16(v.re, -v.im);
      e = cmul(e, e128);
    }
  }
  __syncthreads();
  f32x4 acc = {0.f, 0.f, 0.f, 0.f};
  const int c = lane & 15, g = lane >> 4;
  if (w < 3) {
#pragma unroll
    for (int ks = 0; ks < 8; ++ks) {
      v4u a = *(const v4u*)(rowsL + c * RS + 16 * ks + 4 * g);
      v4u b = *(const v4u*)(colsL + (16 * w + c) * RS + 16 * ks + 4 * g);
      acc = mfma_h(a, b, acc);
    }
  }
  const float s = scl[0];
  __syncthreads();
  u16* tab = (u16*)lds;
  if (w < 3) {
    int cv;
    if (w == 0) cv = 128 * (c >> 3) + (c & 7);
    else if (w == 1) cv = c < 8 ? 256 + 128 * (c >> 2) + 2 * (c & 3) : 512 + 128 * ((c - 8) >> 1) + 4 * (c & 1);
    else cv = 1024 + 128 * c;
#pragma unroll
    for (int r = 0; r < 4; ++r) {
      const int l = 8 * (4 * g + r) + cv;
      if (l <= 2048) tab[l] = (u16)(pk_bf16(acc[r] * s, 0.f) & 0xffffu);
    }
  }
  __syncthreads();
  unsigned char* img = p.ws + O_KIMG + (size_t)d * KIMG_B;
  for (int j = tid; j < KL / 8; j += 256) {
    unsigned v[9];
#pragma unroll
    for (int i = 0; i < 9; ++i) {
      const int idx = 2048 - (8 * j + i);
      const int r = (int)rbf((float)idx);
      v[i] = (idx >= 0 && idx <= 2047) ? (unsigned)tab[r < 0 ? 0 : r] : 0u;
    }
    v4u c0 = {v[0] | (v[1] << 16), v[2] | (v[3] << 16), v[4] | (v[5] << 16), v[6] | (v[7] << 16)};
    v4u c1 = {v[1] | (v[2] << 16), v[3] | (v[4] << 16), v[5] | (v[6] << 16), v[7] | (v[8] << 16)};
    *(v4u*)(img + j * 16) = c0;
    *(v4u*)(img + KL * 2 + j * 16) = c1;
  }
}

DEVI void mod_block(const Params& p, float* lds, int b) {
  const int tid = threadIdx.x, cg = b >> 2, kq = b & 3, rg = tid >> 3, cl = tid & 7, lane = tid & 63, w = tid >> 6;
  v4u wv[5]; float sv[5];
#pragma unroll
  for (int i = 0; i < 5; ++i) {
    const int row = 160 * kq + 32 * i + rg;
    wv[i] = *(const v4u*)(p.w_mod + (size_t)row * 3840 + 64 * cg + 8 * cl);
    const float c = bf2f(p.cvec[row]);
    sv[i] = rbf(c / (1.0f + __expf(-c)));
  }
  float acc[8];
#pragma unroll
  for (int j = 0; j < 8; ++j) acc[j] = 0.f;
#pragma unroll
  for (int i = 0; i < 5; ++i)
#pragma unroll
    for (int j = 0; j < 4; ++j) {
      acc[2 * j] += sv[i] * bflo(wv[i][j]);
      acc[2 * j + 1] += sv[i] * bfhi(wv[i][j]);
    }
#pragma unroll
  for (int j = 0; j < 8; ++j) {
    acc[j] += __shfl_xor(acc[j], 8);
    acc[j] += __shfl_xor(acc[j], 16);
    acc[j] += __shfl_xor(acc[j], 32);
  }
  if (lane < 8) {
#pragma unroll
    for (int j = 0; j < 8; ++j) lds[w * 64 + lane * 8 + j] = acc[j];
  }
  __syncthreads();
  if (tid < 64) {
    float* modp = (float*)(p.ws + O_MODP);
    modp[kq * 3840 + 64 * cg + tid] = (lds[tid] + lds[64 + tid]) + (lds[128 + tid] + lds[192 + tid]);
  }
}

DEVI void stat_block(const Params& p, float* lds, int b) {
  const int tid = threadIdx.x, lane = tid & 63, w = tid >> 6;
  const int row = lane >> 2, q = lane & 3;
  const int t = 16 * b + row;
  v4u xv[5];
#pragma unroll
  for (int i = 0; i < 5; ++i) xv[i] = *(const v4u*)(p.u + (size_t)t * D + 8 * (20 * w + 4 * i + q));
  float s = 0.f;
#pragma unroll
  for (int i = 0; i < 5; ++i)
#pragma unroll
    for (int j = 0; j < 4; ++j) s += bflo(xv[i][j]) + bfhi(xv[i][j]);
  s += __shfl_xor(s, 1); s += __shfl_xor(s, 2);
  if (q == 0) lds[w * 16 + row] = s;
  unsigned char* xg = p.ws + O_XG;
#pragma unroll
  for (int i = 0; i < 5; ++i) {
    const int u8 = 20 * w + 4 * i + q;
    *(v2u*)(xg + ((size_t)(2 * u8) * T + t) * 8) = v2u{xv[i][0], xv[i][1]};
    *(v2u*)(xg + ((size_t)(2 * u8 + 1) * T + t) * 8) = v2u{xv[i][2], xv[i][3]};
  }
  __syncthreads();
  const float m = ((lds[row] + lds[16 + row]) + (lds[32 + row] + lds[48 + row])) * (1.0f / D);
  float v = 0.f;
#pragma unroll
  for (int i = 0; i < 5; ++i)
#pragma unroll
    for (int j = 0; j < 4; ++j) {
      float a = bflo(xv[i][j]) - m, c = bfhi(xv[i][j]) - m;
      v += a * a + c * c;
    }
  v += __shfl_xor(v, 1); v += __shfl_xor(v, 2);
  if (q == 0) lds[64 + w * 16 + row] = v;
  __syncthreads();
  if (tid < 16) {
    const float mm = ((lds[tid] + lds[16 + tid]) + (lds[32 + tid] + lds[48 + tid])) * (1.0f / D);
    const float var = ((lds[64 + tid] + lds[80 + tid]) + (lds[96 + tid] + lds[112 + tid])) * (1.0f / D);
    ((f32x2*)(p.ws + O_STAT1))[16 * b + tid] = f32x2{mm, 1.0f / sqrtf(var + 1e-5f)};
  }
  if (tid < 128) ((unsigned long long*)(p.ws + O_ZST))[b * 128 + tid] = ~0ull;
}

DEVI void wsp_block(const Params& p, int tb) {
  const int tid = threadIdx.x, lane = tid & 63, w = tid >> 6;
  const int row = lane >> 2, q = lane & 3;
  const u16* src = p.w_sp + (size_t)(16 * tb + row) * T + 8 * q;
  unsigned char* dst = p.ws + O_WSPF + (size_t)tb * 65536 + (q * 16 + row) * 16;
  v4u v[16];
#pragma unroll
  for (int i = 0; i < 16; ++i) v[i] = __builtin_nontemporal_load((const v4u*)(src + 32 * (16 * w + i)));
#pragma unroll
  for (int i = 0; i < 16; ++i) *(v4u*)(dst + (16 * w + i) * 1024) = v[i];
}

__global__ void __launch_bounds__(256, 4) k1_prep(Params p) {
  __shared__ __attribute__((aligned(16))) unsigned lds[64 * RS + 4];
  int b = blockIdx.x;
  STAMP(0, 0);
  if (b < K1_KGEN) { kgen_block(p, lds, b); }
  else if (b < K1_KGEN + K1_MOD) { mod_block(p, (float*)lds, b - K1_KGEN); }
  else if (b < K1_KGEN + K1_MOD + K1_STAT) { stat_block(p, (float*)lds, b - K1_KGEN - K1_MOD); }
  else wsp_block(p, b - K1_KGEN - K1_MOD - K1_STAT);
  STAMP(0, 15);
}

constexpr int HL = 2304;
constexpr int K2_LDS = 4 * KIMG_B + 4 * HL * 2;

DEVI void conv_block(const Params& p, unsigned char* lds, int q4) {
  const int tid = threadIdx.x, lane = tid & 63, w = __builtin_amdgcn_readfirstlane(tid >> 6);
  unsigned char* kimg = lds;
  u16* himg = (u16*)(lds + 4 * KIMG_B);
  {
    const unsigned char* src = p.ws + O_KIMG + (size_t)(4 * q4) * KIMG_B;
#pragma unroll
    for (int i = 0; i < 9; ++i) {
      const int unit = 256 * i + tid;
      if (unit < 2080)
        __builtin_amdgcn_global_load_lds((const GAS void*)(src + unit * 16), (LAS void*)(kimg + (256 * i + 64 * w) * 16), 16, 0, 0);
    }
  }
  v4u xv[4]; f32x4 st[4];
#pragma unroll
  for (int i = 0; i < 4; ++i) {
    const int pr = 256 * i + tid;
    xv[i] = *(const v4u*)(p.ws + O_XG + ((size_t)q4 * T + 2 * pr) * 8);
    st[i] = *(const f32x4*)(p.ws + O_STAT1 + (size_t)(2 * pr) * 8);
  }
  const float* modp = (const float*)(p.ws + O_MODP);
  float sa[4], c1[4], ga[4], dk[4];
#pragma unroll
  for (int c = 0; c < 4; ++c) {
    const int d = 4 * q4 + c;
    sa[c] = modval(modp, d);
    c1[c] = rbf(1.0f + modval(modp, D + d));
    ga[c] = modval(modp, 2 * D + d);
    dk[c] = bf2f(p.dskip[d]);
  }
  if (tid < 128) *(v4u*)(himg + (tid >> 5) * HL + (tid & 31) * 8) = v4u{0, 0, 0, 0};
  unsigned hv[4][4];
#pragma unroll
  for (int i = 0; i < 4; ++i) {
    const int pr = 256 * i + tid;
    const float x0[4] = {bflo(xv[i][0]), bfhi(xv[i][0]), bflo(xv[i][1]), bfhi(xv[i][1])};
    const float x1[4] = {bflo(xv[i][2]), bfhi(xv[i][2]), bflo(xv[i][3]), bfhi(xv[i][3])};
#pragma unroll
    for (int c = 0; c < 4; ++c) {
      const float h0 = (x0[c] - st[i][0]) * st[i][1] * c1[c] + sa[c];
      const float h1 = (x1[c] - st[i][2]) * st[i][3] * c1[c] + sa[c];
      hv[i][c] = pk_bf16(h0, h1);
      *(unsigned*)(himg + c * HL + 256 + 2 * pr) = hv[i][c];
    }
  }
  asm volatile("s_waitcnt vmcnt(0)" ::: "memory");
  __syncthreads();
  STAMP(1, 1);
  const int i16 = lane & 15, g = lane >> 4;
  const unsigned* abase = (const unsigned*)(kimg + w * KIMG_B + (i16 & 1) * (KL * 2)) + ((2032 - (i16 & ~1) - 2 * (i16 & 1) + 8 * g) >> 1);
  abase += (i16 & 1) ? 0 : 0;
  const u16* bbase = himg + w * HL + 256 - 16 + 16 * i16 + 8 * g;
  f32x4 acc[8];
#pragma unroll
  for (int c = 0; c < 8; ++c) acc[c] = f32x4{0.f, 0.f, 0.f, 0.f};
#pragma unroll 1
  for (int r = 0; r < 8; ++r) {
    v4u A[8], B[8];
#pragma unroll
    for (int a = 0; a < 8; ++a) {
      const unsigned* ap = abase - 16 * (8 * a + r);
      A[a] = v4u{ap[0], ap[1], ap[2], ap[3]};
      B[a] = *(const v4u*)(bbase + 32 * (8 * a - r));
    }
#pragma unroll
    for (int c = 0; c < 8; ++c)
#pragma unroll
      for (int a = 0; a <= c; ++a) acc[c] = mfma_bf(A[a], B[c - a], acc[c]);
  }
  __syncthreads();
  STAMP(1, 2);
  u16* ybuf = (u16*)lds;
#pragma unroll
  for (int c = 0; c < 8; ++c)
    *(v2u*)(ybuf + w * T + 256 * c + 16 * i16 + 4 * g) = v2u{pk_bf16(acc[c][0], acc[c][1]), pk_bf16(acc[c][2], acc[c][3])};
  __syncthreads();
#pragma unroll
  for (int i = 0; i < 4; ++i) {
    const int pr = 256 * i + tid;
    const float x0[4] = {bflo(xv[i][0]), bfhi(xv[i][0]), bflo(xv[i][1]), bfhi(xv[i][1])};
    const float x1[4] = {bflo(xv[i][2]), bfhi(xv[i][2]), bflo(xv[i][3]), bfhi(xv[i][3])};
    float o0[4], o1[4];
#pragma unroll
    for (int c = 0; c < 4; ++c) {
      const unsigned yv = *(const unsigned*)(ybuf + c * T + 2 * pr);
      o0[c] = x0[c] + ga[c] * (bflo(yv) + bflo(hv[i][c]) * dk[c]);
      o1[c] = x1[c] + ga[c] * (bfhi(yv) + bfhi(hv[i][c]) * dk[c]);
    }
    *(v4u*)(p.ws + O_X1G + ((size_t)q4 * T + 2 * pr) * 8) =
        v4u{pk_bf16(o0[0], o0[1]), pk_bf16(o0[2], o0[3]), pk_bf16(o1[0], o1[1]), pk_bf16(o1[2], o1[3])};
  }
}

DEVI void win_block(const Params& p, unsigned char* lds, int b) {
  const int tid = threadIdx.x, fp = tid & 15, rgp = tid >> 4;
  float* c1L = (float*)lds;
  float* sbL = c1L + D;
  float* red = sbL + D;
  const float* modp = (const float*)(p.ws + O_MODP);
  unsigned wv[5][8];
#pragma unroll
  for (int i = 0; i < 5; ++i)
#pragma unroll
    for (int j = 0; j < 8; ++j)
      wv[i][j] = *(const unsigned*)(p.w_in + (size_t)(8 * (rgp + 16 * i) + j) * F2 + 32 * b + 2 * fp);
  for (int d = tid; d < D; d += 256) {
    sbL[d] = modval(modp, 3 * D + d);
    c1L[d] = rbf(1.0f + modval(modp, 4 * D + d));
  }
  __syncthreads();
  float u1a = 0.f, u1b = 0.f, u2a = 0.f, u2b = 0.f;
#pragma unroll
  for (int i = 0; i < 5; ++i) {
    const int e = rgp + 16 * i;
    unsigned lo[8], hi[8];
#pragma unroll
    for (int j = 0; j < 8; ++j) {
      const float c1 = c1L[8 * e + j], sb = sbL[8 * e + j];
      const float a = bflo(wv[i][j]), c = bfhi(wv[i][j]);
      const unsigned r = pk_bf16(a * c1, c * c1);
      u1a += bflo(r); u1b += bfhi(r);
      u2a += sb * a; u2b += sb * c;
      lo[j] = r & 0xffffu; hi[j] = r >> 16;
    }
    const int f = 32 * b + 2 * fp, nb = f >> 4;
    unsigned char* dst = p.ws + O_WINF + ((size_t)nb * 20 + (e >> 2)) * 1024 + ((e & 3) * 16 + (f & 15)) * 16;
    *(v4u*)dst = v4u{lo[0] | (lo[1] << 16), lo[2] | (lo[3] << 16), lo[4] | (lo[5] << 16), lo[6] | (lo[7] << 16)};
    *(v4u*)(dst + 16) = v4u{hi[0] | (hi[1] << 16), hi[2] | (hi[3] << 16), hi[4] | (hi[5] << 16), hi[6] | (hi[7] << 16)};
  }
  *(f32x4*)(red + (rgp * 16 + fp) * 4) = f32x4{u1a, u2a, u1b, u2b};
  __syncthreads();
  if (tid < 64) {
    float s = 0.f;
#pragma unroll
    for (int r = 0; r < 16; ++r) s += red[r * 64 + tid];
    ((float*)(p.ws + O_U))[64 * b + tid] = s;
  }
}

DEVI void wout_block(const Params& p, int b, int nb) {
  const int tid = threadIdx.x, lane = tid & 63, w = tid >> 6;
  for (int u = b * 4 + w; u < 540; u += nb * 4) {
    const int dc = u % 5, kg = u / 5, ks = kg >> 2, g = kg & 3;
    const int d = 128 * dc + 2 * lane;
    unsigned wv[8];
#pragma unroll
    for (int j = 0; j < 8; ++j) wv[j] = *(const unsigned*)(p.w_out + (size_t)(32 * ks + 16 * (j >> 2) + 4 * g + (j & 3)) * D + d);
    unsigned char* dst = p.ws + O_WOUTF + ((size_t)(d >> 4) * 27 + ks) * 1024 + (g * 16 + (d & 15)) * 16;
    *(v4u*)dst = v4u{(wv[0] & 0xffffu) | (wv[1] << 16), (wv[2] & 0xffffu) | (wv[3] << 16), (wv[4] & 0xffffu) | (wv[5] << 16), (wv[6] & 0xffffu) | (wv[7] << 16)};
    *(v4u*)(dst + 16) = v4u{(wv[0] >> 16) | (wv[1] & 0xffff0000u), (wv[2] >> 16) | (wv[3] & 0xffff0000u), (wv[4] >> 16) | (wv[5] & 0xffff0000u), (wv[6] >> 16) | (wv[7] & 0xffff0000u)};
  }
}

__global__ void __launch_bounds__(256, 1) k2_conv(Params p) {
  extern __shared__ __attribute__((aligned(16))) unsigned char smem[];
  const int b = blockIdx.x;
  STAMP(1, 0);
  if (b < 160) conv_block(p, smem, b);
  else if (b < 214) win_block(p, smem, b - 160);
  else wout_block(p, b - 214, 42);
  STAMP(1, 15);
}

DEVI void tile_map(int b, int& panel, int& j) {
  const int x = b & 7, i = b >> 3;
  panel = (x >> 1) * 8 + (i >> 2);
  j = (x & 1) * 4 + (i & 3);
}
DEVI int blk0(int j) { return j < 6 ? 7 * j : 42 + 6 * (j - 6); }
DEVI int nblk(int j) { return j < 6 ? 7 : 6; }

constexpr int K3_LDS = 81920 + 4096;

__global__ void __launch_bounds__(256, 1) k3_gemm1(Params p) {
  extern __shared__ __attribute__((aligned(16))) unsigned char smem[];
  const int tid = threadIdx.x, lane = tid & 63, w = __builtin_amdgcn_readfirstlane(tid >> 6);
  const int i16 = lane & 15, g = lane >> 4;
  int panel, j;
  tile_map(blockIdx.x, panel, j);
  const int b0 = blk0(j), nb = nblk(j);
  STAMP(2, 0);
  unsigned char* xs = smem;
  float* stL = (float*)(smem + 81920);
  float* redL = stL + 128;
  float* sxL = redL + 512;
#pragma unroll
  for (int i = 0; i < 20; ++i) {
    const int piece = 4 * i + w, q = 2 * piece + (lane >> 5), pp = lane & 31;
    const int tl = (2 * pp) ^ (4 * (piece & 1));
    __builtin_amdgcn_global_load_lds((const GAS void*)(p.ws + O_X1G + ((size_t)q * T + 64 * panel + tl) * 8), (LAS void*)(xs + piece * 1024), 16, 0, 0);
  }
  const unsigned char* wf = p.ws + O_WINF + lane * 16;
  const int lb0 = w, lb1 = w + 4;
  const bool has1 = lb1 < nb;
  const unsigned char* z2p0 = wf + (size_t)(54 + b0 + lb0) * 20480;
  const unsigned char* z2p1 = wf + (size_t)(54 + b0 + (has1 ? lb1 : lb0)) * 20480;
  const unsigned char* z1p0 = wf + (size_t)(b0 + lb0) * 20480;
  const unsigned char* z1p1 = wf + (size_t)(b0 + (has1 ? lb1 : lb0)) * 20480;
  v4u B0[20], B1[20];
#pragma unroll
  for (int ks = 0; ks < 20; ++ks) { B0[ks] = *(const v4u*)(z2p0 + ks * 1024); B1[ks] = *(const v4u*)(z2p1 + ks * 1024); }
  asm volatile("s_waitcnt vmcnt(0)" ::: "memory");
  __syncthreads();
  STAMP(2, 1);
  int aoff[4];
#pragma unroll
  for (int i = 0; i < 4; ++i) {
    const int rb = (i + w) & 3;
    const int t = 32 * (rb >> 1) + 8 * (i16 >> 2) + 4 * (rb & 1) + (i16 & 3);
    aoff[i] = (2 * g) * 512 + ((t ^ (4 * (g & 1))) * 8);
  }
  const v4u ones = {0x3f803f80u, 0x3f803f80u, 0x3f803f80u, 0x3f803f80u};
  f32x4 a0[4], a1[4], gram = {0.f, 0.f, 0.f, 0.f};
#pragma unroll
  for (int i = 0; i < 4; ++i) { a0[i] = f32x4{0.f, 0.f, 0.f, 0.f}; a1[i] = a0[i]; }
#pragma unroll
  for (int ks = 0; ks < 20; ++ks) {
    v4u A[4];
#pragma unroll
    for (int i = 0; i < 4; ++i) {
      const v2u lo = *(const v2u*)(xs + ks * 4096 + aoff[i]), hi = *(const v2u*)(xs + ks * 4096 + aoff[i] + 512);
      A[i] = v4u{lo[0], lo[1], hi[0], hi[1]};
    }
    const v4u b1 = (w == 3) ? ones : B1[ks];
#pragma unroll
    for (int i = 0; i < 4; ++i) { a0[i] = mfma_bf(A[i], B0[ks], a0[i]); a1[i] = mfma_bf(A[i], b1, a1[i]); }
    gram = mfma_bf(A[0], A[0], gram);
  }
#pragma unroll
  for (int ks = 0; ks < 20; ++ks) { B0[ks] = *(const v4u*)(z1p0 + ks * 1024); B1[ks] = *(const v4u*)(z1p1 + ks * 1024); }
  if (w == 3 && i16 == 0) {
#pragma unroll
    for (int i = 0; i < 4; ++i) {
      const int rb = (i + w) & 3;
      *(f32x4*)(sxL + 32 * (rb >> 1) + 8 * g + 4 * (rb & 1)) = a1[i];
    }
  }
  if ((i16 >> 2) == g) {
    const int rb = w;
    const float v = (i16 & 3) == 0 ? gram[0] : (i16 & 3) == 1 ? gram[1] : (i16 & 3) == 2 ? gram[2] : gram[3];
    sxL[64 + 32 * (rb >> 1) + 8 * g + 4 * (rb & 1) + (i16 & 3)] = v;
  }
  __syncthreads();
  STAMP(2, 2);
  if (tid < 64) {
    const float m = sxL[tid] * (1.0f / D);
    const float var = fmaxf(sxL[64 + tid] * (1.0f / D) - m * m, 0.f);
    stL[2 * tid] = m; stL[2 * tid + 1] = 1.0f / sqrtf(var + 1e-5f);
  }
  __syncthreads();
  const f32x2* U = (const f32x2*)(p.ws + O_U);
  const f32x2 u0 = U[FH + 16 * (b0 + lb0) + i16], u1 = U[FH + 16 * (b0 + (has1 ? lb1 : lb0)) + i16];
  float s1[4][4], s2[4][4];
#pragma unroll
  for (int i = 0; i < 4; ++i) {
    const int rb = (i + w) & 3;
    const int t0 = 32 * (rb >> 1) + 8 * g + 4 * (rb & 1);
    const f32x4 sA = *(const f32x4*)(stL + 2 * t0), sB = *(const f32x4*)(stL + 2 * t0 + 4);
    const float mm[4] = {sA[0], sA[2], sB[0], sB[2]}, rr[4] = {sA[1], sA[3], sB[1], sB[3]};
#pragma unroll
    for (int r = 0; r < 4; ++r) {
      const float v0 = rbf(gelu_t(rbf(rr[r] * (a0[i][r] - mm[r] * u0[0]) + u0[1])));
      const float v1 = has1 ? rbf(gelu_t(rbf(rr[r] * (a1[i][r] - mm[r] * u1[0]) + u1[1]))) : 0.f;
      a0[i][r] = v0; a1[i][r] = v1;
      s1[i][r] = v0 + v1; s2[i][r] = v0 * v0 + v1 * v1;
    }
  }
#pragma unroll
  for (int i = 0; i < 4; ++i)
#pragma unroll
    for (int r = 0; r < 4; ++r) {
#pragma unroll
      for (int o = 1; o <= 8; o <<= 1) { s1[i][r] += __shfl_xor(s1[i][r], o); s2[i][r] += __shfl_xor(s2[i][r], o); }
    }
  if (i16 == 0) {
#pragma unroll
    for (int i = 0; i < 4; ++i) {
      const int rb = (i + w) & 3;
      const int t0 = 32 * (rb >> 1) + 8 * g + 4 * (rb & 1);
#pragma unroll
      for (int r = 0; r < 4; ++r) *(f32x2*)(redL + (w * 64 + t0 + r) * 2) = f32x2{s1[i][r], s2[i][r]};
    }
  }
  __syncthreads();
  STAMP(2, 3);
  GAS unsigned long long* zst = (GAS unsigned long long*)(p.ws + O_ZST) + panel * 512;
  if (tid < 64) {
    f32x2 s = *(f32x2*)(redL + tid * 2);
#pragma unroll
    for (int ww = 1; ww < 4; ++ww) s += *(f32x2*)(redL + (ww * 64 + tid) * 2);
    const float sx = s[0], sy = s[1];
    unsigned lo = __builtin_bit_cast(unsigned, sx), hi = __builtin_bit_cast(unsigned, sy);
    if (lo == 0xffffffffu) lo = 0x7fc00000u;
    __hip_atomic_store(zst + j * 64 + tid, ((unsigned long long)hi << 32) | lo, RLX_AGENT);
  }
  f32x4 c0[4], c1[4];
#pragma unroll
  for (int i = 0; i < 4; ++i) { c0[i] = f32x4{0.f, 0.f, 0.f, 0.f}; c1[i] = c0[i]; }
  int boff[4];
#pragma unroll
  for (int i = 0; i < 4; ++i) boff[i] = (2 * g) * 512 + (((16 * i + i16) ^ (4 * (g & 1))) * 8);
#pragma unroll
  for (int ks = 0; ks < 20; ++ks) {
    v4u A[4];
#pragma unroll
    for (int i = 0; i < 4; ++i) {
      const v2u lo = *(const v2u*)(xs + ks * 4096 + boff[i]), hi = *(const v2u*)(xs + ks * 4096 + boff[i] + 512);
      A[i] = v4u{lo[0], lo[1], hi[0], hi[1]};
    }
#pragma unroll
    for (int i = 0; i < 4; ++i) { c0[i] = mfma_bf(B0[ks], A[i], c0[i]); c1[i] = mfma_bf(B1[ks], A[i], c1[i]); }
  }
  {
    f32x2 ua[4], ub[4];
#pragma unroll
    for (int r = 0; r < 4; ++r) { ua[r] = U[16 * (b0 + lb0) + 4 * g + r]; ub[r] = U[16 * (b0 + (has1 ? lb1 : lb0)) + 4 * g + r]; }
#pragma unroll
    for (int i = 0; i < 4; ++i) {
      const f32x2 s = *(const f32x2*)(stL + 2 * (16 * i + i16));
      float v[4], x[4];
#pragma unroll
      for (int r = 0; r < 4; ++r) {
        v[r] = gelu_t(rbf(s[1] * (c0[i][r] - s[0] * ua[r][0]) + ua[r][1]));
        x[r] = gelu_t(rbf(s[1] * (c1[i][r] - s[0] * ub[r][0]) + ub[r][1]));
      }
      unsigned char* z1 = p.ws + O_Z1 + (size_t)(4 * panel + i) * 512 + lane * 8;
      *(v2u*)(z1 + (size_t)(b0 + lb0) * 65536) = v2u{pk_bf16(v[0], v[1]), pk_bf16(v[2], v[3])};
      if (has1) *(v2u*)(z1 + (size_t)(b0 + lb1) * 65536) = v2u{pk_bf16(x[0], x[1]), pk_bf16(x[2], x[3])};
    }
  }
  __syncthreads();
  STAMP(2, 4);
  {
    const int row = tid & 63;
    unsigned long long v0, v1;
    for (unsigned spin = 0; spin < (1u << 20); ++spin) {
      v0 = __hip_atomic_load(zst + w * 64 + row, RLX_AGENT);
      v1 = __hip_atomic_load(zst + (w + 4) * 64 + row, RLX_AGENT);
      if (__all((unsigned)v0 != 0xffffffffu && (unsigned)v1 != 0xffffffffu)) break;
      __builtin_amdgcn_s_sleep(1);
    }
    const float a = __builtin_bit_cast(float, (unsigned)v0) + __builtin_bit_cast(float, (unsigned)v1);
    const float b = __builtin_bit_cast(float, (unsigned)(v0 >> 32)) + __builtin_bit_cast(float, (unsigned)(v1 >> 32));
    *(f32x2*)(redL + (w * 64 + row) * 2) = f32x2{a, b};
  }
  __syncthreads();
  if (tid < 64) {
    f32x2 s = *(f32x2*)(redL + tid * 2);
#pragma unroll
    for (int ww = 1; ww < 4; ++ww) s += *(f32x2*)(redL + (ww * 64 + tid) * 2);
    const float m = s[0] * (1.0f / FH);
    const float var = fmaxf(s[1] * (1.0f / FH) - m * m, 0.f);
    stL[2 * tid] = m; stL[2 * tid + 1] = 1.0f / sqrtf(var + 1e-5f);
  }
  __syncthreads();
  STAMP(2, 5);
  {
    unsigned o0[4][2], o1[4][2];
#pragma unroll
    for (int i = 0; i < 4; ++i) {
      const int rb = (i + w) & 3;
      const int t0 = 32 * (rb >> 1) + 8 * g + 4 * (rb & 1);
      const f32x4 sA = *(const f32x4*)(stL + 2 * t0), sB = *(const f32x4*)(stL + 2 * t0 + 4);
      o0[i][0] = pk_bf16((a0[i][0] - sA[0]) * sA[1], (a0[i][1] - sA[2]) * sA[3]);
      o0[i][1] = pk_bf16((a0[i][2] - sB[0]) * sB[1], (a0[i][3] - sB[2]) * sB[3]);
      o1[i][0] = pk_bf16((a1[i][0] - sA[0]) * sA[1], (a1[i][1] - sA[2]) * sA[3]);
      o1[i][1] = pk_bf16((a1[i][2] - sB[0]) * sB[1], (a1[i][3] - sB[2]) * sB[3]);
      unsigned char* z = p.ws + O_Z2N + (size_t)(2 * panel + (rb >> 1)) * 1024 + lane * 16 + (rb & 1) * 8;
      *(v2u*)(z + (size_t)(b0 + lb0) * 65536) = v2u{o0[i][0], o0[i][1]};
      if (has1) *(v2u*)(z + (size_t)(b0 + lb1) * 65536) = v2u{o1[i][0], o1[i][1]};
    }
  }
  STAMP(2, 15);
}

constexpr int K4_LDS = 4 * 21 * 1024;
__global__ void __launch_bounds__(256, 1) k4_gemm2(Params p) {
  extern __shared__ __attribute__((aligned(16))) unsigned char smem[];
  const int tid = threadIdx.x, lane = tid & 63, w = __builtin_amdgcn_readfirstlane(tid >> 6);
  const int i16 = lane & 15, g = lane >> 4;
  int panel, j;
  tile_map(blockIdx.x, panel, j);
  const int b0 = blk0(j), nb = nblk(j);
  STAMP(3, 0);
  const unsigned char* zb = p.ws + O_Z2N + (size_t)b0 * 65536 + lane * 16;
  const unsigned char* wb = p.ws + O_WSPF + (size_t)(4 * panel) * 65536 + lane * 16;
  const int fb6 = nb > 6 ? 6 : 5;
  f32x4 acc[7][4];
#pragma unroll
  for (int f = 0; f < 7; ++f)
#pragma unroll
    for (int t = 0; t < 4; ++t) acc[f][t] = f32x4{0.f, 0.f, 0.f, 0.f};
  constexpr int PD = 3;
  v4u Z[PD][7], W[PD][4];
  const int rot = panel & 15;
  auto ksof = [&](int i) { return 16 * w + ((i + rot) & 15); };
  auto load = [&](int s, int i) {
    const int ks = ksof(i);
#pragma unroll
    for (int f = 0; f < 7; ++f) Z[s][f] = *(const v4u*)(zb + (size_t)(f < 6 ? f : fb6) * 65536 + ks * 1024);
#pragma unroll
    for (int t = 0; t < 4; ++t) W[s][t] = *(const v4u*)(wb + (size_t)t * 65536 + ks * 1024);
  };
#pragma unroll
  for (int s = 0; s < PD - 1; ++s) load(s, s);
  __builtin_amdgcn_sched_barrier(0);
#pragma unroll
  for (int i = 0; i < 16; ++i) {
    if (i + PD - 1 < 16) load((i + PD - 1) % PD, i + PD - 1);
    __builtin_amdgcn_sched_barrier(0);
#pragma unroll
    for (int f = 0; f < 7; ++f)
#pragma unroll
      for (int t = 0; t < 4; ++t) acc[f][t] = mfma_bf(Z[i % PD][f], W[i % PD][t], acc[f][t]);
    __builtin_amdgcn_sched_barrier(0);
  }
  STAMP(3, 1);
  v2u z1v[7];
#pragma unroll
  for (int f = 0; f < 7; ++f) z1v[f] = *(const v2u*)(p.ws + O_Z1 + (size_t)(b0 + (f < 6 ? f : fb6)) * 65536 + (size_t)(4 * panel + w) * 512 + lane * 8);
  const float bias = bf2f(p.b_sp[64 * panel + 16 * w + i16]);
  f32x4* red = (f32x4*)smem;
#pragma unroll
  for (int t = 0; t < 4; ++t) {
    if (t != w) {
      const int slot = t * 3 + ((w - t - 1) & 3);
#pragma unroll
      for (int f = 0; f < 7; ++f) red[(slot * 7 + f) * 64 + lane] = acc[f][t];
    }
  }
  __syncthreads();
  f32x4 o[7];
#pragma unroll
  for (int f = 0; f < 7; ++f) {
    o[f] = w == 0 ? acc[f][0] : w == 1 ? acc[f][1] : w == 2 ? acc[f][2] : acc[f][3];
#pragma unroll
    for (int s = 0; s < 3; ++s) o[f] += red[((w * 3 + s) * 7 + f) * 64 + lane];
  }
#pragma unroll
  for (int f = 0; f < 7; ++f) {
    if (f < nb) {
      const int fb = b0 + f;
      const float p0 = bflo(z1v[f][0]) * rbf(rbf(o[f][0]) + bias), p1 = bfhi(z1v[f][0]) * rbf(rbf(o[f][1]) + bias);
      const float p2 = bflo(z1v[f][1]) * rbf(rbf(o[f][2]) + bias), p3 = bfhi(z1v[f][1]) * rbf(rbf(o[f][3]) + bias);
      *(v2u*)(p.ws + O_P + ((size_t)(4 * panel + w) * 27 + (fb >> 1)) * 1024 + lane * 16 + (fb & 1) * 8) = v2u{pk_bf16(p0, p1), pk_bf16(p2, p3)};
    }
  }
  STAMP(3, 15);
}

constexpr int K5_LDS = 4 * 15 * 1024;
__global__ void __launch_bounds__(256, 1) k5_gemm3(Params p) {
  extern __shared__ __attribute__((aligned(16))) unsigned char smem[];
  const int tid = threadIdx.x, lane = tid & 63, w = __builtin_amdgcn_readfirstlane(tid >> 6);
  const int i16 = lane & 15, g = lane >> 4;
  int panel, j;
  tile_map(blockIdx.x, panel, j);
  STAMP(4, 0);
  const unsigned char* pb = p.ws + O_P + (size_t)(4 * panel) * 27 * 1024 + lane * 16;
  const unsigned char* wb = p.ws + O_WOUTF + (size_t)(5 * j) * 27 * 1024 + lane * 16;
  v4u P[7][4], W[7][5];
#pragma unroll
  for (int i = 0; i < 7; ++i) {
    const int ks = min(7 * w + i, 26);
#pragma unroll
    for (int t = 0; t < 4; ++t) P[i][t] = *(const v4u*)(pb + (t * 27 + ks) * 1024);
#pragma unroll
    for (int d = 0; d < 5; ++d) W[i][d] = *(const v4u*)(wb + (d * 27 + ks) * 1024);
  }
  v2u x1v[5]; float gb[5][4];
  const float* modp = (const float*)(p.ws + O_MODP);
  const int t = 64 * panel + 16 * w + i16;
#pragma unroll
  for (int d = 0; d < 5; ++d) {
    const int dd = 80 * j + 16 * d + 4 * g;
    x1v[d] = *(const v2u*)(p.ws + O_X1G + ((size_t)(dd >> 2) * T + t) * 8);
#pragma unroll
    for (int r = 0; r < 4; ++r) gb[d][r] = modval(modp, 5 * D + dd + r);
  }
  __builtin_amdgcn_sched_barrier(0);
  f32x4 acc[5][4];
#pragma unroll
  for (int d = 0; d < 5; ++d)
#pragma unroll
    for (int tt = 0; tt < 4; ++tt) acc[d][tt] = f32x4{0.f, 0.f, 0.f, 0.f};
#pragma unroll
  for (int i = 0; i < 7; ++i) {
    if (i < 6 || w != 3) {
#pragma unroll
      for (int d = 0; d < 5; ++d)
#pragma unroll
        for (int tt = 0; tt < 4; ++tt) acc[d][tt] = mfma_bf(W[i][d], P[i][tt], acc[d][tt]);
    }
  }
  STAMP(4, 1);
  f32x4* red = (f32x4*)smem;
#pragma unroll
  for (int tt = 0; tt < 4; ++tt) {
    if (tt != w) {
      const int slot = tt * 3 + ((w - tt - 1) & 3);
#pragma unroll
      for (int d = 0; d < 5; ++d) red[(slot * 5 + d) * 64 + lane] = acc[d][tt];
    }
  }
  __syncthreads();
#pragma unroll
  for (int d = 0; d < 5; ++d) {
    f32x4 o = w == 0 ? acc[d][0] : w == 1 ? acc[d][1] : w == 2 ? acc[d][2] : acc[d][3];
#pragma unroll
    for (int s = 0; s < 3; ++s) o += red[((w * 3 + s) * 5 + d) * 64 + lane];
    const float r0 = bflo(x1v[d][0]) + rbf(gb[d][0] * rbf(o[0])), r1 = bfhi(x1v[d][0]) + rbf(gb[d][1] * rbf(o[1]));
    const float r2 = bflo(x1v[d][1]) + rbf(gb[d][2] * rbf(o[2])), r3 = bfhi(x1v[d][1]) + rbf(gb[d][3] * rbf(o[3]));
    *(v2u*)(p.out + (size_t)t * D + 80 * j + 16 * d + 4 * g) = v2u{pk_bf16(r0, r1), pk_bf16(r2, r3)};
  }
  STAMP(4, 15);
}

extern "C" void kernel_launch(void* const* d_in, const int* in_sizes, int n_in, void* d_out, int out_size, void* d_ws,
                              size_t ws_size, hipStream_t stream) {
  Params p;
  p.u = (const u16*)d_in[0]; p.a_re = (const u16*)d_in[1]; p.a_im = (const u16*)d_in[2];
  p.b_re = (const u16*)d_in[3]; p.b_im = (const u16*)d_in[4]; p.c_re = (const u16*)d_in[5]; p.c_im = (const u16*)d_in[6];
  p.log_dt = (const u16*)d_in[7]; p.dskip = (const u16*)d_in[8];
  p.w_in = (const u16*)d_in[10]; p.w_sp = (const u16*)d_in[11]; p.b_sp = (const u16*)d_in[12]; p.w_out = (const u16*)d_in[13];
  p.cvec = (const u16*)d_in[14]; p.w_mod = (const u16*)d_in[15];
  p.out = (u16*)d_out; p.ws = (unsigned char*)d_ws;
  (void)hipFuncSetAttribute((const void*)k3_gemm1, hipFuncAttributeMaxDynamicSharedMemorySize, K3_LDS);
  (void)hipFuncSetAttribute((const void*)k4_gemm2, hipFuncAttributeMaxDynamicSharedMemorySize, K4_LDS);
  k1_prep<<<K1_KGEN + K1_MOD + K1_STAT + K1_WSP, 256, 0, stream>>>(p);
  k2_conv<<<256, 256, K2_LDS, stream>>>(p);
  k3_gemm1<<<256, 256, K3_LDS, stream>>>(p);
  k4_gemm2<<<256, 256, K4_LDS, stream>>>(p);
  k5_gemm3<<<256, 256, K5_LDS, stream>>>(p);
}
```

```cpp
#include <hip/hip_runtime.h>
#include <hip/hip_bf16.h>
#include <stdint.h>

typedef unsigned v4u __attribute__((ext_vector_type(4)));
typedef unsigned v2u __attribute__((ext_vector_type(2)));
typedef float f32x4 __attribute__((ext_vector_type(4)));
typedef float f32x2 __attribute__((ext_vector_type(2)));
typedef short bf16x8 __attribute__((ext_vector_type(8)));
typedef _Float16 f16x8 __attribute__((ext_vector_type(8)));
typedef _Float16 f16x2 __attribute__((ext_vector_type(2)));
typedef __bf16 bf16x2_t __attribute__((ext_vector_type(2)));
typedef unsigned short u16;
typedef unsigned long long u64;
#define GAS __attribute__((address_space(1)))
#define LAS __attribute__((address_space(3)))
#define RLX_AGENT __ATOMIC_RELAXED, __HIP_MEMORY_SCOPE_AGENT
#define DEVI __device__ __forceinline__
#define SB() __builtin_amdgcn_sched_barrier(0)
#define DRAIN() asm volatile("s_waitcnt vmcnt(0)" ::: "memory")

constexpr int T = 2048, D = 640, NST = 128, FH = 864, F2 = 1728;
constexpr int KL = 2080, KIMG_B = 2 * KL * 2, HL = 2304;
constexpr unsigned SPIN = 1u << 17;
constexpr int AUX_SC1 = 16, AUX_NT = 2;
constexpr int O_EPOCH = 0;
constexpr int O_INIT = 256;
constexpr int O_CNT = 4096;
constexpr int O_FLAG = O_CNT + 3 * 64 * 256;
constexpr int O_MODG = O_FLAG + 8192;
constexpr int O_STG = O_MODG + 3840 * 8 + 2048;
constexpr int O_U = O_STG + 2048 * 16;
constexpr int O_ZST = O_U + 16384;
constexpr int O_X1G = O_ZST + 32 * 32 * 64 * 16;
constexpr int O_WINF = O_X1G + T * D * 2;
constexpr int O_WOUTF = O_WINF + 108 * 20 * 1024;
constexpr int O_Z2N = O_WOUTF + 40 * 27 * 1024;
constexpr int O_P = O_Z2N + 54 * 64 * 1024;
constexpr int O_END = O_P + 128 * 27 * 1024;
constexpr int C_X1 = 0, C_W = 1, C_S = 2;

constexpr size_t O_STAMP = 200u << 20;
#define STAMP(idx) do {} while (0)
#define TS(k) do {} while (0)
#define TSDUMP(base, n) do {} while (0)

DEVI float u2f(unsigned u) { return __builtin_bit_cast(float, u); }
DEVI unsigned f2u(float f) { return __builtin_bit_cast(unsigned, f); }
DEVI float bf2f(unsigned b) { return u2f(b << 16); }
DEVI float bflo(unsigned u) { return u2f(u << 16); }
DEVI float bfhi(unsigned u) { return u2f(u & 0xffff0000u); }
DEVI unsigned pk_bf16(float a, float b) {
  f32x2 v = {a, b};
  bf16x2_t r = __builtin_convertvector(v, bf16x2_t);
  return __builtin_bit_cast(unsigned, r);
}
DEVI float rbf(float a) { return bflo(pk_bf16(a, 0.f)); }
DEVI unsigned pk_f16(float a, float b) {
  f32x2 v = {a, b};
  f16x2 r = __builtin_convertvector(v, f16x2);
  return __builtin_bit_cast(unsigned, r);
}
DEVI f32x4 mfma_bf(v4u a, v4u b, f32x4 c) {
  return __builtin_amdgcn_mfma_f32_16x16x32_bf16(__builtin_bit_cast(bf16x8, a), __builtin_bit_cast(bf16x8, b), c, 0, 0, 0);
}
DEVI f32x4 mfma_h(v4u a, v4u b, f32x4 c) {
  return __builtin_amdgcn_mfma_f32_16x16x32_f16(__builtin_bit_cast(f16x8, a), __builtin_bit_cast(f16x8, b), c, 0, 0, 0);
}
typedef __amdgpu_buffer_rsrc_t rsrc_t;
DEVI v4u bld1(rsrc_t rs, int voff, int soff = 0) { return __builtin_amdgcn_raw_buffer_load_b128(rs, voff, soff, AUX_SC1); }
DEVI void bst1(rsrc_t rs, int voff, v4u v) { __builtin_amdgcn_raw_buffer_store_b128(v, rs, voff, 0, AUX_SC1); }
DEVI void bst1_64(rsrc_t rs, int voff, v2u v) { __builtin_amdgcn_raw_buffer_store_b64(v, rs, voff, 0, AUX_SC1); }
template <int CTRL> DEVI float dppf(float x) { return u2f((unsigned)__builtin_amdgcn_update_dpp(0, (int)f2u(x), CTRL, 0xf, 0xf, true)); }
#define ORDER() __builtin_amdgcn_fence(__ATOMIC_ACQUIRE, "wavefront")

struct Params {
  const u16 *u, *a_re, *a_im, *b_re, *b_im, *c_re, *c_im, *log_dt, *dskip, *w_in, *w_sp, *b_sp, *w_out, *cvec, *w_mod;
  u16* out;
  unsigned char* ws;
};

DEVI int blk0(int j) { return j < 6 ? 7 * j : 42 + 6 * (j - 6); }
DEVI int pair_blk(int ks3, int h) {
  if (ks3 < 24) { const int j = ks3 / 3, w = ks3 - 3 * j; return blk0(j) + 2 * w + h; }
  return 7 * (2 * (ks3 - 24) + h) + 6;
}
DEVI void arrive(const Params& p, int c, int tid) {
  if (tid < 8) __hip_atomic_fetch_add((GAS unsigned*)(p.ws + O_CNT) + ((c * 8 + tid) * 8 + (blockIdx.x & 7)) * 64, 1u, RLX_AGENT);
}
DEVI unsigned cnt_load(const Params& p, int c, int lane) {
  return __hip_atomic_load((GAS unsigned*)(p.ws + O_CNT) + ((c * 8 + ((blockIdx.x >> 3) & 7)) * 8 + (lane & 7)) * 64, RLX_AGENT);
}
DEVI void cnt_wait(const Params& p, int c, unsigned want, int lane, unsigned first) {
  unsigned v = first;
  for (unsigned s = 0; s < SPIN; ++s) {
    if (__all(v >= want)) break;
    __builtin_amdgcn_s_sleep(1);
    v = cnt_load(p, c, lane);
  }
  ORDER();
}
DEVI void flag_wait8(GAS unsigned* f, unsigned tag, int lane) {
  for (unsigned s = 0; s < SPIN; ++s) {
    const unsigned v = __hip_atomic_load(f + (lane & 7), RLX_AGENT);
    if (__all(v == tag)) break;
    __builtin_amdgcn_s_sleep(1);
  }
  ORDER();
}

struct C2 { f32x2 re, im; };
DEVI C2 cmul2(C2 a, C2 b) { return C2{a.re * b.re - a.im * b.im, a.re * b.im + a.im * b.re}; }
DEVI C2 cexp2(f32x2 are2, f32x2 arev, float l) {
  const f32x2 e = are2 * l, q = arev * l;
  const f32x2 m = {__builtin_amdgcn_exp2f(e[0]), __builtin_amdgcn_exp2f(e[1])};
  const float p0 = __builtin_amdgcn_fractf(q[0]), p1 = __builtin_amdgcn_fractf(q[1]);
  return C2{m * f32x2{__builtin_amdgcn_cosf(p0), __builtin_amdgcn_cosf(p1)}, m * f32x2{__builtin_amdgcn_sinf(p0), __builtin_amdgcn_sinf(p1)}};
}
constexpr int RS = 132;
DEVI void put_row(unsigned* buf, int row, int lane, C2 v) { *(v2u*)(buf + row * RS + 2 * lane) = v2u{pk_f16(v.re[0], v.im[0]), pk_f16(v.re[1], v.im[1])}; }
DEVI void put_col(unsigned* buf, int row, int lane, C2 v) { *(v2u*)(buf + row * RS + 2 * lane) = v2u{pk_f16(v.re[0], -v.im[0]), pk_f16(v.re[1], -v.im[1])}; }
DEVI f32x2 unp(unsigned u) { return f32x2{bflo(u), bfhi(u)}; }

template <class HK> DEVI void kgen_wave(unsigned par[6], float dt, unsigned* buf0, unsigned* buf1, u16* tab, unsigned char* img, int lane, HK&& hook) {
  const int c = lane & 15, g = lane >> 4;
  const f32x2 are2 = unp(par[0]) * dt * 1.4426950408889634f, arev = unp(par[1]) * dt * 0.15915494309189535f;
  C2 W = cmul2(C2{unp(par[4]), unp(par[5])}, C2{unp(par[2]) * dt, unp(par[3]) * dt});
  float mx = fmaxf(fmaxf(fabsf(W.re[0]), fabsf(W.re[1])), fmaxf(fabsf(W.im[0]), fabsf(W.im[1])));
  mx = fmaxf(mx, dppf<0x128>(mx)); mx = fmaxf(mx, dppf<0x124>(mx)); mx = fmaxf(mx, dppf<0x122>(mx)); mx = fmaxf(mx, dppf<0x121>(mx));
  const unsigned mu = f2u(mx);
  const unsigned m0 = __builtin_amdgcn_readlane(mu, 0), m1 = __builtin_amdgcn_readlane(mu, 16), m2 = __builtin_amdgcn_readlane(mu, 32), m3 = __builtin_amdgcn_readlane(mu, 48);
  unsigned eb = (max(max(m0, m1), max(m2, m3)) >> 23) & 0xffu;
  eb = eb < 2u ? 2u : (eb > 250u ? 250u : eb);
  const float sc = u2f((eb + 1u) << 23), isc = u2f((253u - eb) << 23);
  W.re *= isc; W.im *= isc;
  {
    const C2 e8 = cexp2(are2, arev, 8.f);
#pragma unroll
    for (int b = 0; b < 16; ++b) { put_row(buf0, b, lane, W); if (b < 15) W = cmul2(W, e8); if (b == 0) hook(2); if (b == 8) hook(3); }
  }
  v4u A[8];
#pragma unroll
  for (int ks = 0; ks < 8; ++ks) A[ks] = *(const v4u*)(buf0 + c * RS + 16 * ks + 4 * g);
  C2 er[8];
  er[0] = C2{f32x2{1.f, 1.f}, f32x2{0.f, 0.f}};
  er[1] = cexp2(are2, arev, 1.f);
  er[2] = cmul2(er[1], er[1]); er[3] = cmul2(er[2], er[1]); er[4] = cmul2(er[2], er[2]);
  er[5] = cmul2(er[4], er[1]); er[6] = cmul2(er[3], er[3]); er[7] = cmul2(er[4], er[3]);
  hook(4);
  const C2 e128 = cexp2(are2, arev, 128.f);
#pragma unroll
  for (int r = 0; r < 8; ++r) { put_col(buf1, r, lane, er[r]); put_col(buf1, 8 + r, lane, r ? cmul2(e128, er[r]) : e128); }
  f32x4 acc0 = {0.f, 0.f, 0.f, 0.f}, acc1 = acc0, acc2 = acc0;
#pragma unroll
  for (int ks = 0; ks < 8; ++ks) acc0 = mfma_h(A[ks], *(const v4u*)(buf1 + c * RS + 16 * ks + 4 * g), acc0);
  hook(5);
  C2 e = cmul2(e128, e128);
#pragma unroll
  for (int a = 0; a < 2; ++a) {
#pragma unroll
    for (int r = 0; r < 4; ++r) put_col(buf0, 4 * a + r, lane, r ? cmul2(e, er[2 * r]) : e);
    e = cmul2(e, e128);
  }
  hook(6);
#pragma unroll
  for (int a = 0; a < 4; ++a) { put_col(buf0, 8 + 2 * a, lane, e); put_col(buf0, 9 + 2 * a, lane, cmul2(e, er[4])); e = cmul2(e, e128); }
#pragma unroll
  for (int ks = 0; ks < 8; ++ks) acc1 = mfma_h(A[ks], *(const v4u*)(buf0 + c * RS + 16 * ks + 4 * g), acc1);
  hook(7);
  e = cexp2(are2, arev, 1024.f);
#pragma unroll
  for (int a = 0; a < 9; ++a) { put_col(buf1, a, lane, e); if (a < 8) e = cmul2(e, e128); }
#pragma unroll
  for (int ks = 0; ks < 8; ++ks) acc2 = mfma_h(A[ks], *(const v4u*)(buf1 + c * RS + 16 * ks + 4 * g), acc2);
  const int cv0 = 128 * (c >> 3) + (c & 7);
  const int cv1 = c < 8 ? 256 + 128 * (c >> 2) + 2 * (c & 3) : 512 + 128 * ((c - 8) >> 1) + 4 * (c & 1);
  const int cv2 = 1024 + 128 * c;
#pragma unroll
  for (int r = 0; r < 4; ++r) {
    const int lb = 8 * (4 * g + r);
    const unsigned q01 = pk_bf16(acc0[r] * sc, acc1[r] * sc);
    tab[lb + cv0] = (u16)q01;
    tab[lb + cv1] = (u16)(q01 >> 16);
    if (lb + cv2 <= 2048) tab[lb + cv2] = (u16)pk_bf16(acc2[r] * sc, 0.f);
  }
#pragma unroll
  for (int it = 0; it < 2; ++it) {
    const int j = 64 * it + lane, base = 2048 - 8 * j;
    const unsigned H = tab[base], L = tab[base - 8], X = (j & 1) ? L : H;
    const unsigned HH = H | (H << 16), LL = L | (L << 16);
    *(v4u*)(img + j * 16) = v4u{HH, HH, X | (L << 16), LL};
    *(v4u*)(img + KL * 2 + j * 16) = v4u{HH, H | (X << 16), LL, LL};
  }
  {
    const int j = 128 + lane, base = 2048 - 8 * j;
    const unsigned H = tab[base], M = tab[base - 4], L = tab[base - 8];
    const unsigned HH = H | (H << 16), MM = M | (M << 16), LL = L | (L << 16);
    *(v4u*)(img + j * 16) = v4u{HH, H | (M << 16), MM, LL};
    *(v4u*)(img + KL * 2 + j * 16) = v4u{HH, MM, M | (L << 16), LL};
  }
  {
    const int j = 192 + lane, base = 2048 - 8 * j;
    const int o = lane < 32 ? 1 : 0;
    const unsigned v0 = tab[base], v1 = tab[base - 1 + o], v2 = tab[base - 2], v3 = tab[base - 3 - o], v4 = tab[base - 4];
    const unsigned v5 = tab[base - 5 + o], v6 = tab[base - 6], v7 = tab[base - 7 - o], v8 = tab[base - 8];
    *(v4u*)(img + j * 16) = v4u{v0 | (v1 << 16), v2 | (v3 << 16), v4 | (v5 << 16), v6 | (v7 << 16)};
    *(v4u*)(img + KL * 2 + j * 16) = v4u{v1 | (v2 << 16), v3 | (v4 << 16), v5 | (v6 << 16), v7 | (v8 << 16)};
    if (lane < 8) *(v4u*)(img + (lane >> 2) * (KL * 2) + (256 + (lane & 3)) * 16) = v4u{lane == 0 ? (unsigned)tab[0] : 0u, 0u, 0u, 0u};
  }
}

DEVI void wout_unit(const Params& p, rsrc_t rs, unsigned char* lw, int unit, int lane) {
  const int ks = unit / 5, dc = unit - 5 * ks, d = 128 * dc + 2 * lane;
  const int f0 = 16 * pair_blk(ks, 0), f1 = 16 * pair_blk(ks, 1);
  unsigned wv[4][8];
#pragma unroll
  for (int g = 0; g < 4; ++g)
#pragma unroll
    for (int j = 0; j < 8; ++j) wv[g][j] = *(const unsigned*)(p.w_out + (size_t)((j < 4 ? f0 : f1) + 4 * g + (j & 3)) * D + d);
#pragma unroll
  for (int g = 0; g < 4; ++g) {
    unsigned char* dst = lw + (lane >> 3) * 1024 + (g * 16 + 2 * (lane & 7)) * 16;
    *(v4u*)dst = v4u{(wv[g][0] & 0xffffu) | (wv[g][1] << 16), (wv[g][2] & 0xffffu) | (wv[g][3] << 16), (wv[g][4] & 0xffffu) | (wv[g][5] << 16), (wv[g][6] & 0xffffu) | (wv[g][7] << 16)};
    *(v4u*)(dst + 16) = v4u{(wv[g][0] >> 16) | (wv[g][1] & 0xffff0000u), (wv[g][2] >> 16) | (wv[g][3] & 0xffff0000u), (wv[g][4] >> 16) | (wv[g][5] & 0xffff0000u), (wv[g][6] >> 16) | (wv[g][7] & 0xffff0000u)};
  }
#pragma unroll
  for (int i = 0; i < 8; ++i) bst1(rs, O_WOUTF + ((8 * dc + i) * 27 + ks) * 1024 + lane * 16, *(const v4u*)(lw + i * 1024 + lane * 16));
}

DEVI void aux_role(const Params& p, rsrc_t rs, unsigned char* smem, int a, unsigned tag) {
  const int tid = threadIdx.x, lane = tid & 63, w = __builtin_amdgcn_readfirstlane(tid >> 6);
  float* fl = (float*)smem;
  unsigned wv[5][8];
  if (a < 60) {
    const int rg = tid >> 3, cl = tid & 7;
    v4u mv[20]; v2u cv[5];
#pragma unroll
    for (int i = 0; i < 5; ++i) cv[i] = *(const v2u*)(p.cvec + 20 * rg + 4 * i);
#pragma unroll
    for (int i = 0; i < 20; ++i) mv[i] = __builtin_nontemporal_load((const v4u*)(p.w_mod + (size_t)(20 * rg + i) * 3840 + 64 * a + 8 * cl));
    SB();
    float sl[20];
#pragma unroll
    for (int i = 0; i < 20; ++i) {
      const unsigned cu = cv[i >> 2][(i >> 1) & 1];
      const float c = (i & 1) ? bfhi(cu) : bflo(cu);
      sl[i] = rbf(c * __builtin_amdgcn_rcpf(1.0f + __builtin_amdgcn_exp2f(-1.4426950408889634f * c)));
    }
    f32x2 acc[4];
#pragma unroll
    for (int j = 0; j < 4; ++j) acc[j] = f32x2{0.f, 0.f};
#pragma unroll
    for (int i = 0; i < 20; ++i)
#pragma unroll
      for (int j = 0; j < 4; ++j) acc[j] += unp(mv[i][j]) * sl[i];
    *(f32x4*)(fl + rg * 68 + cl * 8) = f32x4{acc[0][0], acc[0][1], acc[1][0], acc[1][1]};
    *(f32x4*)(fl + rg * 68 + cl * 8 + 4) = f32x4{acc[2][0], acc[2][1], acc[3][0], acc[3][1]};
    __syncthreads();
    {
      float s = 0.f;
#pragma unroll
      for (int r = 0; r < 8; ++r) s += fl[(8 * (tid & 3) + r) * 68 + (tid >> 2)];
      s += dppf<0xB1>(s); s += dppf<0x4E>(s);
      if ((tid & 3) == 0) bst1_64(rs, O_MODG + (64 * a + (tid >> 2)) * 8, v2u{f2u(rbf(s)), tag});
    }
    SB();
    if (a < 54) {
#pragma unroll
      for (int i = 0; i < 5; ++i)
#pragma unroll
        for (int j = 0; j < 8; ++j) wv[i][j] = *(const unsigned*)(p.w_in + (size_t)(8 * ((tid >> 4) + 16 * i) + j) * F2 + 32 * a + 2 * (tid & 15));
    }
    __syncthreads();
  } else if (a < 92) {
    const int row = lane >> 3, c = lane & 7;
    v4u xv[2][10];
#pragma unroll
    for (int h = 0; h < 2; ++h)
#pragma unroll
      for (int i = 0; i < 10; ++i) xv[h][i] = *(const v4u*)(p.u + (size_t)(64 * (a - 60) + 16 * w + 8 * h + row) * D + 64 * i + 8 * c);
    SB();
#pragma unroll
    for (int h = 0; h < 2; ++h) {
      f32x2 s = {0.f, 0.f};
#pragma unroll
      for (int i = 0; i < 10; ++i)
#pragma unroll
        for (int j = 0; j < 4; ++j) s += unp(xv[h][i][j]);
      float m = s[0] + s[1];
      m += dppf<0xB1>(m); m += dppf<0x4E>(m); m += dppf<0x141>(m);
      m *= (1.0f / D);
      f32x2 q = {0.f, 0.f};
#pragma unroll
      for (int i = 0; i < 10; ++i)
#pragma unroll
        for (int j = 0; j < 4; ++j) { const f32x2 dlt = unp(xv[h][i][j]) - m; q += dlt * dlt; }
      float v = q[0] + q[1];
      v += dppf<0xB1>(v); v += dppf<0x4E>(v); v += dppf<0x141>(v);
      const float r = __builtin_amdgcn_rsqf(v * (1.0f / D) + 1e-5f);
      if (c == 0) bst1(rs, O_STG + (64 * (a - 60) + 16 * w + 8 * h + row) * 16, v4u{f2u(m), tag, f2u(r), tag});
    }
  } else if (a == 92) {
    if (tid < 192) __hip_atomic_store((GAS unsigned*)(p.ws + O_CNT) + tid * 64, 0u, RLX_AGENT);
    DRAIN();
    __syncthreads();
    if (tid == 0) bst1_64(rs, O_INIT, v2u{0u, tag});
  }
  STAMP(1);
  for (unsigned s = 0; s < SPIN; ++s) {
    const u64 v = __hip_atomic_load((GAS u64*)(p.ws + O_INIT), RLX_AGENT);
    if (__all((unsigned)(v >> 32) == tag)) break;
    __builtin_amdgcn_s_sleep(1);
  }
  ORDER();
  if (a < 54) {
    float* c1L = fl;
    float* sbL = fl + D;
    float* red = fl + 2 * D;
    unsigned char* stg = smem + 16384;
    {
      for (unsigned s = 0; s < SPIN; ++s) {
        bool ok = true;
#pragma unroll
        for (int i = 0; i < 5; ++i) {
          const int e = 256 * i + tid;
          const u64 v = __hip_atomic_load((GAS u64*)(p.ws + O_MODG) + 3 * D + e, RLX_AGENT);
          ok = ok && ((unsigned)(v >> 32) == tag);
          const float x = u2f((unsigned)v);
          if (e < D) sbL[e] = x; else c1L[e - D] = rbf(1.0f + x);
        }
        if (__all(ok)) break;
        __builtin_amdgcn_s_sleep(1);
      }
    }
    __syncthreads();
    const int fp = tid & 15, rgp = tid >> 4;
    f32x2 u1 = {0.f, 0.f}, u2 = {0.f, 0.f};
#pragma unroll
    for (int i = 0; i < 5; ++i) {
      const int e = rgp + 16 * i;
      const f32x4 ca = *(const f32x4*)(c1L + 8 * e), cb = *(const f32x4*)(c1L + 8 * e + 4);
      const f32x4 sa = *(const f32x4*)(sbL + 8 * e), sb = *(const f32x4*)(sbL + 8 * e + 4);
      unsigned r[8];
#pragma unroll
      for (int j = 0; j < 8; ++j) {
        const f32x2 x = unp(wv[i][j]);
        const f32x2 y = x * (j < 4 ? ca[j & 3] : cb[j & 3]);
        r[j] = pk_bf16(y[0], y[1]);
        u1 += unp(r[j]);
        u2 += x * (j < 4 ? sa[j & 3] : sb[j & 3]);
      }
      unsigned char* dst = stg + ((fp >> 3) * 20 + (e >> 2)) * 1024 + ((e & 3) * 16 + 2 * (fp & 7)) * 16;
      *(v4u*)dst = v4u{(r[0] & 0xffffu) | (r[1] << 16), (r[2] & 0xffffu) | (r[3] << 16), (r[4] & 0xffffu) | (r[5] << 16), (r[6] & 0xffffu) | (r[7] << 16)};
      *(v4u*)(dst + 16) = v4u{(r[0] >> 16) | (r[1] & 0xffff0000u), (r[2] >> 16) | (r[3] & 0xffff0000u), (r[4] >> 16) | (r[5] & 0xffff0000u), (r[6] >> 16) | (r[7] & 0xffff0000u)};
    }
    *(f32x4*)(red + (rgp * 16 + fp) * 4) = f32x4{u1[0], u2[0], u1[1], u2[1]};
    __syncthreads();
#pragma unroll
    for (int i = 0; i < 10; ++i) bst1(rs, O_WINF + a * 40960 + i * 4096 + tid * 16, *(const v4u*)(stg + i * 4096 + tid * 16));
    if (tid < 16) {
      f32x4 s = {0.f, 0.f, 0.f, 0.f};
#pragma unroll
      for (int r = 0; r < 16; ++r) s += *(const f32x4*)(red + r * 64 + tid * 4);
      bst1(rs, O_U + a * 256 + tid * 16, __builtin_bit_cast(v4u, s));
    }
    DRAIN();
    __syncthreads();
  }
  if (a < 56) arrive(p, C_W, tid);
  STAMP(2);
  unsigned char* lw = smem + 65536 + w * 8192;
  if (a >= 56) {
    const int wi = (a - 56) * 4 + w;
    if (wi < 135) wout_unit(p, rs, lw, wi, lane);
  }
  {
    const int wi = (a >> 3) * 4 + w;
    const unsigned char* src = (const unsigned char*)p.w_sp + (size_t)((a & 7) >> 1) * (512 * 4096);
    unsigned sink = 0;
#pragma unroll
    for (int i = 0; i < 11; ++i) {
      const int piece = (wi * 11 + i) * 64 + lane;
      if (piece < 32768) sink ^= *(const unsigned*)(src + (size_t)piece * 64);
    }
    asm volatile("" :: "v"(sink));
  }
  DRAIN();
  __syncthreads();
  arrive(p, C_S, tid);
  STAMP(3);
}

template <int CTRL> DEVI float dpp_add(float keep, float send) { return keep + dppf<CTRL>(send); }
DEVI float bfly16(const float (&v)[16], int i16) {
  float a[8], b[4], c[2];
  const bool h8 = i16 & 8, h4 = i16 & 4, h2 = i16 & 2, h1 = i16 & 1;
#pragma unroll
  for (int k = 0; k < 8; ++k) a[k] = dpp_add<0x128>(h8 ? v[k + 8] : v[k], h8 ? v[k] : v[k + 8]);
#pragma unroll
  for (int k = 0; k < 4; ++k) b[k] = dpp_add<0x141>(h4 ? a[k + 4] : a[k], h4 ? a[k] : a[k + 4]);
#pragma unroll
  for (int k = 0; k < 2; ++k) c[k] = dpp_add<0x4E>(h2 ? b[k + 2] : b[k], h2 ? b[k] : b[k + 2]);
  return dpp_add<0xB1>(h1 ? c[1] : c[0], h1 ? c[0] : c[1]);
}
DEVI f32x2 gelu2(f32x2 v) {
  const float c0 = -2.0f * 0.7978845608028654f * 1.4426950408889634f, c1 = c0 * 0.044715f;
  const f32x2 u = v * (v * v * c1 + c0);
  const f32x2 d = f32x2{__builtin_amdgcn_exp2f(u[0]), __builtin_amdgcn_exp2f(u[1])} + 1.0f;
  return v * f32x2{__builtin_amdgcn_rcpf(d[0]), __builtin_amdgcn_rcpf(d[1])};
}
constexpr int L_KIMG = 0, L_HIMG = 4 * KIMG_B, L_KSCR = L_HIMG + 4 * HL * 2, L_TAB = L_KSCR + 8 * 16 * RS * 4;
constexpr int L_MISC = 86016, L_WL = L_MISC + 8192, L_TOTAL = L_WL + 65536;
static_assert(L_TAB + 4 * 4224 <= L_TOTAL && L_KSCR + 16384 <= L_WL, "lds");
constexpr int OOB = 64 << 20;
constexpr int CA[36] = {3,3,3,3,3,4,4,4,4, 2,2,2,2,2,2,5,5,5, 1,1,1,1,6,6,1,1,1, 0,0,0,0,7,0,0,0,0};
constexpr int CB[36] = {4,3,2,1,0,3,2,1,0, 5,4,3,2,1,0,2,1,0, 6,5,4,3,1,0,2,1,0, 7,6,5,4,0,3,2,1,0};
constexpr int CL[16] = {8+4,3,8+3,8+2,8+1,8+0,4,8+5,2,5,8+6,1,6,8+7,0,7};

DEVI void dma16(rsrc_t r, int voff, int soff, unsigned ldsaddr) {
  unsigned keep;
  asm volatile("s_mov_b32 %0, m0\n\ts_mov_b32 m0, %3\n\ts_nop 0\n\tbuffer_load_dwordx4 %1, %2, %4 offen lds\n\ts_mov_b32 m0, %0"
               : "=&s"(keep) : "v"(voff), "s"(r), "s"(ldsaddr), "s"(soff) : "memory");
}

__global__ void __launch_bounds__(256, 1) k_fused(Params p) {
  extern __shared__ __attribute__((aligned(16))) unsigned char smem[];
  const int tid = threadIdx.x, lane = tid & 63, w = __builtin_amdgcn_readfirstlane(tid >> 6);
  const int i16 = lane & 15, g = lane >> 4, l16 = lane * 16;
  const int bx = blockIdx.x & 7, bi = blockIdx.x >> 3;
  const rsrc_t rs = __builtin_amdgcn_make_buffer_rsrc((void*)p.ws, 0, O_END, 0x00020000);
  const rsrc_t rw = __builtin_amdgcn_make_buffer_rsrc((void*)p.w_sp, 0, T * T * 2, 0x00020000);
  u64 ts_[20];
  STAMP(0);
  const unsigned tag = __hip_atomic_load((GAS unsigned*)(p.ws + O_EPOCH), RLX_AGENT) + 1u;
  const bool is_conv = bi < 20;
  const int q4 = bx * 20 + bi;
  const int panel = (bx >> 1) * 8 + (bi >> 2), j = (bx & 1) * 4 + (bi & 3);
  const int b0 = blk0(j);
  const int lb0 = (w < 3) ? 2 * w : 6, lb1 = 2 * w + 1;
  const bool has0 = (w < 3) || (j < 6), has1 = (w < 3);
  const int z2s0 = has0 ? O_WINF + (54 + b0 + lb0) * 20480 : OOB, z2s1 = has1 ? O_WINF + (54 + b0 + lb1) * 20480 : OOB;
  const int z1s0 = has0 ? O_WINF + (b0 + lb0) * 20480 : OOB, z1s1 = has1 ? O_WINF + (b0 + lb1) * 20480 : OOB;
  const int rot = (panel & 7) * 2;
  const int wvo = (8 * (lane >> 4) + ((lane >> 2) & 3)) * 4096 + (((lane & 3) ^ ((lane >> 4) & 3)) * 16);
  const int wso = (64 * panel) * 4096 + 16 * w * 64;
  const unsigned lds0 = (unsigned)(size_t)(LAS unsigned char*)smem;
#define WSP_DMA(step, tb, ldsoff) dma16(rw, wvo, wso + (32 * ((tb) >> 1) + 4 * ((tb) & 1)) * 4096 + ((((step) + rot) & 15) * 64), lds0 + (ldsoff) + w * 16384 + ((((step) & 3) * 4 + (tb)) * 1024))
  const int wrd = w * 16384 + (i16 * 4 + (g ^ ((i16 >> 2) & 3))) * 16;
  v4u R[40];
  v4u xv[4]; unsigned hv[4][4]; float ga[4], dk[4];
  bool wrdy = false;
  if (is_conv) {
    const int d = 4 * q4 + w;
    unsigned par[6];
    par[0] = *(const unsigned*)(p.a_re + d * NST + 2 * lane); par[1] = *(const unsigned*)(p.a_im + d * NST + 2 * lane);
    par[2] = *(const unsigned*)(p.b_re + d * NST + 2 * lane); par[3] = *(const unsigned*)(p.b_im + d * NST + 2 * lane);
    par[4] = *(const unsigned*)(p.c_re + d * NST + 2 * lane); par[5] = *(const unsigned*)(p.c_im + d * NST + 2 * lane);
    const float ldt = bf2f(p.log_dt[d]);
    v2u xa[8];
    auto hook = [&](int i) { SB(); xa[i] = *(const v2u*)(p.u + (size_t)(512 * (i >> 1) + 2 * tid + (i & 1)) * D + 4 * q4); SB(); };
    hook(0); hook(1);
#pragma unroll
    for (int c = 0; c < 4; ++c) dk[c] = bf2f(p.dskip[4 * q4 + c]);
    u16* himg = (u16*)(smem + L_HIMG);
    if (tid < 128) *(v4u*)(himg + (tid >> 5) * HL + (tid & 31) * 8) = v4u{0, 0, 0, 0};
    const float dt = rbf(__builtin_amdgcn_exp2f(ldt * 1.4426950408889634f));
    kgen_wave(par, dt, (unsigned*)(smem + L_KSCR) + (2 * w) * 16 * RS, (unsigned*)(smem + L_KSCR) + (2 * w + 1) * 16 * RS,
              (u16*)(smem + L_TAB + w * 4224), smem + L_KIMG + w * KIMG_B, lane, hook);
#pragma unroll
    for (int i = 0; i < 4; ++i) xv[i] = v4u{xa[2 * i][0], xa[2 * i][1], xa[2 * i + 1][0], xa[2 * i + 1][1]};
    STAMP(1);
    v4u st[4][2]; v2u mg;
    const int mo = lane < 12 ? O_MODG + ((lane >> 2) * D + 4 * q4 + (lane & 3)) * 8 : O_INIT;
    for (unsigned s = 0; s < SPIN; ++s) {
      const u64 m = __hip_atomic_load((GAS u64*)(p.ws + mo), RLX_AGENT);
      mg = v2u{(unsigned)m, (unsigned)(m >> 32)};
#pragma unroll
      for (int i = 0; i < 4; ++i) {
        st[i][0] = bld1(rs, O_STG + (256 * i + tid) * 32);
        st[i][1] = bld1(rs, O_STG + (256 * i + tid) * 32 + 16);
      }
      unsigned bad = mg[1] ^ tag;
#pragma unroll
      for (int i = 0; i < 4; ++i) bad |= (st[i][0][1] ^ tag) | (st[i][0][3] ^ tag) | (st[i][1][1] ^ tag) | (st[i][1][3] ^ tag);
      if (__all(bad == 0u)) break;
      __builtin_amdgcn_s_sleep(2);
      asm volatile("" ::: "memory");
    }
    ORDER();
    STAMP(2);
    float sa[4], c1[4];
#pragma unroll
    for (int c = 0; c < 4; ++c) {
      sa[c] = u2f(__builtin_amdgcn_readlane(mg[0], c));
      c1[c] = rbf(1.0f + u2f(__builtin_amdgcn_readlane(mg[0], 4 + c)));
      ga[c] = u2f(__builtin_amdgcn_readlane(mg[0], 8 + c));
    }
#pragma unroll
    for (int i = 0; i < 4; ++i) {
      const int pr = 256 * i + tid;
      const f32x2 m = {u2f(st[i][0][0]), u2f(st[i][1][0])}, r = {u2f(st[i][0][2]), u2f(st[i][1][2])};
      const f32x2 x[4] = {f32x2{bflo(xv[i][0]), bflo(xv[i][2])}, f32x2{bfhi(xv[i][0]), bfhi(xv[i][2])}, f32x2{bflo(xv[i][1]), bflo(xv[i][3])}, f32x2{bfhi(xv[i][1]), bfhi(xv[i][3])}};
#pragma unroll
      for (int c = 0; c < 4; ++c) {
        const f32x2 h = (x[c] - m) * r * c1[c] + sa[c];
        hv[i][c] = pk_bf16(h[0], h[1]);
        *(unsigned*)(himg + c * HL + 256 + 2 * pr) = hv[i][c];
      }
    }
    __syncthreads();
    STAMP(3);
    const unsigned* abase = (const unsigned*)(smem + L_KIMG + w * KIMG_B + (i16 & 1) * (KL * 2)) + ((2032 - (i16 & ~1) - 2 * (i16 & 1) + 8 * g) >> 1);
    const u16* bbase = himg + w * HL + 256 - 16 + 16 * i16 + 8 * g;
    f32x4 acc[8];
#pragma unroll
    for (int c = 0; c < 8; ++c) acc[c] = f32x4{0.f, 0.f, 0.f, 0.f};
    v4u O[2][16];
#define CLOAD1(buf, r, k) { if (CL[k] < 8) { const unsigned* ap = abase - 16 * (8 * CL[k] + (r)); O[buf][CL[k]] = v4u{ap[0], ap[1], ap[2], ap[3]}; } \
                            else O[buf][CL[k]] = *(const v4u*)(bbase + 32 * (8 * (CL[k] - 8) - (r))); }
#pragma unroll
    for (int k = 0; k < 16; ++k) CLOAD1(0, 0, k);
    SB();
    unsigned cwv = 0;
    int ws0 = OOB, ws1 = OOB;
#pragma unroll
    for (int r = 0; r < 8; ++r) {
      TS(r);
      if (r == 6) { wrdy = __all(cwv >= 7u); ORDER(); ws0 = wrdy ? z2s0 : OOB; ws1 = wrdy ? z2s1 : OOB; }
#pragma unroll
      for (int m = 0; m < 36; ++m) {
        acc[CA[m] + CB[m]] = mfma_bf(O[r & 1][CA[m]], O[r & 1][8 + CB[m]], acc[CA[m] + CB[m]]);
        if (r < 7 && (m & 1) == 0 && m < 32) CLOAD1((r + 1) & 1, r + 1, m >> 1);
        if ((m & 3) == 1) {
          const int v = m >> 2;
          if (r < 2 && v < 8) WSP_DMA(2 * r + (v >> 2), v & 3, L_WL);
          if (r == 5 && v == 0) cwv = cnt_load(p, C_W, lane);
          if (r >= 6) { const int q = 9 * (r - 6) + v; R[q] = bld1(rs, l16, ((q & 1) ? ws1 : ws0) + (q >> 1) * 1024); }
        }
        SB();
      }
    }
    TS(8); TSDUMP(0, 9);
    u16* ybuf = (u16*)(smem + L_KSCR);
#pragma unroll
    for (int c = 0; c < 8; ++c)
      *(v2u*)(ybuf + w * T + 256 * c + 16 * i16 + 4 * g) = v2u{pk_bf16(acc[c][0], acc[c][1]), pk_bf16(acc[c][2], acc[c][3])};
    __syncthreads();
    STAMP(4);
#pragma unroll
    for (int i = 0; i < 4; ++i) {
      const int pr = 256 * i + tid;
      const f32x2 x[4] = {f32x2{bflo(xv[i][0]), bflo(xv[i][2])}, f32x2{bfhi(xv[i][0]), bfhi(xv[i][2])}, f32x2{bflo(xv[i][1]), bflo(xv[i][3])}, f32x2{bfhi(xv[i][1]), bfhi(xv[i][3])}};
      f32x2 o[4];
#pragma unroll
      for (int c = 0; c < 4; ++c) o[c] = x[c] + (unp(*(const unsigned*)(ybuf + c * T + 2 * pr)) + unp(hv[i][c]) * dk[c]) * ga[c];
      bst1(rs, O_X1G + (q4 * T + 2 * pr) * 8, v4u{pk_bf16(o[0][0], o[1][0]), pk_bf16(o[2][0], o[3][0]), pk_bf16(o[0][1], o[1][1]), pk_bf16(o[2][1], o[3][1])});
    }
    SB();
    STAMP(5);
#pragma unroll
    for (int q = 18; q < 30; ++q) R[q] = bld1(rs, l16, ((q & 1) ? ws1 : ws0) + (q >> 1) * 1024);
    SB();
    asm volatile("s_waitcnt vmcnt(12)" ::: "memory");
    __syncthreads();
    arrive(p, C_X1, tid);
    SB();
#pragma unroll
    for (int q = 30; q < 40; ++q) R[q] = bld1(rs, l16, ((q & 1) ? ws1 : ws0) + (q >> 1) * 1024);
  } else {
    aux_role(p, rs, smem, (bi - 20) * 8 + bx, tag);
#pragma unroll
    for (int q = 0; q < 16; ++q) WSP_DMA(q >> 2, q & 3, L_WL);
  }
  if (!wrdy) {
    cnt_wait(p, C_W, 7u, lane, 0u);
#pragma unroll
    for (int q = 0; q < 40; ++q) R[q] = bld1(rs, l16, ((q & 1) ? z2s1 : z2s0) + (q >> 1) * 1024);
  }
  STAMP(6);
  unsigned char* xs = smem;
  float* sxL = (float*)(smem + L_MISC);
  float* st1 = (float*)(smem + L_MISC + 512) + w * 128;
  float* gsum = (float*)(smem + L_MISC + 2560);
  float* st2 = (float*)(smem + L_MISC + 4608) + w * 128;
  const v4u uz2a = bld1(rs, O_U + (FH + 16 * (b0 + (has0 ? lb0 : 0)) + (i16 & ~1)) * 8), uz2b = bld1(rs, O_U + (FH + 16 * (b0 + (has1 ? lb1 : 0)) + (i16 & ~1)) * 8);
  if (w == 3) {
#pragma unroll
    for (int ks = 0; ks < 20; ++ks) R[2 * ks + 1] = v4u{0x3f803f80u, 0x3f803f80u, 0x3f803f80u, 0x3f803f80u};
  }
  if (w == 0) cnt_wait(p, C_X1, 20u, lane, 0u);
  __syncthreads();
  STAMP(7);
  v4u X[20];
  {
    const int xoff = (8 * (lane >> 5) * T + 2 * (lane & 31)) * 8;
    const int xso = O_X1G + (2 * w * T + 64 * panel) * 8;
#pragma unroll
    for (int i = 0; i < 10; ++i) { X[2 * i] = bld1(rs, xoff, xso + i * (16 * T * 8)); X[2 * i + 1] = bld1(rs, xoff, xso + i * (16 * T * 8) + T * 8); }
  }
  SB();
  int xw;
  {
    const int t = 2 * (lane & 31);
    const int tp = (2 * (t >> 5) + ((t >> 2) & 1)) * 16 + 4 * ((t >> 3) & 3) + (t & 3);
    xw = (((lane >> 5) * 4 + w) * 64 + tp) * 16;
  }
  const int aoff = (g * 64 + i16) * 16;
  f32x4 a0[4], a1[4], gram = {0.f, 0.f, 0.f, 0.f};
#pragma unroll
  for (int i = 0; i < 4; ++i) { a0[i] = f32x4{0.f, 0.f, 0.f, 0.f}; a1[i] = a0[i]; }
#define XW(i, h) *(v4u*)(xs + (i) * 8192 + xw + 16 * (h)) = v4u{X[2 * (i)][2 * (h)], X[2 * (i)][2 * (h) + 1], X[2 * (i) + 1][2 * (h)], X[2 * (i) + 1][2 * (h) + 1]}
#define LDA(ks, i) (*(const v4u*)(xs + (ks) * 4096 + aoff + ((i) < 4 ? (i) : w) * 256))
  XW(0, 0); XW(0, 1); XW(1, 0); XW(1, 1);
  __syncthreads();
  STAMP(8);
  v4u WA[16], WB[16];
  {
    v4u A[2][5];
#pragma unroll
    for (int i = 0; i < 5; ++i) A[0][i] = LDA(0, i);
    SB();
#pragma unroll
    for (int ks = 0; ks < 20; ++ks) {
      if ((ks & 1) == 0) TS(ks >> 1);
      if ((ks & 3) == 3 && ks < 19) { __syncthreads(); SB(); }
#pragma unroll
      for (int m = 0; m < 9; ++m) {
        if (m < 8) { if (m & 1) a1[m >> 1] = mfma_bf(A[ks & 1][m >> 1], R[2 * ks + 1], a1[m >> 1]); else a0[m >> 1] = mfma_bf(A[ks & 1][m >> 1], R[2 * ks], a0[m >> 1]); }
        else gram = mfma_bf(A[ks & 1][4], A[ks & 1][4], gram);
        if (m < 5 && ks < 19) A[(ks + 1) & 1][m] = LDA(ks + 1, m);
        if (m == 5 && ks > 0) R[2 * ks - 2] = bld1(rs, l16, z1s0 + (ks - 1) * 1024);
        if (m == 8 && ks > 0) R[2 * ks - 1] = bld1(rs, l16, z1s1 + (ks - 1) * 1024);
        if ((m == 6 || m == 7) && (ks & 3) < 2 && ks < 16) XW(2 * (ks >> 2) + 2 + (ks & 3), m - 6);
        if ((m == 6 || m == 7) && ks >= 12 && !((ks & 3) < 2 && ks < 16)) { const int q = (ks < 16 ? 2 * (ks - 14) : 4 + 2 * (ks - 16)) + (m - 6); WA[q] = *(const v4u*)(smem + L_WL + wrd + q * 1024); }
        SB();
      }
    }
    TS(10); TSDUMP(16, 11);
    R[38] = bld1(rs, l16, z1s0 + 19 * 1024);
    R[39] = bld1(rs, l16, z1s1 + 19 * 1024);
#pragma unroll
    for (int q = 12; q < 16; ++q) WA[q] = *(const v4u*)(smem + L_WL + wrd + q * 1024);
  }
  if (w == 3 && i16 == 0) {
#pragma unroll
    for (int i = 0; i < 4; ++i) *(f32x4*)(sxL + i * 16 + 4 * g) = a1[i];
  }
  if ((i16 >> 2) == g) sxL[64 + w * 16 + i16] = (i16 & 3) == 0 ? gram[0] : (i16 & 3) == 1 ? gram[1] : (i16 & 3) == 2 ? gram[2] : gram[3];
  __syncthreads();
  STAMP(9);
  {
    const float m = sxL[lane] * (1.0f / D);
    const float var = fmaxf(sxL[64 + lane] * (1.0f / D) - m * m, 0.f);
    const float r = __builtin_amdgcn_rsqf(var + 1e-5f);
    *(f32x2*)(st1 + lane * 2) = f32x2{r, r * m};
  }
  {
    const float u0x = u2f((i16 & 1) ? uz2a[2] : uz2a[0]), u0y = u2f((i16 & 1) ? uz2a[3] : uz2a[1]);
    const float u1x = u2f((i16 & 1) ? uz2b[2] : uz2b[0]), u1y = u2f((i16 & 1) ? uz2b[3] : uz2b[1]);
    float s1[16], s2[16];
#pragma unroll
    for (int i = 0; i < 4; ++i) {
      const int t0 = i * 16 + 4 * g;
      const f32x4 sA = *(const f32x4*)(st1 + 2 * t0), sB = *(const f32x4*)(st1 + 2 * t0 + 4);
      const f32x2 rr[2] = {f32x2{sA[0], sA[2]}, f32x2{sB[0], sB[2]}}, rm[2] = {f32x2{sA[1], sA[3]}, f32x2{sB[1], sB[3]}};
#pragma unroll
      for (int h = 0; h < 2; ++h) {
        f32x2 v0 = gelu2(f32x2{a0[i][2 * h], a0[i][2 * h + 1]} * rr[h] + (u0y - rm[h] * u0x));
        f32x2 v1 = gelu2(f32x2{a1[i][2 * h], a1[i][2 * h + 1]} * rr[h] + (u1y - rm[h] * u1x));
        if (!has0) v0 = f32x2{0.f, 0.f};
        if (!has1) v1 = f32x2{0.f, 0.f};
        a0[i][2 * h] = v0[0]; a0[i][2 * h + 1] = v0[1]; a1[i][2 * h] = v1[0]; a1[i][2 * h + 1] = v1[1];
        const f32x2 q1 = v0 + v1, q2 = v0 * v0 + v1 * v1;
        s1[4 * i + 2 * h] = q1[0]; s1[4 * i + 2 * h + 1] = q1[1]; s2[4 * i + 2 * h] = q2[0]; s2[4 * i + 2 * h + 1] = q2[1];
        WSP_DMA(4 + (i >> 1), 2 * (i & 1) + h, L_WL);
      }
    }
    const float t1 = bfly16(s1, i16), t2 = bfly16(s2, i16);
    const int tp = (i16 >> 2) * 16 + 4 * g + (i16 & 3);
    bst1(rs, O_ZST + (((panel * 4 + w) * 8 + j) * 64 + tp) * 16, v4u{f2u(t1), tag, f2u(t2), tag});
  }
  SB();
  STAMP(10);
  f32x4 c0[4], c1[4];
#pragma unroll
  for (int i = 0; i < 4; ++i) { c0[i] = f32x4{0.f, 0.f, 0.f, 0.f}; c1[i] = c0[i]; }
  v4u A1[2][4];
#pragma unroll
  for (int i = 0; i < 4; ++i) A1[0][i] = LDA(0, i);
  SB();
#define Z1M(ks, m) { if ((m) & 1) c1[(m) >> 1] = mfma_bf(R[2 * (ks) + 1], A1[(ks) & 1][(m) >> 1], c1[(m) >> 1]); else c0[(m) >> 1] = mfma_bf(R[2 * (ks)], A1[(ks) & 1][(m) >> 1], c0[(m) >> 1]); \
    if ((m) < 4 && (ks) < 19) A1[((ks) + 1) & 1][m] = LDA((ks) + 1, m); }
#pragma unroll
  for (int ks = 0; ks < 10; ++ks)
#pragma unroll
    for (int m = 0; m < 8; ++m) {
      Z1M(ks, m);
      if (m == 5 && ks < 8) WSP_DMA(6 + (ks >> 2), ks & 3, L_WL);
      SB();
    }
  STAMP(11);
  {
    v4u v[8];
    for (unsigned s = 0; s < SPIN; ++s) {
#pragma unroll
      for (int jj = 0; jj < 8; ++jj) v[jj] = bld1(rs, O_ZST + (((panel * 4 + w) * 8 + jj) * 64 + lane) * 16);
      unsigned bad = 0;
#pragma unroll
      for (int jj = 0; jj < 8; ++jj) bad |= (v[jj][1] ^ tag) | (v[jj][3] ^ tag);
      if (__all(bad == 0u)) break;
      __builtin_amdgcn_s_sleep(1);
      asm volatile("" ::: "memory");
    }
    float a = 0.f, b = 0.f;
#pragma unroll
    for (int jj = 0; jj < 8; ++jj) { a += u2f(v[jj][0]); b += u2f(v[jj][2]); }
    *(f32x2*)(gsum + (w * 64 + lane) * 2) = f32x2{a, b};
  }
  DRAIN();
#pragma unroll
  for (int q = 0; q < 16; ++q) WB[q] = *(const v4u*)(smem + L_WL + wrd + q * 1024);
  __syncthreads();
  STAMP(12);
  {
    const f32x2 s = (*(const f32x2*)(gsum + lane * 2) + *(const f32x2*)(gsum + 128 + lane * 2)) + (*(const f32x2*)(gsum + 256 + lane * 2) + *(const f32x2*)(gsum + 384 + lane * 2));
    const float m = s[0] * (1.0f / FH);
    const float var = fmaxf(s[1] * (1.0f / FH) - m * m, 0.f);
    const float r = __builtin_amdgcn_rsqf(var + 1e-5f);
    *(f32x2*)(st2 + lane * 2) = f32x2{r, r * m};
  }
  {
    unsigned o0[4][2], o1[4][2];
#pragma unroll
    for (int i = 0; i < 4; ++i) {
      const int t0 = i * 16 + 4 * g;
      const f32x4 sA = *(const f32x4*)(st2 + 2 * t0), sB = *(const f32x4*)(st2 + 2 * t0 + 4);
      o0[i][0] = pk_bf16(a0[i][0] * sA[0] - sA[1], a0[i][1] * sA[2] - sA[3]);
      o0[i][1] = pk_bf16(a0[i][2] * sB[0] - sB[1], a0[i][3] * sB[2] - sB[3]);
      o1[i][0] = pk_bf16(a1[i][0] * sA[0] - sA[1], a1[i][1] * sA[2] - sA[3]);
      o1[i][1] = pk_bf16(a1[i][2] * sB[0] - sB[1], a1[i][3] * sB[2] - sB[3]);
    }
    const int zo0 = has0 ? O_Z2N + (2 * panel) * 1024 + (b0 + lb0) * 65536 + l16 : OOB, zo1 = has1 ? O_Z2N + (2 * panel) * 1024 + (b0 + lb1) * 65536 + l16 : OOB;
#pragma unroll
    for (int pp = 0; pp < 2; ++pp) {
      bst1(rs, zo0 + pp * 1024, v4u{o0[2 * pp][0], o0[2 * pp][1], o0[2 * pp + 1][0], o0[2 * pp + 1][1]});
      bst1(rs, zo1 + pp * 1024, v4u{o1[2 * pp][0], o1[2 * pp][1], o1[2 * pp + 1][0], o1[2 * pp + 1][1]});
    }
  }
  SB();
#pragma unroll
  for (int ks = 10; ks < 20; ++ks)
#pragma unroll
    for (int m = 0; m < 8; ++m) {
      Z1M(ks, m);
      if ((m == 4 || m == 7) && ks < 18) { const int q = 2 * (ks - 10) + (m == 7); WSP_DMA(8 + (q >> 2), q & 3, L_WL); }
      SB();
    }
  asm volatile("s_waitcnt vmcnt(16)" ::: "memory");
  __syncthreads();
  GAS unsigned* flags = (GAS unsigned*)(p.ws + O_FLAG);
  if (tid == 0) __hip_atomic_store(flags + j * 32 + panel, tag, RLX_AGENT);
  STAMP(13);
  const int fa = 16 * (b0 + (has0 ? lb0 : 0)) + 4 * g, fb = 16 * (b0 + (has1 ? lb1 : 0)) + 4 * g;
  const v4u ua0 = bld1(rs, O_U + fa * 8), ua1 = bld1(rs, O_U + fa * 8 + 16), ub0 = bld1(rs, O_U + fb * 8), ub1 = bld1(rs, O_U + fb * 8 + 16);
  const int tcol = 64 * panel + 8 * (i16 >> 2) + (i16 & 3);
  unsigned bias_u[4];
#pragma unroll
  for (int tb = 0; tb < 4; ++tb) bias_u[tb] = p.b_sp[tcol + 32 * (tb >> 1) + 4 * (tb & 1)];
  const unsigned csv = cnt_load(p, C_S, lane);
  SB();
#pragma unroll
  for (int q = 0; q < 16; ++q) WSP_DMA(12 + (q >> 2), q & 3, 0);
  SB();
  v4u Z[16][7];
  const int zso = O_Z2N + b0 * 65536 + 16 * w * 1024;
  const int f6 = j < 6 ? 6 * 65536 : OOB;
#define Z_LD(i, f) Z[i][f] = bld1(rs, l16, zso + (((i) + rot) & 15) * 1024 + ((f) < 6 ? (f) * 65536 : f6))
  unsigned z1p[2][4][2];
  {
    const f32x2 ux[2][2] = {{f32x2{u2f(ua0[0]), u2f(ua0[2])}, f32x2{u2f(ua1[0]), u2f(ua1[2])}}, {f32x2{u2f(ub0[0]), u2f(ub0[2])}, f32x2{u2f(ub1[0]), u2f(ub1[2])}}};
    const f32x2 uy[2][2] = {{f32x2{u2f(ua0[1]), u2f(ua0[3])}, f32x2{u2f(ua1[1]), u2f(ua1[3])}}, {f32x2{u2f(ub0[1]), u2f(ub0[3])}, f32x2{u2f(ub1[1]), u2f(ub1[3])}}};
    STAMP(14);
    flag_wait8(flags + j * 32 + 8 * w, tag, lane);
    STAMP(15);
#pragma unroll
    for (int i = 0; i < 4; ++i) {
#pragma unroll
      for (int f = 0; f < 7; ++f) Z_LD(i, f);
      SB();
      const f32x2 s = *(const f32x2*)(st1 + 2 * (16 * i + i16));
#pragma unroll
      for (int h = 0; h < 2; ++h) {
        const f32x2 v0 = gelu2(f32x2{c0[i][2 * h], c0[i][2 * h + 1]} * s[0] + (uy[0][h] - ux[0][h] * s[1]));
        const f32x2 v1 = gelu2(f32x2{c1[i][2 * h], c1[i][2 * h + 1]} * s[0] + (uy[1][h] - ux[1][h] * s[1]));
        z1p[0][i][h] = pk_bf16(v0[0], v0[1]);
        z1p[1][i][h] = pk_bf16(v1[0], v1[1]);
        asm volatile("" : "+v"(z1p[0][i][h]), "+v"(z1p[1][i][h]));
      }
      SB();
    }
  }
  cnt_wait(p, C_S, 12u, lane, csv);
  STAMP(16);
  f32x4 acc[7][4];
#pragma unroll
  for (int f = 0; f < 7; ++f)
#pragma unroll
    for (int t = 0; t < 4; ++t) acc[f][t] = f32x4{0.f, 0.f, 0.f, 0.f};
  const int woso = O_WOUTF + (5 * j) * 27 * 1024;
  v4u WO[35];
  v4u WL[2][4];
#define WO_LD(q) WO[q] = bld1(rs, l16, woso + (((q) % 5) * 27 + min(7 * w + (q) / 5, 26)) * 1024)
#pragma unroll
  for (int i = 0; i < 16; ++i) {
    TS(i);
#pragma unroll
    for (int f = 0; f < 7; ++f) {
#pragma unroll
      for (int t = 0; t < 4; ++t) {
        acc[f][t] = mfma_bf(Z[i][f], i < 4 ? WA[(i & 3) * 4 + t] : (i < 8 ? WB[(i & 3) * 4 + t] : WL[i & 1][t]), acc[f][t]);
        if (t == 0) { if (i + 4 < 16) Z_LD(i + 4 < 16 ? i + 4 : 0, f); else WO_LD(7 * (i >= 12 ? i - 12 : 0) + f); }
        if (t == 2 && f < 4 && i >= 7 && i < 15) WL[(i + 1) & 1][f] = *(const v4u*)(smem + (i + 1 < 12 ? L_WL : 0) + wrd + (((i + 1) & 3) * 4 + f) * 1024);
        SB();
      }
    }
  }
  TS(16); TSDUMP(32, 17);
#pragma unroll
  for (int q = 28; q < 35; ++q) WO_LD(q);
  __syncthreads();
  STAMP(17);
  f32x4* red = (f32x4*)smem;
#pragma unroll
  for (int f = 0; f < 7; ++f) {
    const int o = f >> 1;
    if (o != w) {
      const int sr = (w - o - 1) & 3;
#pragma unroll
      for (int t = 0; t < 4; ++t) red[((f * 3 + sr) * 4 + t) * 64 + lane] = acc[f][t];
    }
  }
  f32x4 o0[4], o1[4];
#pragma unroll
  for (int t = 0; t < 4; ++t) {
    if (w == 0) { o0[t] = acc[0][t]; o1[t] = acc[1][t]; }
    else if (w == 1) { o0[t] = acc[2][t]; o1[t] = acc[3][t]; }
    else if (w == 2) { o0[t] = acc[4][t]; o1[t] = acc[5][t]; }
    else { o0[t] = acc[6][t]; o1[t] = acc[6][t]; }
  }
  __syncthreads();
  {
    const int f1 = w < 3 ? 2 * w + 1 : 6;
#pragma unroll
    for (int s = 0; s < 3; ++s)
#pragma unroll
      for (int t = 0; t < 4; ++t) {
        o0[t] += red[((2 * w * 3 + s) * 4 + t) * 64 + lane];
        o1[t] += red[((f1 * 3 + s) * 4 + t) * 64 + lane];
      }
    const int ks3 = w < 3 ? 3 * j + w : 24 + (j >> 1);
    const int po = O_P + ((4 * panel) * 27 + ks3) * 1024 + l16;
#pragma unroll
    for (int t = 0; t < 4; ++t) {
      const float bias = bf2f(bias_u[t]);
      const unsigned q0 = pk_bf16(bflo(z1p[0][t][0]) * (o0[t][0] + bias), bfhi(z1p[0][t][0]) * (o0[t][1] + bias));
      const unsigned q1 = pk_bf16(bflo(z1p[0][t][1]) * (o0[t][2] + bias), bfhi(z1p[0][t][1]) * (o0[t][3] + bias));
      const unsigned q2 = pk_bf16(bflo(z1p[1][t][0]) * (o1[t][0] + bias), bfhi(z1p[1][t][0]) * (o1[t][1] + bias));
      const unsigned q3 = pk_bf16(bflo(z1p[1][t][1]) * (o1[t][2] + bias), bfhi(z1p[1][t][1]) * (o1[t][3] + bias));
      if (w < 3) bst1(rs, po + t * 27 * 1024, v4u{q0, q1, q2, q3});
      else if (j < 6) bst1_64(rs, po + (j & 1) * 8 + t * 27 * 1024, v2u{q0, q1});
    }
  }
  STAMP(18);
  DRAIN();
  __syncthreads();
  if (tid == 0) __hip_atomic_store(flags + 256 + panel * 32 + j, tag, RLX_AGENT);
  STAMP(19);
  const int trow = tcol + 32 * (w >> 1) + 4 * (w & 1);
  v2u x1v[5]; v4u gb0[5], gb1[5];
#pragma unroll
  for (int db = 0; db < 5; ++db) {
    const int d = 80 * j + 16 * db + 4 * g;
    x1v[db] = __builtin_amdgcn_raw_buffer_load_b64(rs, O_X1G + ((d >> 2) * T + trow) * 8, 0, AUX_SC1);
    gb0[db] = bld1(rs, O_MODG + (5 * D + d) * 8);
    gb1[db] = bld1(rs, O_MODG + (5 * D + d) * 8 + 16);
  }
  flag_wait8(flags + 256 + panel * 32, tag, lane);
  STAMP(20);
  v4u P[7][4];
  const int pso = O_P + (4 * panel) * 27 * 1024;
#pragma unroll
  for (int kk = 0; kk < 7; ++kk)
#pragma unroll
    for (int t = 0; t < 4; ++t) P[kk][t] = bld1(rs, l16, (7 * w + kk < 27) ? pso + (t * 27 + 7 * w + kk) * 1024 : OOB);
  SB();
  f32x4 e[5][4];
#pragma unroll
  for (int d = 0; d < 5; ++d)
#pragma unroll
    for (int t = 0; t < 4; ++t) e[d][t] = f32x4{0.f, 0.f, 0.f, 0.f};
#pragma unroll
  for (int kk = 0; kk < 7; ++kk) {
#pragma unroll
    for (int t = 0; t < 4; ++t)
#pragma unroll
      for (int d = 0; d < 5; ++d) e[d][t] = mfma_bf(WO[5 * kk + d], P[kk][t], e[d][t]);
    SB();
  }
  STAMP(21);
#pragma unroll
  for (int t = 0; t < 4; ++t) {
    if (t != w) {
      const int sr = (w - t - 1) & 3;
#pragma unroll
      for (int d = 0; d < 5; ++d) red[((t * 3 + sr) * 5 + d) * 64 + lane] = e[d][t];
    }
  }
  f32x4 eo[5];
#pragma unroll
  for (int d = 0; d < 5; ++d) eo[d] = w == 0 ? e[d][0] : w == 1 ? e[d][1] : w == 2 ? e[d][2] : e[d][3];
  __syncthreads();
  u16* orow = p.out + (size_t)trow * D + 80 * j + 4 * g;
#pragma unroll
  for (int d = 0; d < 5; ++d) {
#pragma unroll
    for (int s = 0; s < 3; ++s) eo[d] += red[((w * 3 + s) * 5 + d) * 64 + lane];
    const float r0 = bflo(x1v[d][0]) + u2f(gb0[d][0]) * eo[d][0], r1 = bfhi(x1v[d][0]) + u2f(gb0[d][2]) * eo[d][1];
    const float r2 = bflo(x1v[d][1]) + u2f(gb1[d][0]) * eo[d][2], r3 = bfhi(x1v[d][1]) + u2f(gb1[d][2]) * eo[d][3];
    *(v2u*)(orow + 16 * d) = v2u{pk_bf16(r0, r1), pk_bf16(r2, r3)};
  }
  if (blockIdx.x == 0 && tid == 0) __hip_atomic_store((GAS unsigned*)(p.ws + O_EPOCH), tag, RLX_AGENT);
  STAMP(22);
}

extern "C" void kernel_launch(void* const* d_in, const int* in_sizes, int n_in, void* d_out, int out_size, void* d_ws,
                              size_t ws_size, hipStream_t stream) {
  Params p;
  p.u = (const u16*)d_in[0]; p.a_re = (const u16*)d_in[1]; p.a_im = (const u16*)d_in[2];
  p.b_re = (const u16*)d_in[3]; p.b_im = (const u16*)d_in[4]; p.c_re = (const u16*)d_in[5]; p.c_im = (const u16*)d_in[6];
  p.log_dt = (const u16*)d_in[7]; p.dskip = (const u16*)d_in[8];
  p.w_in = (const u16*)d_in[10]; p.w_sp = (const u16*)d_in[11]; p.b_sp = (const u16*)d_in[12]; p.w_out = (const u16*)d_in[13];
  p.cvec = (const u16*)d_in[14]; p.w_mod = (const u16*)d_in[15];
  p.out = (u16*)d_out; p.ws = (unsigned char*)d_ws;
  (void)hipFuncSetAttribute((const void*)k_fused, hipFuncAttributeMaxDynamicSharedMemorySize, L_TOTAL);
  k_fused<<<256, 256, L_TOTAL, stream>>>(p);
}
```

```cpp
#include <hip/hip_runtime.h>
#include <hip/hip_bf16.h>
#include <stdint.h>
#include <utility>

typedef unsigned v4u __attribute__((ext_vector_type(4)));
typedef unsigned v2u __attribute__((ext_vector_type(2)));
typedef float f32x4 __attribute__((ext_vector_type(4)));
typedef float f32x2 __attribute__((ext_vector_type(2)));
typedef short bf16x8 __attribute__((ext_vector_type(8)));
typedef _Float16 f16x8 __attribute__((ext_vector_type(8)));
typedef _Float16 f16x2 __attribute__((ext_vector_type(2)));
typedef __bf16 bf16x2_t __attribute__((ext_vector_type(2)));
typedef unsigned short u16;
typedef unsigned long long u64;
#define GAS __attribute__((address_space(1)))
#define LAS __attribute__((address_space(3)))
#define RLX_AGENT __ATOMIC_RELAXED, __HIP_MEMORY_SCOPE_AGENT
#define DEVI __device__ __forceinline__
#define SB() __builtin_amdgcn_sched_barrier(0)
template <int... I, class F> __device__ __forceinline__ void sfor_impl(std::integer_sequence<int, I...>, F&& f) { (f(std::integral_constant<int, I>{}), ...); }
template <int N, class F> __device__ __forceinline__ void sfor(F&& f) { sfor_impl(std::make_integer_sequence<int, N>{}, f); }
#define LAM [&](auto ic_) __attribute__((always_inline))
#define DRAIN() asm volatile("s_waitcnt vmcnt(0)" ::: "memory")

constexpr int T = 2048, D = 640, NST = 128, FH = 864, F2 = 1728;
constexpr int KL = 2080, KIMG_B = 2 * KL * 2, HL = 2304;
constexpr unsigned SPIN = 1u << 17;
constexpr int AUX_SC1 = 16, AUX_NT = 2;
constexpr int O_EPOCH = 0;
constexpr int O_INIT = 256;
constexpr int O_CNT = 4096;
constexpr int O_FLAG = O_CNT + 3 * 64 * 256;
constexpr int O_MODG = O_FLAG + 8192;
constexpr int O_STG = O_MODG + 3840 * 8 + 2048;
constexpr int O_U = O_STG + 2048 * 16;
constexpr int O_ZST = O_U + 16384;
constexpr int O_X1G = O_ZST + 32 * 32 * 64 * 16;
constexpr int O_WINF = O_X1G + T * D * 2;
constexpr int O_WOUTF = O_WINF + 108 * 20 * 1024;
constexpr int O_Z2N = O_WOUTF + 40 * 27 * 1024;
constexpr int O_P = O_Z2N + 54 * 64 * 1024;
constexpr int O_END = O_P + 128 * 27 * 1024;
constexpr int C_X1 = 0, C_W = 1, C_S = 2;

constexpr size_t O_STAMP = 200u << 20;
#define TS(k) do {} while (0)
#define TSDUMP(base, n) do {} while (0)
#define STAMP(idx) do {} while (0)

DEVI float u2f(unsigned u) { return __builtin_bit_cast(float, u); }
DEVI unsigned f2u(float f) { return __builtin_bit_cast(unsigned, f); }
DEVI float bf2f(unsigned b) { return u2f(b << 16); }
DEVI float bflo(unsigned u) { return u2f(u << 16); }
DEVI float bfhi(unsigned u) { return u2f(u & 0xffff0000u); }
DEVI unsigned pk_bf16(float a, float b) {
  f32x2 v = {a, b};
  bf16x2_t r = __builtin_convertvector(v, bf16x2_t);
  return __builtin_bit_cast(unsigned, r);
}
DEVI float rbf(float a) { return bflo(pk_bf16(a, 0.f)); }
DEVI unsigned pk_f16(float a, float b) {
  f32x2 v = {a, b};
  f16x2 r = __builtin_convertvector(v, f16x2);
  return __builtin_bit_cast(unsigned, r);
}
DEVI f32x4 mfma_bf(v4u a, v4u b, f32x4 c) {
  return __builtin_amdgcn_mfma_f32_16x16x32_bf16(__builtin_bit_cast(bf16x8, a), __builtin_bit_cast(bf16x8, b), c, 0, 0, 0);
}
DEVI f32x4 mfma_h(v4u a, v4u b, f32x4 c) {
  return __builtin_amdgcn_mfma_f32_16x16x32_f16(__builtin_bit_cast(f16x8, a), __builtin_bit_cast(f16x8, b), c, 0, 0, 0);
}
typedef __amdgpu_buffer_rsrc_t rsrc_t;
DEVI v4u bld1(rsrc_t rs, int voff, int soff = 0) { return __builtin_amdgcn_raw_buffer_load_b128(rs, voff, soff, AUX_SC1); }
DEVI void bst1(rsrc_t rs, int voff, v4u v) { __builtin_amdgcn_raw_buffer_store_b128(v, rs, voff, 0, AUX_SC1); }
DEVI void bst1_64(rsrc_t rs, int voff, v2u v) { __builtin_amdgcn_raw_buffer_store_b64(v, rs, voff, 0, AUX_SC1); }
template <int CTRL> DEVI float dppf(float x) { return u2f((unsigned)__builtin_amdgcn_update_dpp(0, (int)f2u(x), CTRL, 0xf, 0xf, true)); }
DEVI float dot2(unsigned a, unsigned b, float c) { return __builtin_amdgcn_fdot2_f32_bf16(__builtin_bit_cast(bf16x2_t, a), __builtin_bit_cast(bf16x2_t, b), c, false); }
#define PIN(x) asm volatile("" : "+v"(x))
#define ORDER() __builtin_amdgcn_fence(__ATOMIC_ACQUIRE, "wavefront")

struct Params {
  const u16 *u, *a_re, *a_im, *b_re, *b_im, *c_re, *c_im, *log_dt, *dskip, *w_in, *w_sp, *b_sp, *w_out, *cvec, *w_mod;
  u16* out;
  unsigned char* ws;
};

DEVI int blk0(int j) { return j < 6 ? 7 * j : 42 + 6 * (j - 6); }
DEVI int pair_blk(int ks3, int h) {
  if (ks3 < 24) { const int j = ks3 / 3, w = ks3 - 3 * j; return blk0(j) + 2 * w + h; }
  return 7 * (2 * (ks3 - 24) + h) + 6;
}
DEVI void arrive(const Params& p, int c, int tid) {
  if (tid < 8) __hip_atomic_fetch_add((GAS unsigned*)(p.ws + O_CNT) + ((c * 8 + tid) * 8 + (blockIdx.x & 7)) * 64, 1u, RLX_AGENT);
}
DEVI unsigned cnt_load(const Params& p, int c, int lane) {
  return __hip_atomic_load((GAS unsigned*)(p.ws + O_CNT) + ((c * 8 + ((blockIdx.x >> 3) & 7)) * 8 + (lane & 7)) * 64, RLX_AGENT);
}
DEVI void cnt_wait(const Params& p, int c, unsigned want, int lane, unsigned first) {
  unsigned v = first;
  for (unsigned s = 0; s < SPIN; ++s) {
    if (__all(v >= want)) break;
    __builtin_amdgcn_s_sleep(1);
    v = cnt_load(p, c, lane);
  }
  ORDER();
}
DEVI void flag_wait8(GAS unsigned* f, unsigned tag, int lane) {
  for (unsigned s = 0; s < SPIN; ++s) {
    const unsigned v = __hip_atomic_load(f + (lane & 7), RLX_AGENT);
    if (__all(v == tag)) break;
    __builtin_amdgcn_s_sleep(1);
  }
  ORDER();
}

struct C2 { f32x2 re, im; };
DEVI C2 cmul2(C2 a, C2 b) { return C2{a.re * b.re - a.im * b.im, a.re * b.im + a.im * b.re}; }
DEVI C2 cexp2(f32x2 are2, f32x2 arev, float l) {
  const f32x2 e = are2 * l, q = arev * l;
  const f32x2 m = {__builtin_amdgcn_exp2f(e[0]), __builtin_amdgcn_exp2f(e[1])};
  const float p0 = __builtin_amdgcn_fractf(q[0]), p1 = __builtin_amdgcn_fractf(q[1]);
  return C2{m * f32x2{__builtin_amdgcn_cosf(p0), __builtin_amdgcn_cosf(p1)}, m * f32x2{__builtin_amdgcn_sinf(p0), __builtin_amdgcn_sinf(p1)}};
}
constexpr int RS = 132;
DEVI void put_row(unsigned* buf, int row, int lane, C2 v) { *(v2u*)(buf + row * RS + 2 * lane) = v2u{pk_f16(v.re[0], v.im[0]), pk_f16(v.re[1], v.im[1])}; }
DEVI f32x2 unp(unsigned u) { return f32x2{bflo(u), bfhi(u)}; }

template <class HK> DEVI void kgen_wave(unsigned par[6], float dt, unsigned* buf0, unsigned* buf1, u16* tab, unsigned char* img, int lane, HK&& hook) {
  const int c = lane & 15, g = lane >> 4;
  const f32x2 are2 = unp(par[0]) * (dt * 1.4426950408889634f), arev = unp(par[1]) * (dt * 0.15915494309189535f), nrev = -arev;
  C2 W = cmul2(C2{unp(par[4]), unp(par[5])}, C2{unp(par[2]) * dt, unp(par[3]) * dt});
  float mx = fmaxf(fmaxf(fabsf(W.re[0]), fabsf(W.re[1])), fmaxf(fabsf(W.im[0]), fabsf(W.im[1])));
  unsigned mu = f2u(mx);
  mu = max(mu, f2u(dppf<0x128>(u2f(mu)))); mu = max(mu, f2u(dppf<0x124>(u2f(mu)))); mu = max(mu, f2u(dppf<0x122>(u2f(mu)))); mu = max(mu, f2u(dppf<0x121>(u2f(mu))));
  const unsigned m0 = __builtin_amdgcn_readlane(mu, 0), m1 = __builtin_amdgcn_readlane(mu, 16), m2 = __builtin_amdgcn_readlane(mu, 32), m3 = __builtin_amdgcn_readlane(mu, 48);
  unsigned eb = (max(max(m0, m1), max(m2, m3)) >> 23) & 0xffu;
  eb = eb < 2u ? 2u : (eb > 250u ? 250u : eb);
  const float sc = u2f((eb + 1u) << 23), isc = u2f((253u - eb) << 23);
  W.re *= isc; W.im *= isc;
  {
    const C2 e8 = cexp2(are2, arev, 8.f);
    C2 W8 = cmul2(W, cexp2(are2, arev, 64.f));
#pragma unroll
    for (int b = 0; b < 8; ++b) {
      put_row(buf0, b, lane, W); put_row(buf0, 8 + b, lane, W8);
      if (b < 7) { W = cmul2(W, e8); W8 = cmul2(W8, e8); }
      if (b == 0) hook(2);
      if (b == 4) hook(3);
    }
  }
  v4u A[8];
#pragma unroll
  for (int ks = 0; ks < 8; ++ks) A[ks] = *(const v4u*)(buf0 + c * RS + 16 * ks + 4 * g);
  C2 er[8];
  er[0] = C2{f32x2{1.f, 1.f}, f32x2{0.f, 0.f}};
  er[1] = cexp2(are2, nrev, 1.f);
  er[2] = cmul2(er[1], er[1]); er[3] = cmul2(er[2], er[1]); er[4] = cmul2(er[2], er[2]);
  er[5] = cmul2(er[4], er[1]); er[6] = cmul2(er[3], er[3]); er[7] = cmul2(er[4], er[3]);
  hook(4);
  const C2 e128 = cexp2(are2, nrev, 128.f);
#pragma unroll
  for (int r = 0; r < 8; ++r) { put_row(buf1, r, lane, er[r]); put_row(buf1, 8 + r, lane, r ? cmul2(e128, er[r]) : e128); }
  f32x4 acc0 = {0.f, 0.f, 0.f, 0.f}, acc1 = acc0, acc2 = acc0;
#pragma unroll
  for (int ks = 0; ks < 8; ++ks) acc0 = mfma_h(A[ks], *(const v4u*)(buf1 + c * RS + 16 * ks + 4 * g), acc0);
  hook(5);
  C2 e = cmul2(e128, e128);
  C2 f = cexp2(are2, nrev, 1024.f);
#pragma unroll
  for (int a = 0; a < 2; ++a) {
#pragma unroll
    for (int r = 0; r < 4; ++r) put_row(buf0, 4 * a + r, lane, r ? cmul2(e, er[2 * r]) : e);
    e = cmul2(e, e128);
  }
  hook(6);
#pragma unroll
  for (int a = 0; a < 4; ++a) {
    put_row(buf0, 8 + 2 * a, lane, e); put_row(buf0, 9 + 2 * a, lane, cmul2(e, er[4])); if (a < 3) e = cmul2(e, e128);
    put_row(buf1, 2 * a, lane, f); f = cmul2(f, e128); put_row(buf1, 2 * a + 1, lane, f); f = cmul2(f, e128);
    if (a == 1) hook(7);
  }
  put_row(buf1, 8, lane, f);
  hook(8);
#pragma unroll
  for (int ks = 0; ks < 8; ++ks) {
    acc1 = mfma_h(A[ks], *(const v4u*)(buf0 + c * RS + 16 * ks + 4 * g), acc1);
    acc2 = mfma_h(A[ks], *(const v4u*)(buf1 + c * RS + 16 * ks + 4 * g), acc2);
  }
  const int cv0 = 128 * (c >> 3) + (c & 7);
  const int cv1 = c < 8 ? 256 + 128 * (c >> 2) + 2 * (c & 3) : 512 + 128 * ((c - 8) >> 1) + 4 * (c & 1);
  const int cv2 = c < 9 ? 1024 + 128 * c : 2048;
#pragma unroll
  for (int r = 0; r < 4; ++r) {
    const int lb = 8 * (4 * g + r);
    const unsigned q01 = pk_bf16(acc0[r] * sc, acc1[r] * sc);
    tab[lb + cv0] = (u16)q01;
    tab[lb + cv1] = (u16)(q01 >> 16);
    tab[lb + cv2 + (c > 8 ? 1 : 0)] = (u16)pk_bf16(acc2[r] * sc, 0.f);
  }
#pragma unroll
  for (int it = 0; it < 2; ++it) {
    const int j = 64 * it + lane, base = 2048 - 8 * j;
    const unsigned H = tab[base], L = tab[base - 8], X = (j & 1) ? L : H;
    const unsigned HH = H | (H << 16), LL = L | (L << 16);
    *(v4u*)(img + j * 16) = v4u{HH, HH, X | (L << 16), LL};
    *(v4u*)(img + KL * 2 + j * 16) = v4u{HH, H | (X << 16), LL, LL};
  }
  {
    const int j = 128 + lane, base = 2048 - 8 * j;
    const unsigned H = tab[base], M = tab[base - 4], L = tab[base - 8];
    const unsigned HH = H | (H << 16), MM = M | (M << 16), LL = L | (L << 16);
    *(v4u*)(img + j * 16) = v4u{HH, H | (M << 16), MM, LL};
    *(v4u*)(img + KL * 2 + j * 16) = v4u{HH, MM, M | (L << 16), LL};
  }
  {
    const int j = 192 + lane, base = 2048 - 8 * j;
    const int o = lane < 32 ? 1 : 0;
    const unsigned v0 = tab[base], v1 = tab[base - 1 + o], v2 = tab[base - 2], v3 = tab[base - 3 - o], v4 = tab[base - 4];
    const unsigned v5 = tab[base - 5 + o], v6 = tab[base - 6], v7 = tab[base - 7 - o], v8 = tab[base - 8];
    *(v4u*)(img + j * 16) = v4u{v0 | (v1 << 16), v2 | (v3 << 16), v4 | (v5 << 16), v6 | (v7 << 16)};
    *(v4u*)(img + KL * 2 + j * 16) = v4u{v1 | (v2 << 16), v3 | (v4 << 16), v5 | (v6 << 16), v7 | (v8 << 16)};
    if (lane < 8) *(v4u*)(img + (lane >> 2) * (KL * 2) + (256 + (lane & 3)) * 16) = v4u{lane == 0 ? (unsigned)tab[0] : 0u, 0u, 0u, 0u};
  }
}

DEVI unsigned plo(unsigned a, unsigned b) { return __builtin_amdgcn_perm(b, a, 0x05040100u); }
DEVI unsigned phi(unsigned a, unsigned b) { return __builtin_amdgcn_perm(b, a, 0x07060302u); }
DEVI void wout_unit(const Params& p, rsrc_t rs, unsigned char* lw, int unit, int lane) {
  const int ks = unit / 5, dc = unit - 5 * ks, d = 128 * dc + 2 * lane;
  const int f0 = 16 * pair_blk(ks, 0), f1 = 16 * pair_blk(ks, 1);
  unsigned wv[4][8];
#pragma unroll
  for (int g = 0; g < 4; ++g)
#pragma unroll
    for (int j = 0; j < 8; ++j) wv[g][j] = *(const unsigned*)(p.w_out + (size_t)((j < 4 ? f0 : f1) + 4 * g + (j & 3)) * D + d);
#pragma unroll
  for (int g = 0; g < 4; ++g) {
    unsigned char* dst = lw + (lane >> 3) * 1024 + (g * 16 + 2 * (lane & 7)) * 16;
    *(v4u*)dst = v4u{plo(wv[g][0], wv[g][1]), plo(wv[g][2], wv[g][3]), plo(wv[g][4], wv[g][5]), plo(wv[g][6], wv[g][7])};
    *(v4u*)(dst + 16) = v4u{phi(wv[g][0], wv[g][1]), phi(wv[g][2], wv[g][3]), phi(wv[g][4], wv[g][5]), phi(wv[g][6], wv[g][7])};
  }
#pragma unroll
  for (int i = 0; i < 8; ++i) bst1(rs, O_WOUTF + ((8 * dc + i) * 27 + ks) * 1024 + lane * 16, *(const v4u*)(lw + i * 1024 + lane * 16));
}

DEVI void aux_role(const Params& p, rsrc_t rs, unsigned char* smem, int a, unsigned tag) {
  const int tid = threadIdx.x, lane = tid & 63, w = __builtin_amdgcn_readfirstlane(tid >> 6);
  float* fl = (float*)smem;
  constexpr unsigned ONES = 0x3f803f80u;
  unsigned wv[5][8];
  if (a < 60) {
    const int rg = tid >> 3, cl = tid & 7;
    unsigned* sL = (unsigned*)(smem + 12288);
    v4u mv[20];
    const v2u cv = *(const v2u*)(p.cvec + 4 * min(tid, 159));
#pragma unroll
    for (int i = 0; i < 20; ++i) mv[i] = __builtin_nontemporal_load((const v4u*)(p.w_mod + (size_t)(20 * rg + i) * 3840 + 64 * a + 8 * cl));
    SB();
    if (tid < 160) {
      unsigned sv[4];
#pragma unroll
      for (int i = 0; i < 4; ++i) {
        const float c = (i & 1) ? bfhi(cv[i >> 1]) : bflo(cv[i >> 1]);
        sv[i] = pk_bf16(c * __builtin_amdgcn_rcpf(1.0f + __builtin_amdgcn_exp2f(-1.4426950408889634f * c)), 0.f);
      }
      *(v4u*)(sL + 4 * tid) = v4u{sv[0], sv[1], sv[2], sv[3]};
    }
    __syncthreads();
    v4u s4[5];
#pragma unroll
    for (int i = 0; i < 5; ++i) s4[i] = *(const v4u*)(sL + 20 * rg + 4 * i);
    float acc[8];
#pragma unroll
    for (int j = 0; j < 8; ++j) acc[j] = 0.f;
    const u16* wsrc = p.w_in + (size_t)(8 * (tid >> 4)) * F2 + 32 * min(a, 53) + 2 * (tid & 15);
#pragma unroll
    for (int i = 0; i < 20; ++i) {
      const unsigned slo = s4[i >> 2][i & 3], shi = slo << 16;
#pragma unroll
      for (int j = 0; j < 4; ++j) { acc[2 * j] = dot2(mv[i][j], slo, acc[2 * j]); acc[2 * j + 1] = dot2(mv[i][j], shi, acc[2 * j + 1]); }
      wv[i >> 2][2 * (i & 3)] = *(const unsigned*)(wsrc + (size_t)(128 * (i >> 2) + 2 * (i & 3)) * F2);
      wv[i >> 2][2 * (i & 3) + 1] = *(const unsigned*)(wsrc + (size_t)(128 * (i >> 2) + 2 * (i & 3) + 1) * F2);
      SB();
    }
    *(f32x4*)(fl + rg * 68 + cl * 8) = f32x4{acc[0], acc[1], acc[2], acc[3]};
    *(f32x4*)(fl + rg * 68 + cl * 8 + 4) = f32x4{acc[4], acc[5], acc[6], acc[7]};
    __syncthreads();
    {
      float s = 0.f;
#pragma unroll
      for (int r = 0; r < 8; ++r) s += fl[(8 * (tid & 3) + r) * 68 + (tid >> 2)];
      s += dppf<0xB1>(s); s += dppf<0x4E>(s);
      if ((tid & 3) == 0) bst1_64(rs, O_MODG + (64 * a + (tid >> 2)) * 8, v2u{f2u(rbf(s)), tag});
    }
  } else if (a < 92) {
    const int row = lane >> 3, c = lane & 7;
    v4u xv[2][10];
#pragma unroll
    for (int h = 0; h < 2; ++h)
#pragma unroll
      for (int i = 0; i < 10; ++i) xv[h][i] = *(const v4u*)(p.u + (size_t)(64 * (a - 60) + 16 * w + 8 * h + row) * D + 64 * i + 8 * c);
    SB();
#pragma unroll
    for (int h = 0; h < 2; ++h) {
      float s0 = 0.f, s1 = 0.f, q0 = 0.f, q1 = 0.f;
#pragma unroll
      for (int i = 0; i < 10; ++i)
#pragma unroll
        for (int j = 0; j < 4; j += 2) {
          s0 = dot2(xv[h][i][j], ONES, s0); q0 = dot2(xv[h][i][j], xv[h][i][j], q0);
          s1 = dot2(xv[h][i][j + 1], ONES, s1); q1 = dot2(xv[h][i][j + 1], xv[h][i][j + 1], q1);
        }
      float m = s0 + s1, v = q0 + q1;
      m += dppf<0xB1>(m); v += dppf<0xB1>(v); m += dppf<0x4E>(m); v += dppf<0x4E>(v); m += dppf<0x141>(m); v += dppf<0x141>(v);
      m *= (1.0f / D);
      const float r = __builtin_amdgcn_rsqf(fmaxf(v * (1.0f / D) - m * m, 0.f) + 1e-5f);
      if (c == 0) bst1(rs, O_STG + (64 * (a - 60) + 16 * w + 8 * h + row) * 16, v4u{f2u(m), tag, f2u(r), tag});
    }
  } else if (a == 92) {
    if (tid < 192) __hip_atomic_store((GAS unsigned*)(p.ws + O_CNT) + tid * 64, 0u, RLX_AGENT);
    DRAIN();
    __syncthreads();
    if (tid == 0) bst1_64(rs, O_INIT, v2u{0u, tag});
  }
  STAMP(1);
  if (a < 54) {
    float* c1L = (float*)(smem + 61440);
    u16* sbL = (u16*)(smem + 61440 + 2560);
    float* red = (float*)(smem + 57344);
    unsigned char* stg = smem + 16384;
    const int fp = tid & 15, rgp = tid >> 4;
    unsigned tA[5][4], tB[5][4];
#pragma unroll
    for (int i = 0; i < 5; ++i)
#pragma unroll
      for (int j = 0; j < 4; ++j) { tA[i][j] = plo(wv[i][2 * j], wv[i][2 * j + 1]); tB[i][j] = phi(wv[i][2 * j], wv[i][2 * j + 1]); PIN(tA[i][j]); PIN(tB[i][j]); }
    for (unsigned s = 0; s < SPIN; ++s) {
      u64 v[6];
#pragma unroll
      for (int i = 0; i < 5; ++i) v[i] = __hip_atomic_load((GAS u64*)(p.ws + O_MODG) + 3 * D + 256 * i + tid, RLX_AGENT);
      v[5] = __hip_atomic_load((GAS u64*)(p.ws + O_INIT), RLX_AGENT);
      unsigned bad = (unsigned)(v[5] >> 32) ^ tag;
#pragma unroll
      for (int i = 0; i < 5; ++i) {
        const int e = 256 * i + tid;
        bad |= (unsigned)(v[i] >> 32) ^ tag;
        if (e < D) sbL[e] = (u16)((unsigned)v[i] >> 16); else c1L[e - D] = rbf(1.0f + u2f((unsigned)v[i]));
      }
      if (__all(bad == 0u)) break;
      __builtin_amdgcn_s_sleep(1);
    }
    ORDER();
    __syncthreads();
    float u1a = 0.f, u1b = 0.f, u2a = 0.f, u2b = 0.f;
#pragma unroll
    for (int i = 0; i < 5; ++i) {
      const int e = rgp + 16 * i;
      const f32x4 ca = *(const f32x4*)(c1L + 8 * e), cb = *(const f32x4*)(c1L + 8 * e + 4);
      const v4u sb = *(const v4u*)(sbL + 8 * e);
      unsigned ra[4], rb[4];
#pragma unroll
      for (int j = 0; j < 4; ++j) {
        const f32x2 cc = j == 0 ? f32x2{ca[0], ca[1]} : j == 1 ? f32x2{ca[2], ca[3]} : j == 2 ? f32x2{cb[0], cb[1]} : f32x2{cb[2], cb[3]};
        const f32x2 ya = unp(tA[i][j]) * cc, yb = unp(tB[i][j]) * cc;
        ra[j] = pk_bf16(ya[0], ya[1]); rb[j] = pk_bf16(yb[0], yb[1]);
        u1a = dot2(ra[j], ONES, u1a); u1b = dot2(rb[j], ONES, u1b);
        u2a = dot2(tA[i][j], sb[j], u2a); u2b = dot2(tB[i][j], sb[j], u2b);
      }
      unsigned char* dst = stg + ((fp >> 3) * 20 + (e >> 2)) * 1024 + ((e & 3) * 16 + 2 * (fp & 7)) * 16;
      *(v4u*)dst = v4u{ra[0], ra[1], ra[2], ra[3]};
      *(v4u*)(dst + 16) = v4u{rb[0], rb[1], rb[2], rb[3]};
    }
    *(f32x4*)(red + (rgp * 16 + fp) * 4) = f32x4{u1a, u2a, u1b, u2b};
    __syncthreads();
#pragma unroll
    for (int i = 0; i < 10; ++i) bst1(rs, O_WINF + a * 40960 + i * 4096 + tid * 16, *(const v4u*)(stg + i * 4096 + tid * 16));
    if (tid < 16) {
      f32x4 s = {0.f, 0.f, 0.f, 0.f};
#pragma unroll
      for (int r = 0; r < 16; ++r) s += *(const f32x4*)(red + r * 64 + tid * 4);
      bst1(rs, O_U + a * 256 + tid * 16, __builtin_bit_cast(v4u, s));
    }
    DRAIN();
    __syncthreads();
  } else {
    if (a >= 56) {
      const int wi = (a - 56) * 4 + w;
      if (wi < 135) wout_unit(p, rs, smem + 65536 + w * 8192, wi, lane);
    }
    for (unsigned s = 0; s < SPIN; ++s) {
      const u64 v = __hip_atomic_load((GAS u64*)(p.ws + O_INIT), RLX_AGENT);
      if (__all((unsigned)(v >> 32) == tag)) break;
      __builtin_amdgcn_s_sleep(1);
    }
    ORDER();
    DRAIN();
    __syncthreads();
  }
  if (a < 56) arrive(p, C_W, tid);
  else arrive(p, C_S, tid);
  STAMP(2);
  {
    const int wi = (a >> 3) * 4 + w;
    const unsigned char* src = (const unsigned char*)p.w_sp + (size_t)((a & 7) >> 1) * (512 * 4096);
    v4u sink = {0u, 0u, 0u, 0u};
#pragma unroll
    for (int i = 0; i < 22; ++i) {
      const int pc = min(wi + 48 * i, 1023) * 2 + (lane >> 5);
      const int row = pc >> 2, rot = ((row >> 6) & 7) * 2;
      sink ^= *(const v4u*)(src + (size_t)row * 4096 + (pc & 3) * 1024 + ((512 + rot * 64 + (lane & 31) * 16) & 1023));
    }
    asm volatile("" :: "v"(sink));
  }
  STAMP(3);
}

template <int CTRL> DEVI float dpp_add(float keep, float send) { return keep + dppf<CTRL>(send); }
DEVI float bfly16(const float (&v)[16], int i16) {
  float a[8], b[4], c[2];
  const bool h8 = i16 & 8, h4 = i16 & 4, h2 = i16 & 2, h1 = i16 & 1;
#pragma unroll
  for (int k = 0; k < 8; ++k) a[k] = dpp_add<0x128>(h8 ? v[k + 8] : v[k], h8 ? v[k] : v[k + 8]);
#pragma unroll
  for (int k = 0; k < 4; ++k) b[k] = dpp_add<0x141>(h4 ? a[k + 4] : a[k], h4 ? a[k] : a[k + 4]);
#pragma unroll
  for (int k = 0; k < 2; ++k) c[k] = dpp_add<0x4E>(h2 ? b[k + 2] : b[k], h2 ? b[k] : b[k + 2]);
  return dpp_add<0xB1>(h1 ? c[1] : c[0], h1 ? c[0] : c[1]);
}
DEVI f32x2 gelu2(f32x2 v) {
  const float c0 = -2.0f * 0.7978845608028654f * 1.4426950408889634f, c1 = c0 * 0.044715f;
  const f32x2 u = v * (v * v * c1 + c0);
  const f32x2 d = f32x2{__builtin_amdgcn_exp2f(u[0]), __builtin_amdgcn_exp2f(u[1])} + 1.0f;
  return v * f32x2{__builtin_amdgcn_rcpf(d[0]), __builtin_amdgcn_rcpf(d[1])};
}
constexpr int TABB = 4352;
constexpr int L_KIMG = 0, L_HIMG = 4 * KIMG_B, L_KSCR = L_HIMG + 4 * HL * 2, L_TAB = L_KSCR + 8 * 16 * RS * 4;
constexpr int L_MISC = 86016, L_WL = L_MISC + 8192, L_TOTAL = L_WL + 65536;
static_assert(L_TAB + 4 * TABB <= L_TOTAL && L_KSCR + 16384 <= L_WL, "lds");
constexpr int OOB = 64 << 20;
constexpr int CA[36] = {3,3,3,3,3,4,4,4,4, 2,2,2,2,2,2,5,5,5, 1,1,1,1,6,6,1,1,1, 0,0,0,0,7,0,0,0,0};
constexpr int CB[36] = {4,3,2,1,0,3,2,1,0, 5,4,3,2,1,0,2,1,0, 6,5,4,3,1,0,2,1,0, 7,6,5,4,0,3,2,1,0};
constexpr int CL[16] = {8+4,3,8+3,8+2,8+1,8+0,4,8+5,2,5,8+6,1,6,8+7,0,7};

DEVI void dma16(rsrc_t r, int voff, int soff, unsigned ldsaddr) {
  asm volatile("s_mov_b32 m0, %2\n\ts_nop 0\n\tbuffer_load_dwordx4 %0, %1, %3 offen lds" :: "v"(voff), "s"(r), "s"(ldsaddr), "s"(soff) : "memory");
}

__global__ void __launch_bounds__(256, 1) k_fused(Params p) {
  extern __shared__ __attribute__((aligned(16))) unsigned char smem[];
  const int tid = threadIdx.x, lane = tid & 63, w = __builtin_amdgcn_readfirstlane(tid >> 6);
  const int i16 = lane & 15, g = lane >> 4, l16 = lane * 16;
  const int bx = blockIdx.x & 7, bi = blockIdx.x >> 3;
  const rsrc_t rs = __builtin_amdgcn_make_buffer_rsrc((void*)p.ws, 0, O_END, 0x00020000);
  const rsrc_t rw = __builtin_amdgcn_make_buffer_rsrc((void*)p.w_sp, 0, T * T * 2, 0x00020000);
  u64 ts_[20];
  STAMP(0);
  const unsigned tag = __hip_atomic_load((GAS unsigned*)(p.ws + O_EPOCH), RLX_AGENT) + 1u;
  const unsigned lds0 = (unsigned)(size_t)(LAS unsigned char*)smem;
  {
    u64 pc; unsigned sz;
    asm volatile("s_getpc_b64 %0\n.Lkf_beg:\n\ts_mov_b32 %1, .Lkf_end-.Lkf_beg" : "=s"(pc), "=s"(sz));
    const rsrc_t rc = __builtin_amdgcn_make_buffer_rsrc((void*)(pc & ~15ull), 0, (int)sz, 0x00020000);
    if (w < 3) dma16(rc, l16, (bi * 3 + w) * 1024, lds0 + L_TOTAL - 4096 + w * 1024);
  }
  const bool is_conv = bi < 20;
  const int q4 = bx * 20 + bi;
  const int panel = (bx >> 1) * 8 + (bi >> 2), j = (bx & 1) * 4 + (bi & 3);
  const int b0 = blk0(j);
  const int lb0 = (w < 3) ? 2 * w : 6, lb1 = 2 * w + 1;
  const bool has0 = (w < 3) || (j < 6), has1 = (w < 3);
  const int z2s0 = has0 ? O_WINF + (54 + b0 + lb0) * 20480 : OOB, z2s1 = has1 ? O_WINF + (54 + b0 + lb1) * 20480 : OOB;
  const int z1s0 = has0 ? O_WINF + (b0 + lb0) * 20480 : OOB, z1s1 = has1 ? O_WINF + (b0 + lb1) * 20480 : OOB;
  const int rot = (panel & 7) * 2;
  const int wvo = (8 * (lane >> 4) + ((lane >> 2) & 3)) * 4096 + (((lane & 3) ^ ((lane >> 4) & 3)) * 16);
  const int wso = (64 * panel) * 4096 + 16 * w * 64;
#define WSP_DMA(step, tb, ldsoff) dma16(rw, wvo, wso + (32 * ((tb) >> 1) + 4 * ((tb) & 1)) * 4096 + ((((step) + rot) & 15) * 64), lds0 + (ldsoff) + w * 16384 + ((((step) & 3) * 4 + (tb)) * 1024))
  const int wrd = w * 16384 + (i16 * 4 + (g ^ ((i16 >> 2) & 3))) * 16;
  v4u R[40];
  bool wrdy = false;
  if (is_conv) {
    const int d = 4 * q4 + w;
    unsigned par[6];
    par[0] = *(const unsigned*)(p.a_re + d * NST + 2 * lane); par[1] = *(const unsigned*)(p.a_im + d * NST + 2 * lane);
    par[2] = *(const unsigned*)(p.b_re + d * NST + 2 * lane); par[3] = *(const unsigned*)(p.b_im + d * NST + 2 * lane);
    par[4] = *(const unsigned*)(p.c_re + d * NST + 2 * lane); par[5] = *(const unsigned*)(p.c_im + d * NST + 2 * lane);
    const float ldt = bf2f(p.log_dt[d]);
    const v2u dkv = *(const v2u*)(p.dskip + 4 * q4);
    v2u xa[8];
    v4u st[4][2]; u64 mg;
    const int mo = lane < 12 ? O_MODG + ((lane >> 2) * D + 4 * q4 + (lane & 3)) * 8 : O_INIT;
    auto gather = [&]() {
      mg = __hip_atomic_load((GAS u64*)(p.ws + mo), RLX_AGENT);
#pragma unroll
      for (int i = 0; i < 4; ++i) { st[i][0] = bld1(rs, O_STG + (256 * i + tid) * 32); st[i][1] = bld1(rs, O_STG + (256 * i + tid) * 32 + 16); }
    };
    auto hook = [&](int i) {
      SB();
      if (i < 8) xa[i] = *(const v2u*)(p.u + (size_t)(512 * (i >> 1) + 2 * tid + (i & 1)) * D + 4 * q4);
      else gather();
      SB();
    };
    hook(0); hook(1);
    u16* himg = (u16*)(smem + L_HIMG);
    if (tid < 128) *(v4u*)(himg + (tid >> 5) * HL + (tid & 31) * 8) = v4u{0, 0, 0, 0};
    const float dt = rbf(__builtin_amdgcn_exp2f(ldt * 1.4426950408889634f));
    kgen_wave(par, dt, (unsigned*)(smem + L_KSCR) + (2 * w) * 16 * RS, (unsigned*)(smem + L_KSCR) + (2 * w + 1) * 16 * RS,
              (u16*)(smem + L_TAB + w * TABB), smem + L_KIMG + w * KIMG_B, lane, hook);
    STAMP(1);
    for (unsigned s = 0; s < SPIN; ++s) {
      unsigned bad = (unsigned)(mg >> 32) ^ tag;
#pragma unroll
      for (int i = 0; i < 4; ++i) bad |= (st[i][0][1] ^ tag) | (st[i][0][3] ^ tag) | (st[i][1][1] ^ tag) | (st[i][1][3] ^ tag);
      if (__all(bad == 0u)) break;
      asm volatile("" ::: "memory");
      gather();
    }
    ORDER();
    STAMP(2);
    float sa[4], c1[4], ga[4];
#pragma unroll
    for (int c = 0; c < 4; ++c) {
      sa[c] = u2f(__builtin_amdgcn_readlane((unsigned)mg, c));
      c1[c] = rbf(1.0f + u2f(__builtin_amdgcn_readlane((unsigned)mg, 4 + c)));
      ga[c] = u2f(__builtin_amdgcn_readlane((unsigned)mg, 8 + c));
    }
    unsigned hv[4][4];
#pragma unroll
    for (int i = 0; i < 4; ++i) {
      const int pr = 256 * i + tid;
      const f32x2 m = {u2f(st[i][0][0]), u2f(st[i][1][0])}, r = {u2f(st[i][0][2]), u2f(st[i][1][2])};
      const f32x2 x[4] = {f32x2{bflo(xa[2 * i][0]), bflo(xa[2 * i + 1][0])}, f32x2{bfhi(xa[2 * i][0]), bfhi(xa[2 * i + 1][0])},
                          f32x2{bflo(xa[2 * i][1]), bflo(xa[2 * i + 1][1])}, f32x2{bfhi(xa[2 * i][1]), bfhi(xa[2 * i + 1][1])}};
#pragma unroll
      for (int c = 0; c < 4; ++c) {
        const f32x2 h = (x[c] - m) * r * c1[c] + sa[c];
        hv[i][c] = pk_bf16(h[0], h[1]);
        *(unsigned*)(himg + c * HL + 256 + 2 * pr) = hv[i][c];
      }
    }
    unsigned ab[8];
#pragma unroll
    for (int a = 0; a < 8; ++a) { ab[a] = lds0 + L_KIMG + w * KIMG_B + (i16 & 1) * (KL * 2) + (2032 - (i16 & ~1) - 2 * (i16 & 1) + 8 * g) * 2 - 64 * (8 * a + 7); PIN(ab[a]); }
    unsigned bb = lds0 + L_HIMG + (w * HL + 256 - 16 + 16 * i16 + 8 * g) * 2 - 64 * 7; PIN(bb);
    __syncthreads();
    STAMP(3);
    f32x4 acc[8];
#pragma unroll
    for (int c = 0; c < 8; ++c) acc[c] = f32x4{0.f, 0.f, 0.f, 0.f};
    v4u O[2][16];
#define CLOAD1(buf, r, k) { if constexpr (CL[k] < 8) { const LAS unsigned* ap = (const LAS unsigned*)(size_t)ab[CL[k] & 7] + 16 * (7 - (r)); O[buf][CL[k]] = v4u{ap[0], ap[1], ap[2], ap[3]}; } \
                            else O[buf][CL[k]] = *((const LAS v4u*)(size_t)bb + 4 * (8 * (CL[k] - 8) + 7 - (r))); }
    sfor<16>(LAM { constexpr int k = ic_; CLOAD1(0, 0, k); });
    SB();
    unsigned cwv = 0;
    int ws0 = OOB, ws1 = OOB;
    sfor<8 * 36>(LAM {
      constexpr int r = ic_ / 36, m = ic_ % 36;
      if constexpr (m == 0) TS(r);
      if constexpr (r == 6 && m == 0) { wrdy = __all(cwv >= 7u); ORDER(); ws0 = wrdy ? z2s0 : OOB; ws1 = wrdy ? z2s1 : OOB; }
      acc[CA[m] + CB[m]] = mfma_bf(O[r & 1][CA[m]], O[r & 1][8 + CB[m]], acc[CA[m] + CB[m]]);
      if constexpr (r < 7 && (m & 1) == 0 && m < 32) CLOAD1((r + 1) & 1, r + 1, m >> 1);
      if constexpr ((m & 3) == 1) {
        constexpr int v = m >> 2;
        if constexpr (r < 4 && (v & 1) == 0 && v < 8) WSP_DMA(r, v >> 1, L_WL);
        if constexpr (r == 5 && v == 2) cwv = cnt_load(p, C_W, lane);
        if constexpr (r >= 6) { constexpr int q = 9 * (r - 6) + v; R[q] = bld1(rs, l16, ((q & 1) ? ws1 : ws0) + (q >> 1) * 1024); }
      }
      SB();
    });
    TS(8); TSDUMP(0, 9);
    u16* ybuf = (u16*)(smem + L_KSCR);
#pragma unroll
    for (int c = 0; c < 8; ++c)
      *(v2u*)(ybuf + w * T + 256 * c + 16 * i16 + 4 * g) = v2u{pk_bf16(acc[c][0], acc[c][1]), pk_bf16(acc[c][2], acc[c][3])};
    __syncthreads();
    STAMP(4);
    const float dk[4] = {bflo(dkv[0]), bfhi(dkv[0]), bflo(dkv[1]), bfhi(dkv[1])};
#pragma unroll
    for (int i = 0; i < 4; ++i) {
      const int pr = 256 * i + tid;
      const f32x2 x[4] = {f32x2{bflo(xa[2 * i][0]), bflo(xa[2 * i + 1][0])}, f32x2{bfhi(xa[2 * i][0]), bfhi(xa[2 * i + 1][0])},
                          f32x2{bflo(xa[2 * i][1]), bflo(xa[2 * i + 1][1])}, f32x2{bfhi(xa[2 * i][1]), bfhi(xa[2 * i + 1][1])}};
      f32x2 o[4];
#pragma unroll
      for (int c = 0; c < 4; ++c) o[c] = x[c] + (unp(*(const unsigned*)(ybuf + c * T + 2 * pr)) + unp(hv[i][c]) * dk[c]) * ga[c];
      bst1(rs, O_X1G + (q4 * T + 2 * pr) * 8, v4u{pk_bf16(o[0][0], o[1][0]), pk_bf16(o[2][0], o[3][0]), pk_bf16(o[0][1], o[1][1]), pk_bf16(o[2][1], o[3][1])});
    }
    SB();
    STAMP(5);
#pragma unroll
    for (int q = 18; q < 30; ++q) R[q] = bld1(rs, l16, ((q & 1) ? ws1 : ws0) + (q >> 1) * 1024);
    SB();
    asm volatile("s_waitcnt vmcnt(12)" ::: "memory");
    __syncthreads();
    arrive(p, C_X1, tid);
    SB();
#pragma unroll
    for (int q = 30; q < 40; ++q) R[q] = bld1(rs, l16, ((q & 1) ? ws1 : ws0) + (q >> 1) * 1024);
  } else {
    aux_role(p, rs, smem, (bi - 20) * 8 + bx, tag);
#pragma unroll
    for (int q = 0; q < 16; ++q) WSP_DMA(q >> 2, q & 3, L_WL);
  }
  if (!wrdy) {
    cnt_wait(p, C_W, 7u, lane, 0u);
#pragma unroll
    for (int q = 0; q < 40; ++q) R[q] = bld1(rs, l16, ((q & 1) ? z2s1 : z2s0) + (q >> 1) * 1024);
  }
  STAMP(6);
  unsigned char* xs = smem;
  float* sxL = (float*)(smem + L_MISC);
  float* st1 = (float*)(smem + L_MISC + 512) + w * 128;
  float* gsum = (float*)(smem + L_MISC + 2560);
  float* st2 = (float*)(smem + L_MISC + 4608) + w * 128;
  const v2u uz0 = __builtin_amdgcn_raw_buffer_load_b64(rs, i16 * 8, has0 ? O_U + (FH + 16 * (b0 + lb0)) * 8 : OOB, AUX_SC1);
  const v2u uz1 = __builtin_amdgcn_raw_buffer_load_b64(rs, i16 * 8, has1 ? O_U + (FH + 16 * (b0 + lb1)) * 8 : OOB, AUX_SC1);
  if (w == 3) {
#pragma unroll
    for (int ks = 0; ks < 20; ++ks) R[2 * ks + 1] = v4u{0x3f803f80u, 0x3f803f80u, 0x3f803f80u, 0x3f803f80u};
  }
  if (w == 0) cnt_wait(p, C_X1, 20u, lane, 0u);
  __syncthreads();
  STAMP(7);
  v4u X[20];
  {
    const int xoff = (8 * (lane >> 5) * T + 2 * (lane & 31)) * 8;
    const int xso = O_X1G + (2 * w * T + 64 * panel) * 8;
#pragma unroll
    for (int i = 0; i < 10; ++i) { X[2 * i] = bld1(rs, xoff, xso + i * (16 * T * 8)); X[2 * i + 1] = bld1(rs, xoff, xso + i * (16 * T * 8) + T * 8); }
  }
  SB();
  int xw;
  {
    const int t = 2 * (lane & 31);
    const int tp = (2 * (t >> 5) + ((t >> 2) & 1)) * 16 + 4 * ((t >> 3) & 3) + (t & 3);
    xw = (((lane >> 5) * 4 + w) * 64 + tp) * 16;
  }
  const int aoff = (g * 64 + i16) * 16;
  f32x4 a0[4], a1[4], gram = {0.f, 0.f, 0.f, 0.f};
#pragma unroll
  for (int i = 0; i < 4; ++i) { a0[i] = f32x4{0.f, 0.f, 0.f, 0.f}; a1[i] = a0[i]; }
#define XW(i, h) *(v4u*)(xs + (i) * 8192 + xw + 16 * (h)) = v4u{X[2 * (i)][2 * (h)], X[2 * (i)][2 * (h) + 1], X[2 * (i) + 1][2 * (h)], X[2 * (i) + 1][2 * (h) + 1]}
#define LDA(ks, i) (*(const v4u*)(xs + (ks) * 4096 + aoff + ((i) < 4 ? (i) : w) * 256))
  XW(0, 0); XW(0, 1); XW(1, 0); XW(1, 1);
  __syncthreads();
  STAMP(8);
  v4u WR[32];
  {
    v4u A[2][5];
#pragma unroll
    for (int i = 0; i < 5; ++i) A[0][i] = LDA(0, i);
    SB();
    sfor<20 * 9>(LAM {
      constexpr int ks = ic_ / 9, m = ic_ % 9;
      if constexpr (m == 0 && (ks & 1) == 0) TS(ks >> 1);
      if constexpr (m == 0 && (ks & 3) == 3 && ks < 19) { __syncthreads(); SB(); }
      if constexpr (m == 8) gram = mfma_bf(A[ks & 1][4], A[ks & 1][4], gram);
      else if constexpr (m & 1) a1[m >> 1] = mfma_bf(A[ks & 1][m >> 1], R[2 * ks + 1], a1[m >> 1]);
      else a0[m >> 1] = mfma_bf(A[ks & 1][m >> 1], R[2 * ks], a0[m >> 1]);
      if constexpr (m < 5 && ks < 19) A[(ks + 1) & 1][m] = LDA(ks + 1, m);
      if constexpr (m == 5 && ks > 0) R[2 * ks - 2] = bld1(rs, l16, z1s0 + (ks - 1) * 1024);
      if constexpr (m == 8 && ks > 0) R[2 * ks - 1] = bld1(rs, l16, z1s1 + (ks - 1) * 1024);
      if constexpr ((m == 6 || m == 7) && (ks & 3) < 2 && ks < 16) XW(2 * (ks >> 2) + 2 + (ks & 3), m - 6);
      if constexpr ((m == 6 || m == 7) && ks >= 14) { constexpr int q = 2 * (ks - 14) + (m - 6); WR[q] = *(const v4u*)(smem + L_WL + wrd + q * 1024); }
      if constexpr (ks == 19 && m < 4) WR[12 + m] = *(const v4u*)(smem + L_WL + wrd + (12 + m) * 1024);
      SB();
    });
    TS(10); TSDUMP(16, 11);
    R[38] = bld1(rs, l16, z1s0 + 19 * 1024);
    R[39] = bld1(rs, l16, z1s1 + 19 * 1024);
  }
  if (w == 3) {
    if (i16 == 0) {
#pragma unroll
      for (int i = 0; i < 4; ++i) *(f32x4*)(sxL + i * 16 + 4 * g) = a1[i];
    }
#pragma unroll
    for (int i = 0; i < 4; ++i) a1[i] = f32x4{0.f, 0.f, 0.f, 0.f};
  }
  if ((i16 >> 2) == g) sxL[64 + w * 16 + i16] = (i16 & 3) == 0 ? gram[0] : (i16 & 3) == 1 ? gram[1] : (i16 & 3) == 2 ? gram[2] : gram[3];
  __syncthreads();
  STAMP(9);
  {
    const float m = sxL[lane] * (1.0f / D);
    const float var = fmaxf(sxL[64 + lane] * (1.0f / D) - m * m, 0.f);
    const float r = __builtin_amdgcn_rsqf(var + 1e-5f);
    st1[lane] = r; st1[64 + lane] = r * m;
  }
  v4u A1[2][4];
  {
    const float u0x = u2f(uz0[0]), u0y = u2f(uz0[1]), u1x = u2f(uz1[0]), u1y = u2f(uz1[1]);
    float s1[16], s2[16];
#pragma unroll
    for (int i = 0; i < 4; ++i) {
      const f32x4 rr = *(const f32x4*)(st1 + i * 16 + 4 * g), rm = *(const f32x4*)(st1 + 64 + i * 16 + 4 * g);
#pragma unroll
      for (int h = 0; h < 2; ++h) {
        const f32x2 r2 = {rr[2 * h], rr[2 * h + 1]}, m2 = {rm[2 * h], rm[2 * h + 1]};
        const f32x2 v0 = gelu2(f32x2{a0[i][2 * h], a0[i][2 * h + 1]} * r2 + (u0y - m2 * u0x));
        const f32x2 v1 = gelu2(f32x2{a1[i][2 * h], a1[i][2 * h + 1]} * r2 + (u1y - m2 * u1x));
        a0[i][2 * h] = v0[0]; a0[i][2 * h + 1] = v0[1]; a1[i][2 * h] = v1[0]; a1[i][2 * h + 1] = v1[1];
        const f32x2 q1 = v0 + v1, q2 = v0 * v0 + v1 * v1;
        s1[4 * i + 2 * h] = q1[0]; s1[4 * i + 2 * h + 1] = q1[1]; s2[4 * i + 2 * h] = q2[0]; s2[4 * i + 2 * h + 1] = q2[1];
      }
    }
#pragma unroll
    for (int i = 0; i < 4; ++i) A1[0][i] = LDA(0, i);
    const float t1 = bfly16(s1, i16), t2 = bfly16(s2, i16);
    const int tp = (i16 >> 2) * 16 + 4 * g + (i16 & 3);
    bst1(rs, O_ZST + (((panel * 4 + w) * 8 + j) * 64 + tp) * 16, v4u{f2u(t1), tag, f2u(t2), tag});
  }
  SB();
  STAMP(10);
  f32x4 c0[4], c1[4];
#pragma unroll
  for (int i = 0; i < 4; ++i) { c0[i] = f32x4{0.f, 0.f, 0.f, 0.f}; c1[i] = c0[i]; }
#define Z1M(ks, m) { if constexpr ((m) & 1) c1[(m) >> 1] = mfma_bf(R[2 * (ks) + 1], A1[(ks) & 1][(m) >> 1], c1[(m) >> 1]); else c0[(m) >> 1] = mfma_bf(R[2 * (ks)], A1[(ks) & 1][(m) >> 1], c0[(m) >> 1]); \
    if constexpr ((m) < 4 && (ks) < 19) A1[((ks) + 1) & 1][m] = LDA((ks) + 1, m); }
  constexpr int KA = 10;
  v4u zs[8];
  const int zsoff = O_ZST + ((panel * 4 + w) * 8 * 64) * 16;
  sfor<KA * 8>(LAM {
    constexpr int ks = ic_ / 8, m = ic_ % 8;
    Z1M(ks, m);
    if constexpr (m >= 4 && ks < 4) WSP_DMA(4 + ks, m - 4, L_WL);
    if constexpr (m >= 4 && (ks == KA - 3 || ks == KA - 2)) { constexpr int jj = 4 * (ks - (KA - 3)) + m - 4; zs[jj] = bld1(rs, l16, zsoff + jj * 1024); }
    SB();
  });
  STAMP(11);
  {
    for (unsigned s = 0; s < SPIN; ++s) {
      unsigned bad = 0;
#pragma unroll
      for (int jj = 0; jj < 8; ++jj) bad |= (zs[jj][1] ^ tag) | (zs[jj][3] ^ tag);
      if (__all(bad == 0u)) break;
      asm volatile("" ::: "memory");
#pragma unroll
      for (int jj = 0; jj < 8; ++jj) zs[jj] = bld1(rs, l16, zsoff + jj * 1024);
    }
    float a = 0.f, b = 0.f;
#pragma unroll
    for (int jj = 0; jj < 8; ++jj) { a += u2f(zs[jj][0]); b += u2f(zs[jj][2]); }
    *(f32x2*)(gsum + (w * 64 + lane) * 2) = f32x2{a, b};
  }
  DRAIN();
  __syncthreads();
  STAMP(12);
  {
    const f32x2 s = (*(const f32x2*)(gsum + lane * 2) + *(const f32x2*)(gsum + 128 + lane * 2)) + (*(const f32x2*)(gsum + 256 + lane * 2) + *(const f32x2*)(gsum + 384 + lane * 2));
    const float m = s[0] * (1.0f / FH);
    const float var = fmaxf(s[1] * (1.0f / FH) - m * m, 0.f);
    const float r = __builtin_amdgcn_rsqf(var + 1e-5f);
    st2[lane] = r; st2[64 + lane] = r * m;
  }
  {
    unsigned o0[4][2], o1[4][2];
#pragma unroll
    for (int i = 0; i < 4; ++i) {
      const f32x4 rr = *(const f32x4*)(st2 + i * 16 + 4 * g), rm = *(const f32x4*)(st2 + 64 + i * 16 + 4 * g);
      const f32x4 n0 = a0[i] * rr - rm, n1 = a1[i] * rr - rm;
      o0[i][0] = pk_bf16(n0[0], n0[1]); o0[i][1] = pk_bf16(n0[2], n0[3]);
      o1[i][0] = pk_bf16(n1[0], n1[1]); o1[i][1] = pk_bf16(n1[2], n1[3]);
    }
    const int zo0 = has0 ? O_Z2N + (2 * panel) * 1024 + (b0 + lb0) * 65536 + l16 : OOB, zo1 = has1 ? O_Z2N + (2 * panel) * 1024 + (b0 + lb1) * 65536 + l16 : OOB;
#pragma unroll
    for (int pp = 0; pp < 2; ++pp) {
      bst1(rs, zo0 + pp * 1024, v4u{o0[2 * pp][0], o0[2 * pp][1], o0[2 * pp + 1][0], o0[2 * pp + 1][1]});
      bst1(rs, zo1 + pp * 1024, v4u{o1[2 * pp][0], o1[2 * pp][1], o1[2 * pp + 1][0], o1[2 * pp + 1][1]});
    }
  }
  SB();
  sfor<(20 - KA) * 8>(LAM {
    constexpr int ks = KA + ic_ / 8, m = ic_ % 8;
    Z1M(ks, m);
    if constexpr (m >= 4 && ks < KA + 4) { constexpr int q = 4 * (ks - KA) + m - 4; WR[16 + q] = *(const v4u*)(smem + L_WL + wrd + q * 1024); }
    if constexpr (ks == KA + 5 && m == 3) asm volatile("s_waitcnt lgkmcnt(0)" ::: "memory");
    if constexpr (m >= 4 && ks >= KA + 5 && ks < KA + 9) WSP_DMA(8 + ks - (KA + 5), m - 4, L_WL);
    SB();
  });
  asm volatile("s_waitcnt vmcnt(16)" ::: "memory");
  __syncthreads();
  GAS unsigned* flags = (GAS unsigned*)(p.ws + O_FLAG);
  if (tid == 0) __hip_atomic_store(flags + j * 32 + panel, tag, RLX_AGENT);
  STAMP(13);
  const int fa = has0 ? O_U + 16 * (b0 + lb0) * 8 : OOB, fb = has1 ? O_U + 16 * (b0 + lb1) * 8 : OOB;
  const v4u ua0 = bld1(rs, g * 32, fa), ua1 = bld1(rs, g * 32 + 16, fa), ub0 = bld1(rs, g * 32, fb), ub1 = bld1(rs, g * 32 + 16, fb);
  const int tcol = 64 * panel + 8 * (i16 >> 2) + (i16 & 3);
  unsigned bias_u[4];
#pragma unroll
  for (int tb = 0; tb < 4; ++tb) bias_u[tb] = p.b_sp[tcol + 32 * (tb >> 1) + 4 * (tb & 1)];
  const unsigned csv = cnt_load(p, C_S, lane);
  SB();
#pragma unroll
  for (int q = 0; q < 16; ++q) WSP_DMA(12 + (q >> 2), q & 3, 0);
  SB();
  int zof[7], rof[7];
#pragma unroll
  for (int lf = 0; lf < 7; ++lf) {
    int ab = 2 * w + lf; ab = ab >= 7 ? ab - 7 : ab;
    zof[lf] = (ab == 6 && j >= 6) ? OOB : O_Z2N + (b0 + ab) * 65536 + 16 * w * 1024;
    rof[lf] = ((ab * 3 + ((w - (ab >> 1) - 1) & 3)) * 4) * 1024;
  }
  v4u Z[16][7];
#define Z_LD(i, f) Z[i][f] = bld1(rs, l16, zof[f] + (((i) + rot) & 15) * 1024)
  float sr1[4], sm1[4];
#pragma unroll
  for (int i = 0; i < 4; ++i) { sr1[i] = st1[16 * i + i16]; sm1[i] = st1[64 + 16 * i + i16]; }
  cnt_wait(p, C_S, 5u, lane, csv);
  STAMP(14);
  flag_wait8(flags + j * 32 + 8 * w, tag, lane);
  STAMP(15);
  constexpr int ZD = 3;
#pragma unroll
  for (int i = 0; i < ZD; ++i)
#pragma unroll
    for (int f = 0; f < 7; ++f) Z_LD(i, f);
  SB();
  STAMP(16);
  f32x4 acc[7][4];
#pragma unroll
  for (int f = 0; f < 7; ++f)
#pragma unroll
    for (int t = 0; t < 4; ++t) acc[f][t] = f32x4{0.f, 0.f, 0.f, 0.f};
  const int woso = O_WOUTF + (5 * j) * 27 * 1024;
  v4u WO[35];
  v4u WL[2][4];
  unsigned z1p[2][4][2];
  f32x2 ev0, ev1, eq0, eq1;
  ev0 = ev1 = eq0 = eq1 = f32x2{0.f, 0.f};
#define WO_LD(q) WO[q] = bld1(rs, l16, woso + (((q) % 5) * 27 + min(7 * w + (q) / 5, 26)) * 1024)
  sfor<16 * 28>(LAM {
    constexpr int i = ic_ / 28, f = (ic_ % 28) / 4, t = ic_ % 4;
    if constexpr (f == 0 && t == 0) TS(i);
    if constexpr (i < 8) acc[f][t] = mfma_bf(Z[i][f], WR[i * 4 + t], acc[f][t]);
    else acc[f][t] = mfma_bf(Z[i][f], WL[i & 1][t], acc[f][t]);
    if constexpr (t == 0) { if constexpr (i + ZD < 16) Z_LD(i + ZD, f); else WO_LD(7 * (i + ZD - 16) + f); }
    if constexpr (t == 2 && f < 4 && i >= 7 && i < 15) WL[(i + 1) & 1][f] = *(const v4u*)(smem + (i + 1 < 12 ? L_WL : 0) + wrd + (((i + 1) & 3) * 4 + f) * 1024);
    if constexpr (i < 8 && t > 0 && 3 * f + t - 1 < 16) {
      constexpr int k = 3 * f + t - 1, ii = i >> 1, h = i & 1;
      constexpr float g0 = -2.0f * 0.7978845608028654f * 1.4426950408889634f, g1 = g0 * 0.044715f;
      if constexpr (k == 0) { const v4u qa = h ? ua1 : ua0; ev0 = f32x2{c0[ii][2 * h], c0[ii][2 * h + 1]} * sr1[ii] + (f32x2{u2f(qa[1]), u2f(qa[3])} - f32x2{u2f(qa[0]), u2f(qa[2])} * sm1[ii]); PIN(ev0); }
      if constexpr (k == 1) { const v4u qb = h ? ub1 : ub0; ev1 = f32x2{c1[ii][2 * h], c1[ii][2 * h + 1]} * sr1[ii] + (f32x2{u2f(qb[1]), u2f(qb[3])} - f32x2{u2f(qb[0]), u2f(qb[2])} * sm1[ii]); PIN(ev1); }
      if constexpr (k == 2) eq0 = ev0 * ev0 * g1 + g0;
      if constexpr (k == 3) eq1 = ev1 * ev1 * g1 + g0;
      if constexpr (k == 4) { eq0 *= ev0; eq1 *= ev1; }
      if constexpr (k == 5) eq0[0] = __builtin_amdgcn_exp2f(eq0[0]);
      if constexpr (k == 6) eq0[1] = __builtin_amdgcn_exp2f(eq0[1]);
      if constexpr (k == 7) eq1[0] = __builtin_amdgcn_exp2f(eq1[0]);
      if constexpr (k == 8) eq1[1] = __builtin_amdgcn_exp2f(eq1[1]);
      if constexpr (k == 9) { eq0 += 1.0f; eq1 += 1.0f; }
      if constexpr (k == 10) eq0[0] = __builtin_amdgcn_rcpf(eq0[0]);
      if constexpr (k == 11) eq0[1] = __builtin_amdgcn_rcpf(eq0[1]);
      if constexpr (k == 12) eq1[0] = __builtin_amdgcn_rcpf(eq1[0]);
      if constexpr (k == 13) eq1[1] = __builtin_amdgcn_rcpf(eq1[1]);
      if constexpr (k >= 2 && k <= 13) { PIN(eq0); PIN(eq1); }
      if constexpr (k == 14) { ev0 *= eq0; ev1 *= eq1; PIN(ev0); PIN(ev1); }
      if constexpr (k == 15) { z1p[0][ii][h] = pk_bf16(ev0[0], ev0[1]); z1p[1][ii][h] = pk_bf16(ev1[0], ev1[1]); PIN(z1p[0][ii][h]); PIN(z1p[1][ii][h]); }
    }
    SB();
  });
  TS(16); TSDUMP(32, 17);
  __syncthreads();
  STAMP(17);
#pragma unroll
  for (int lf = 2; lf < 7; ++lf)
#pragma unroll
    for (int t = 0; t < 4; ++t) {
      *(f32x4*)(smem + rof[lf] + t * 1024 + l16) = acc[lf][t];
      const int q = 7 * ZD + 4 * (lf - 2) + t;
      if (q < 35) WO_LD(q < 35 ? q : 0);
    }
  if (w == 3) {
#pragma unroll
    for (int t = 0; t < 4; ++t) *(f32x4*)(smem + rof[1] + t * 1024 + l16) = acc[1][t];
  }
  __syncthreads();
  {
    const unsigned char* r0 = smem + (2 * w * 12) * 1024 + l16;
    const unsigned char* r1 = smem + ((w < 3 ? 2 * w + 1 : 6) * 12) * 1024 + l16;
    const int ks3 = w < 3 ? 3 * j + w : 24 + (j >> 1);
    const int po = O_P + ((4 * panel) * 27 + ks3) * 1024 + l16;
    const int po16 = w < 3 ? po : OOB, po8 = (w == 3 && j < 6) ? po + (j & 1) * 8 : OOB;
#pragma unroll
    for (int t = 0; t < 4; ++t) {
      const float bias = bf2f(bias_u[t]);
      f32x4 o0 = acc[0][t] + bias, o1 = acc[1][t] + bias;
#pragma unroll
      for (int s = 0; s < 3; ++s) { o0 += *(const f32x4*)(r0 + (s * 4 + t) * 1024); o1 += *(const f32x4*)(r1 + (s * 4 + t) * 1024); }
      const f32x2 pa = unp(z1p[0][t][0]) * f32x2{o0[0], o0[1]}, pb = unp(z1p[0][t][1]) * f32x2{o0[2], o0[3]};
      const f32x2 pc = unp(z1p[1][t][0]) * f32x2{o1[0], o1[1]}, pd = unp(z1p[1][t][1]) * f32x2{o1[2], o1[3]};
      const unsigned q0 = pk_bf16(pa[0], pa[1]), q1 = pk_bf16(pb[0], pb[1]), q2 = pk_bf16(pc[0], pc[1]), q3 = pk_bf16(pd[0], pd[1]);
      if (w < 3) bst1(rs, po16 + t * 27 * 1024, v4u{q0, q1, q2, q3});
      else bst1_64(rs, po8 + t * 27 * 1024, v2u{q0, q1});
    }
  }
  STAMP(18);
  DRAIN();
  __syncthreads();
  if (tid == 0) __hip_atomic_store(flags + 256 + panel * 32 + j, tag, RLX_AGENT);
  STAMP(19);
  const int trow = tcol + 32 * (w >> 1) + 4 * (w & 1);
  v2u x1v[5]; v4u gb0[5], gb1[5];
#pragma unroll
  for (int db = 0; db < 5; ++db) {
    const int d = 80 * j + 16 * db + 4 * g;
    x1v[db] = __builtin_amdgcn_raw_buffer_load_b64(rs, O_X1G + ((d >> 2) * T + trow) * 8, 0, AUX_SC1);
    gb0[db] = bld1(rs, O_MODG + (5 * D + d) * 8);
    gb1[db] = bld1(rs, O_MODG + (5 * D + d) * 8 + 16);
  }
  int pof[4], eof[4];
#pragma unroll
  for (int lt = 0; lt < 4; ++lt) {
    const int tb = (w + lt) & 3;
    pof[lt] = O_P + ((4 * panel + tb) * 27 + 7 * w) * 1024;
    eof[lt] = ((tb * 3 + ((w - tb - 1) & 3)) * 5) * 1024;
  }
  const int k6 = w < 3 ? 6 * 1024 : OOB;
  flag_wait8(flags + 256 + panel * 32, tag, lane);
  STAMP(20);
  v4u P[7][4];
#define P_LD(q) P[(q) >> 2][(q) & 3] = bld1(rs, l16, pof[(q) & 3] + (((q) >> 2) < 6 ? ((q) >> 2) * 1024 : k6))
  constexpr int PDN = 12;
#pragma unroll
  for (int q = 0; q < PDN; ++q) P_LD(q);
  SB();
  f32x4 e[5][4];
#pragma unroll
  for (int d = 0; d < 5; ++d)
#pragma unroll
    for (int t = 0; t < 4; ++t) e[d][t] = f32x4{0.f, 0.f, 0.f, 0.f};
  sfor<28 * 5>(LAM {
    constexpr int q = ic_ / 5, d = ic_ % 5;
    e[d][q & 3] = mfma_bf(WO[5 * (q >> 2) + d], P[q >> 2][q & 3], e[d][q & 3]);
    if constexpr (d == 0 && q + PDN < 28) P_LD(q + PDN);
    SB();
  });
  STAMP(21);
#pragma unroll
  for (int lt = 1; lt < 4; ++lt)
#pragma unroll
    for (int d = 0; d < 5; ++d) *(f32x4*)(smem + eof[lt] + d * 1024 + l16) = e[d][lt];
  __syncthreads();
  u16* orow = p.out + (size_t)trow * D + 80 * j + 4 * g;
#pragma unroll
  for (int d = 0; d < 5; ++d) {
    f32x4 eo = e[d][0];
#pragma unroll
    for (int s = 0; s < 3; ++s) eo += *(const f32x4*)(smem + ((w * 3 + s) * 5 + d) * 1024 + l16);
    const float r0 = bflo(x1v[d][0]) + u2f(gb0[d][0]) * eo[0], r1 = bfhi(x1v[d][0]) + u2f(gb0[d][2]) * eo[1];
    const float r2 = bflo(x1v[d][1]) + u2f(gb1[d][0]) * eo[2], r3 = bfhi(x1v[d][1]) + u2f(gb1[d][2]) * eo[3];
    *(v2u*)(orow + 16 * d) = v2u{pk_bf16(r0, r1), pk_bf16(r2, r3)};
  }
  if (blockIdx.x == 0 && tid == 0) __hip_atomic_store((GAS unsigned*)(p.ws + O_EPOCH), tag, RLX_AGENT);
  STAMP(22);
  asm volatile(".Lkf_end:");
}

extern "C" void kernel_launch(void* const* d_in, const int* in_sizes, int n_in, void* d_out, int out_size, void* d_ws,
                              size_t ws_size, hipStream_t stream) {
  Params p;
  p.u = (const u16*)d_in[0]; p.a_re = (const u16*)d_in[1]; p.a_im = (const u16*)d_in[2];
  p.b_re = (const u16*)d_in[3]; p.b_im = (const u16*)d_in[4]; p.c_re = (const u16*)d_in[5]; p.c_im = (const u16*)d_in[6];
  p.log_dt = (const u16*)d_in[7]; p.dskip = (const u16*)d_in[8];
  p.w_in = (const u16*)d_in[10]; p.w_sp = (const u16*)d_in[11]; p.b_sp = (const u16*)d_in[12]; p.w_out = (const u16*)d_in[13];
  p.cvec = (const u16*)d_in[14]; p.w_mod = (const u16*)d_in[15];
  p.out = (u16*)d_out; p.ws = (unsigned char*)d_ws;
  (void)hipFuncSetAttribute((const void*)k_fused, hipFuncAttributeMaxDynamicSharedMemorySize, L_TOTAL);
  k_fused<<<256, 256, L_TOTAL, stream>>>(p);
}
```

```cpp
#include <hip/hip_runtime.h>
#include <hip/hip_bf16.h>
#include <stdint.h>
#include <utility>

typedef unsigned v4u __attribute__((ext_vector_type(4)));
typedef unsigned v2u __attribute__((ext_vector_type(2)));
typedef float f32x4 __attribute__((ext_vector_type(4)));
typedef float f32x2 __attribute__((ext_vector_type(2)));
typedef short bf16x8 __attribute__((ext_vector_type(8)));
typedef _Float16 f16x8 __attribute__((ext_vector_type(8)));
typedef _Float16 f16x2 __attribute__((ext_vector_type(2)));
typedef __bf16 bf16x2_t __attribute__((ext_vector_type(2)));
typedef unsigned short u16;
typedef unsigned long long u64;
#define GAS __attribute__((address_space(1)))
#define LAS __attribute__((address_space(3)))
#define RLX_AGENT __ATOMIC_RELAXED, __HIP_MEMORY_SCOPE_AGENT
#define DEVI __device__ __forceinline__
#define SB() __builtin_amdgcn_sched_barrier(0)
template <int... I, class F> __device__ __forceinline__ void sfor_impl(std::integer_sequence<int, I...>, F&& f) { (f(std::integral_constant<int, I>{}), ...); }
template <int N, class F> __device__ __forceinline__ void sfor(F&& f) { sfor_impl(std::make_integer_sequence<int, N>{}, f); }
#define LAM [&](auto ic_) __attribute__((always_inline))
#define DRAIN() asm volatile("s_waitcnt vmcnt(0)" ::: "memory")

constexpr int T = 2048, D = 640, NST = 128, FH = 864, F2 = 1728;
constexpr int KL = 2080, KIMG_B = 2 * KL * 2, HL = 2304;
constexpr unsigned SPIN = 1u << 15;
constexpr int AUX_SC1 = 16, AUX_NT = 2;
constexpr int O_EPOCH = 0;
constexpr int O_INIT = 256;
constexpr int O_CNT = 4096;
constexpr int O_FLAG = O_CNT + 2 * 64 * 256;
constexpr int O_MODG = O_FLAG + 8192;
constexpr int O_STG = O_MODG + 3840 * 8 + 2048;
constexpr int O_U = O_STG + 2048 * 16;
constexpr int O_ZST = O_U + 16384;
constexpr int O_X1G = O_ZST + 32 * 32 * 64 * 16;
constexpr int O_WINF = O_X1G + T * D * 2;
constexpr int O_WOUTF = O_WINF + 108 * 20 * 1024;
constexpr int O_Z2N = O_WOUTF + 40 * 27 * 1024;
constexpr int O_P = O_Z2N + 54 * 64 * 1024;
constexpr int O_END = O_P + 128 * 27 * 1024;
constexpr int C_X1 = 0, C_A = 1;

constexpr size_t O_STAMP = 200u << 20;
#define TS(k) do {} while (0)
#define TSDUMP(base, n) do {} while (0)
#define STAMP(idx) do {} while (0)

DEVI float u2f(unsigned u) { return __builtin_bit_cast(float, u); }
DEVI unsigned f2u(float f) { return __builtin_bit_cast(unsigned, f); }
DEVI float bf2f(unsigned b) { return u2f(b << 16); }
DEVI float bflo(unsigned u) { return u2f(u << 16); }
DEVI float bfhi(unsigned u) { return u2f(u & 0xffff0000u); }
DEVI unsigned pk_bf16(float a, float b) {
  f32x2 v = {a, b};
  bf16x2_t r = __builtin_convertvector(v, bf16x2_t);
  return __builtin_bit_cast(unsigned, r);
}
DEVI float rbf(float a) { return bflo(pk_bf16(a, 0.f)); }
DEVI unsigned pk_f16(float a, float b) {
  f32x2 v = {a, b};
  f16x2 r = __builtin_convertvector(v, f16x2);
  return __builtin_bit_cast(unsigned, r);
}
DEVI f32x4 mfma_bf(v4u a, v4u b, f32x4 c) {
  return __builtin_amdgcn_mfma_f32_16x16x32_bf16(__builtin_bit_cast(bf16x8, a), __builtin_bit_cast(bf16x8, b), c, 0, 0, 0);
}
DEVI f32x4 mfma_h(v4u a, v4u b, f32x4 c) {
  return __builtin_amdgcn_mfma_f32_16x16x32_f16(__builtin_bit_cast(f16x8, a), __builtin_bit_cast(f16x8, b), c, 0, 0, 0);
}
typedef __amdgpu_buffer_rsrc_t rsrc_t;
DEVI v4u bld1(rsrc_t rs, int voff, int soff = 0) { return __builtin_amdgcn_raw_buffer_load_b128(rs, voff, soff, AUX_SC1); }
DEVI void bst1(rsrc_t rs, int voff, v4u v) { __builtin_amdgcn_raw_buffer_store_b128(v, rs, voff, 0, AUX_SC1); }
DEVI void bst1_64(rsrc_t rs, int voff, v2u v) { __builtin_amdgcn_raw_buffer_store_b64(v, rs, voff, 0, AUX_SC1); }
template <int CTRL> DEVI float dppf(float x) { return u2f((unsigned)__builtin_amdgcn_update_dpp(0, (int)f2u(x), CTRL, 0xf, 0xf, true)); }
DEVI float dot2(unsigned a, unsigned b, float c) { return __builtin_amdgcn_fdot2_f32_bf16(__builtin_bit_cast(bf16x2_t, a), __builtin_bit_cast(bf16x2_t, b), c, false); }
#define PIN(x) asm volatile("" : "+v"(x))
#define ORDER() __builtin_amdgcn_fence(__ATOMIC_ACQUIRE, "wavefront")

struct Params {
  const u16 *u, *a_re, *a_im, *b_re, *b_im, *c_re, *c_im, *log_dt, *dskip, *w_in, *w_sp, *b_sp, *w_out, *cvec, *w_mod;
  u16* out;
  unsigned char* ws;
};

DEVI int blk0(int j) { return j < 6 ? 7 * j : 42 + 6 * (j - 6); }
DEVI int pair_blk(int ks3, int h) {
  if (ks3 < 24) { const int j = ks3 / 3, w = ks3 - 3 * j; return blk0(j) + 2 * w + h; }
  return 7 * (2 * (ks3 - 24) + h) + 6;
}
DEVI void arrive(const Params& p, int c, int tid) {
  if (tid < 8) __hip_atomic_fetch_add((GAS unsigned*)(p.ws + O_CNT) + ((c * 8 + tid) * 8 + (blockIdx.x & 7)) * 64, 1u, RLX_AGENT);
}
DEVI unsigned cnt_load(const Params& p, int c, int lane) {
  return __hip_atomic_load((GAS unsigned*)(p.ws + O_CNT) + ((c * 8 + ((blockIdx.x >> 3) & 7)) * 8 + (lane & 7)) * 64, RLX_AGENT);
}
template <int SLP> DEVI void cnt_wait(const Params& p, int c, unsigned want, int lane) {
#pragma unroll 1
  for (unsigned s = 0; s < SPIN; ++s) {
    if (__all(cnt_load(p, c, lane) >= want)) break;
    __builtin_amdgcn_s_sleep(SLP);
  }
  ORDER();
}
DEVI void flag_wait8(GAS unsigned* f, unsigned tag, int lane) {
#pragma unroll 1
  for (unsigned s = 0; s < SPIN; ++s) {
    const unsigned v = __hip_atomic_load(f + (lane & 7), RLX_AGENT);
    if (__all(v == tag)) break;
    __builtin_amdgcn_s_sleep(1);
  }
  ORDER();
}

struct C2 { f32x2 re, im; };
DEVI C2 cmul2(C2 a, C2 b) { return C2{a.re * b.re - a.im * b.im, a.re * b.im + a.im * b.re}; }
DEVI C2 cexp2(f32x2 are2, f32x2 arev, float l) {
  const f32x2 e = are2 * l, q = arev * l;
  const f32x2 m = {__builtin_amdgcn_exp2f(e[0]), __builtin_amdgcn_exp2f(e[1])};
  const float p0 = __builtin_amdgcn_fractf(q[0]), p1 = __builtin_amdgcn_fractf(q[1]);
  return C2{m * f32x2{__builtin_amdgcn_cosf(p0), __builtin_amdgcn_cosf(p1)}, m * f32x2{__builtin_amdgcn_sinf(p0), __builtin_amdgcn_sinf(p1)}};
}
constexpr int RS = 132;
DEVI void put_row(unsigned* buf, int row, int lane, C2 v) { *(v2u*)(buf + row * RS + 2 * lane) = v2u{pk_f16(v.re[0], v.im[0]), pk_f16(v.re[1], v.im[1])}; }
DEVI f32x2 unp(unsigned u) { return f32x2{bflo(u), bfhi(u)}; }

template <class HK> DEVI void kgen_wave(unsigned par[6], float dt, unsigned* buf0, unsigned char* img, int lane, HK&& hook) {
  unsigned* buf1 = (unsigned*)img;
  u16* tab = (u16*)buf0;
  const int c = lane & 15, g = lane >> 4;
  const f32x2 are2 = unp(par[0]) * (dt * 1.4426950408889634f), arev = unp(par[1]) * (dt * 0.15915494309189535f), nrev = -arev;
  C2 W = cmul2(C2{unp(par[4]), unp(par[5])}, C2{unp(par[2]) * dt, unp(par[3]) * dt});
  float mx = fmaxf(fmaxf(fabsf(W.re[0]), fabsf(W.re[1])), fmaxf(fabsf(W.im[0]), fabsf(W.im[1])));
  unsigned mu = f2u(mx);
  mu = max(mu, f2u(dppf<0x128>(u2f(mu)))); mu = max(mu, f2u(dppf<0x124>(u2f(mu)))); mu = max(mu, f2u(dppf<0x122>(u2f(mu)))); mu = max(mu, f2u(dppf<0x121>(u2f(mu))));
  const unsigned m0 = __builtin_amdgcn_readlane(mu, 0), m1 = __builtin_amdgcn_readlane(mu, 16), m2 = __builtin_amdgcn_readlane(mu, 32), m3 = __builtin_amdgcn_readlane(mu, 48);
  unsigned eb = (max(max(m0, m1), max(m2, m3)) >> 23) & 0xffu;
  eb = eb < 2u ? 2u : (eb > 250u ? 250u : eb);
  const float sc = u2f((eb + 1u) << 23), isc = u2f((253u - eb) << 23);
  W.re *= isc; W.im *= isc;
  {
    const C2 e8 = cexp2(are2, arev, 8.f);
    C2 W8 = cmul2(W, cexp2(are2, arev, 64.f));
#pragma unroll
    for (int b = 0; b < 8; ++b) {
      put_row(buf0, b, lane, W); put_row(buf0, 8 + b, lane, W8);
      if (b < 7) { W = cmul2(W, e8); W8 = cmul2(W8, e8); }
      if (b == 0) hook(2);
      if (b == 4) hook(3);
    }
  }
  v4u A[8];
#pragma unroll
  for (int ks = 0; ks < 8; ++ks) A[ks] = *(const v4u*)(buf0 + c * RS + 16 * ks + 4 * g);
  C2 er[8];
  er[0] = C2{f32x2{1.f, 1.f}, f32x2{0.f, 0.f}};
  er[1] = cexp2(are2, nrev, 1.f);
  er[2] = cmul2(er[1], er[1]); er[3] = cmul2(er[2], er[1]); er[4] = cmul2(er[2], er[2]);
  er[5] = cmul2(er[4], er[1]); er[6] = cmul2(er[3], er[3]); er[7] = cmul2(er[4], er[3]);
  hook(4);
  const C2 e128 = cexp2(are2, nrev, 128.f);
#pragma unroll
  for (int r = 0; r < 8; ++r) { put_row(buf0, r, lane, er[r]); put_row(buf0, 8 + r, lane, r ? cmul2(e128, er[r]) : e128); }
  f32x4 acc0 = {0.f, 0.f, 0.f, 0.f}, acc1 = acc0, acc2 = acc0;
#pragma unroll
  for (int ks = 0; ks < 8; ++ks) acc0 = mfma_h(A[ks], *(const v4u*)(buf0 + c * RS + 16 * ks + 4 * g), acc0);
  hook(5);
  C2 e = cmul2(e128, e128);
  C2 f = cexp2(are2, nrev, 1024.f);
#pragma unroll
  for (int a = 0; a < 2; ++a) {
#pragma unroll
    for (int r = 0; r < 4; ++r) put_row(buf0, 4 * a + r, lane, r ? cmul2(e, er[2 * r]) : e);
    e = cmul2(e, e128);
  }
  hook(6);
#pragma unroll
  for (int a = 0; a < 4; ++a) {
    put_row(buf0, 8 + 2 * a, lane, e); put_row(buf0, 9 + 2 * a, lane, cmul2(e, er[4])); if (a < 3) e = cmul2(e, e128);
    put_row(buf1, 2 * a, lane, f); f = cmul2(f, e128); put_row(buf1, 2 * a + 1, lane, f); f = cmul2(f, e128);
    if (a == 1) hook(7);
  }
  put_row(buf1, 8, lane, f);
  hook(8);
  {
    v4u B1[8], B2[8];
#pragma unroll
    for (int ks = 0; ks < 8; ++ks) { B1[ks] = *(const v4u*)(buf0 + c * RS + 16 * ks + 4 * g); B2[ks] = *(const v4u*)(buf1 + c * RS + 16 * ks + 4 * g); }
    SB();
#pragma unroll
    for (int ks = 0; ks < 8; ++ks) { acc1 = mfma_h(A[ks], B1[ks], acc1); acc2 = mfma_h(A[ks], B2[ks], acc2); }
  }
  const int cv0 = 128 * (c >> 3) + (c & 7);
  const int cv1 = c < 8 ? 256 + 128 * (c >> 2) + 2 * (c & 3) : 512 + 128 * ((c - 8) >> 1) + 4 * (c & 1);
  const int cv2 = c < 9 ? 1024 + 128 * c : 2048;
#pragma unroll
  for (int r = 0; r < 4; ++r) {
    const int lb = 8 * (4 * g + r);
    const unsigned q01 = pk_bf16(acc0[r] * sc, acc1[r] * sc);
    tab[lb + cv0] = (u16)q01;
    tab[lb + cv1] = (u16)(q01 >> 16);
    tab[lb + cv2 + (c > 8 ? 1 : 0)] = (u16)pk_bf16(acc2[r] * sc, 0.f);
  }
  {
    const u16* t0 = tab + 2048 - 8 * lane;
    const int o = lane < 32 ? 1 : 0;
    const unsigned H0 = t0[0], L0 = t0[-8], H1 = t0[-512], L1 = t0[-520], H2 = t0[-1024], M2 = t0[-1028], L2 = t0[-1032];
    const u16* t3 = t0 - 1536;
    const unsigned v0 = t3[0], v1 = t3[-1 + o], v2 = t3[-2], v3 = t3[-3 - o], v4 = t3[-4], v5 = t3[-5 + o], v6 = t3[-6], v7 = t3[-7 - o], v8 = t3[-8];
    const unsigned k0 = tab[0];
    SB();
    unsigned char* i0 = img + lane * 16;
    unsigned char* i1 = i0 + KL * 2;
    {
      const unsigned X = (lane & 1) ? L0 : H0, HH = H0 | (H0 << 16), LL = L0 | (L0 << 16);
      *(v4u*)i0 = v4u{HH, HH, X | (L0 << 16), LL};
      *(v4u*)i1 = v4u{HH, H0 | (X << 16), LL, LL};
    }
    {
      const unsigned X = (lane & 1) ? L1 : H1, HH = H1 | (H1 << 16), LL = L1 | (L1 << 16);
      *(v4u*)(i0 + 1024) = v4u{HH, HH, X | (L1 << 16), LL};
      *(v4u*)(i1 + 1024) = v4u{HH, H1 | (X << 16), LL, LL};
    }
    {
      const unsigned HH = H2 | (H2 << 16), MM = M2 | (M2 << 16), LL = L2 | (L2 << 16);
      *(v4u*)(i0 + 2048) = v4u{HH, H2 | (M2 << 16), MM, LL};
      *(v4u*)(i1 + 2048) = v4u{HH, MM, M2 | (L2 << 16), LL};
    }
    *(v4u*)(i0 + 3072) = v4u{v0 | (v1 << 16), v2 | (v3 << 16), v4 | (v5 << 16), v6 | (v7 << 16)};
    *(v4u*)(i1 + 3072) = v4u{v1 | (v2 << 16), v3 | (v4 << 16), v5 | (v6 << 16), v7 | (v8 << 16)};
    if (lane < 8) *(v4u*)(img + (lane >> 2) * (KL * 2) + (256 + (lane & 3)) * 16) = v4u{lane == 0 ? k0 : 0u, 0u, 0u, 0u};
  }
}

constexpr int L_KIMG = 0, L_HIMG = 4 * KIMG_B, L_KSCR = L_HIMG + 4 * HL * 2;
constexpr int L_MISC = 86016, L_WL = L_MISC + 8192, L_DUMMY = L_WL + 65536, L_TOTAL = L_DUMMY + 4096;
static_assert(L_TOTAL <= 163840 && L_KSCR + 4 * 16 * RS * 4 <= L_MISC, "lds");
#define XAUX 0
#define ZPOLL 4
constexpr int OOB = 64 << 20;
constexpr int CA[36] = {3,3,3,3,3,4,4,4,4, 2,2,2,2,2,2,5,5,5, 1,1,1,1,6,6,1,1,1, 0,0,0,0,7,0,0,0,0};
constexpr int CB[36] = {4,3,2,1,0,3,2,1,0, 5,4,3,2,1,0,2,1,0, 6,5,4,3,1,0,2,1,0, 7,6,5,4,0,3,2,1,0};
constexpr int CL[16] = {8+4,3,8+3,8+2,8+1,8+0,4,8+5,2,5,8+6,1,6,8+7,0,7};

DEVI void dma16s(rsrc_t r, int voff, int soff, unsigned ldsaddr) {
  asm volatile("s_mov_b32 m0, %2\n\ts_nop 0\n\tbuffer_load_dwordx4 %0, %1, %3 offen sc1 lds" :: "v"(voff), "s"(r), "s"(ldsaddr), "s"(soff) : "memory");
}
DEVI void dma16(rsrc_t r, int voff, int soff, unsigned ldsaddr) {
  asm volatile("s_mov_b32 m0, %2\n\ts_nop 0\n\tbuffer_load_dwordx4 %0, %1, %3 offen lds" :: "v"(voff), "s"(r), "s"(ldsaddr), "s"(soff) : "memory");
}

DEVI unsigned plo(unsigned a, unsigned b) { return __builtin_amdgcn_perm(b, a, 0x05040100u); }
DEVI unsigned phi(unsigned a, unsigned b) { return __builtin_amdgcn_perm(b, a, 0x07060302u); }
DEVI void wout_unit(const Params& p, rsrc_t rs, unsigned char* lw, int unit, int lane) {
  const int ks = unit / 5, dc = unit - 5 * ks, d = 128 * dc + 2 * lane;
  const int f0 = 16 * pair_blk(ks, 0), f1 = 16 * pair_blk(ks, 1);
  unsigned wv[4][8];
#pragma unroll
  for (int g = 0; g < 4; ++g)
#pragma unroll
    for (int j = 0; j < 8; ++j) wv[g][j] = *(const unsigned*)(p.w_out + (size_t)((j < 4 ? f0 : f1) + 4 * g + (j & 3)) * D + d);
#pragma unroll
  for (int g = 0; g < 4; ++g) {
    unsigned char* dst = lw + (lane >> 3) * 1024 + (g * 16 + 2 * (lane & 7)) * 16;
    *(v4u*)dst = v4u{plo(wv[g][0], wv[g][1]), plo(wv[g][2], wv[g][3]), plo(wv[g][4], wv[g][5]), plo(wv[g][6], wv[g][7])};
    *(v4u*)(dst + 16) = v4u{phi(wv[g][0], wv[g][1]), phi(wv[g][2], wv[g][3]), phi(wv[g][4], wv[g][5]), phi(wv[g][6], wv[g][7])};
  }
#pragma unroll
  for (int i = 0; i < 8; ++i) bst1(rs, O_WOUTF + ((8 * dc + i) * 27 + ks) * 1024 + lane * 16, *(const v4u*)(lw + i * 1024 + lane * 16));
}

DEVI void aux_role(const Params& p, rsrc_t rs, unsigned char* smem, int a, unsigned tag) {
  const int tid = threadIdx.x, lane = tid & 63, w = __builtin_amdgcn_readfirstlane(tid >> 6);
  float* fl = (float*)smem;
  constexpr unsigned ONES = 0x3f803f80u;
  unsigned wv[5][8];
  if (a < 60) {
    const int rg = tid >> 3, cl = tid & 7;
    unsigned* sL = (unsigned*)(smem + 12288);
    v4u mv[20];
    const v2u cv = *(const v2u*)(p.cvec + 4 * min(tid, 159));
#pragma unroll
    for (int i = 0; i < 20; ++i) mv[i] = __builtin_nontemporal_load((const v4u*)(p.w_mod + (size_t)(20 * rg + i) * 3840 + 64 * a + 8 * cl));
    SB();
    if (tid < 160) {
      unsigned sv[4];
#pragma unroll
      for (int i = 0; i < 4; ++i) {
        const float c = (i & 1) ? bfhi(cv[i >> 1]) : bflo(cv[i >> 1]);
        sv[i] = pk_bf16(c * __builtin_amdgcn_rcpf(1.0f + __builtin_amdgcn_exp2f(-1.4426950408889634f * c)), 0.f);
      }
      *(v4u*)(sL + 4 * tid) = v4u{sv[0], sv[1], sv[2], sv[3]};
    }
    __syncthreads();
    v4u s4[5];
#pragma unroll
    for (int i = 0; i < 5; ++i) s4[i] = *(const v4u*)(sL + 20 * rg + 4 * i);
    float acc[8];
#pragma unroll
    for (int j = 0; j < 8; ++j) acc[j] = 0.f;
    const u16* wsrc = p.w_in + (size_t)(8 * (tid >> 4)) * F2 + 32 * min(a, 53) + 2 * (tid & 15);
#pragma unroll
    for (int i = 0; i < 20; ++i) {
      const unsigned slo = s4[i >> 2][i & 3], shi = slo << 16;
#pragma unroll
      for (int j = 0; j < 4; ++j) { acc[2 * j] = dot2(mv[i][j], slo, acc[2 * j]); acc[2 * j + 1] = dot2(mv[i][j], shi, acc[2 * j + 1]); }
      wv[i >> 2][2 * (i & 3)] = *(const unsigned*)(wsrc + (size_t)(128 * (i >> 2) + 2 * (i & 3)) * F2);
      wv[i >> 2][2 * (i & 3) + 1] = *(const unsigned*)(wsrc + (size_t)(128 * (i >> 2) + 2 * (i & 3) + 1) * F2);
      SB();
    }
    *(f32x4*)(fl + rg * 68 + cl * 8) = f32x4{acc[0], acc[1], acc[2], acc[3]};
    *(f32x4*)(fl + rg * 68 + cl * 8 + 4) = f32x4{acc[4], acc[5], acc[6], acc[7]};
    __syncthreads();
    {
      float s = 0.f;
#pragma unroll
      for (int r = 0; r < 8; ++r) s += fl[(8 * (tid & 3) + r) * 68 + (tid >> 2)];
      s += dppf<0xB1>(s); s += dppf<0x4E>(s);
      if ((tid & 3) == 0) bst1_64(rs, O_MODG + (64 * a + (tid >> 2)) * 8, v2u{f2u(rbf(s)), tag});
    }
  } else if (a < 92) {
    const int row = lane >> 3, c = lane & 7;
    v4u xv[2][10];
#pragma unroll
    for (int h = 0; h < 2; ++h)
#pragma unroll
      for (int i = 0; i < 10; ++i) xv[h][i] = *(const v4u*)(p.u + (size_t)(64 * (a - 60) + 16 * w + 8 * h + row) * D + 64 * i + 8 * c);
    SB();
#pragma unroll
    for (int h = 0; h < 2; ++h) {
      float s0 = 0.f, s1 = 0.f, q0 = 0.f, q1 = 0.f;
#pragma unroll
      for (int i = 0; i < 10; ++i)
#pragma unroll
        for (int j = 0; j < 4; j += 2) {
          s0 = dot2(xv[h][i][j], ONES, s0); q0 = dot2(xv[h][i][j], xv[h][i][j], q0);
          s1 = dot2(xv[h][i][j + 1], ONES, s1); q1 = dot2(xv[h][i][j + 1], xv[h][i][j + 1], q1);
        }
      float m = s0 + s1, v = q0 + q1;
      m += dppf<0xB1>(m); v += dppf<0xB1>(v); m += dppf<0x4E>(m); v += dppf<0x4E>(v); m += dppf<0x141>(m); v += dppf<0x141>(v);
      m *= (1.0f / D);
      const float r = __builtin_amdgcn_rsqf(fmaxf(v * (1.0f / D) - m * m, 0.f) + 1e-5f);
      if (c == 0) bst1(rs, O_STG + (64 * (a - 60) + 16 * w + 8 * h + row) * 16, v4u{f2u(m), tag, f2u(r), tag});
    }
  } else if (a == 92) {
    if (tid < 128) __hip_atomic_store((GAS unsigned*)(p.ws + O_CNT) + tid * 64, 0u, RLX_AGENT);
    DRAIN();
    __syncthreads();
    if (tid == 0) bst1_64(rs, O_INIT, v2u{0u, tag});
  }
  STAMP(1);
  if (a < 54) {
    float* c1L = (float*)(smem + 61440);
    u16* sbL = (u16*)(smem + 61440 + 2560);
    float* red = (float*)(smem + 57344);
    unsigned char* stg = smem + 16384;
    const int fp = tid & 15, rgp = tid >> 4;
    unsigned tA[5][4], tB[5][4];
#pragma unroll
    for (int i = 0; i < 5; ++i)
#pragma unroll
      for (int j = 0; j < 4; ++j) { tA[i][j] = plo(wv[i][2 * j], wv[i][2 * j + 1]); tB[i][j] = phi(wv[i][2 * j], wv[i][2 * j + 1]); PIN(tA[i][j]); PIN(tB[i][j]); }
#pragma unroll 1
    for (unsigned s = 0; s < SPIN; ++s) {
      u64 v[5];
#pragma unroll
      for (int i = 0; i < 5; ++i) v[i] = __hip_atomic_load((GAS u64*)(p.ws + O_MODG) + 3 * D + 256 * i + tid, RLX_AGENT);
      unsigned bad = 0;
#pragma unroll
      for (int i = 0; i < 5; ++i) {
        const int e = 256 * i + tid;
        bad |= (unsigned)(v[i] >> 32) ^ tag;
        if (e < D) sbL[e] = (u16)((unsigned)v[i] >> 16); else c1L[e - D] = rbf(1.0f + u2f((unsigned)v[i]));
      }
      if (__all(bad == 0u)) break;
      __builtin_amdgcn_s_sleep(1);
    }
    ORDER();
    __syncthreads();
    float u1a = 0.f, u1b = 0.f, u2a = 0.f, u2b = 0.f;
#pragma unroll
    for (int i = 0; i < 5; ++i) {
      const int e = rgp + 16 * i;
      const f32x4 ca = *(const f32x4*)(c1L + 8 * e), cb = *(const f32x4*)(c1L + 8 * e + 4);
      const v4u sb = *(const v4u*)(sbL + 8 * e);
      unsigned ra[4], rb[4];
#pragma unroll
      for (int j = 0; j < 4; ++j) {
        const f32x2 cc = j == 0 ? f32x2{ca[0], ca[1]} : j == 1 ? f32x2{ca[2], ca[3]} : j == 2 ? f32x2{cb[0], cb[1]} : f32x2{cb[2], cb[3]};
        const f32x2 ya = unp(tA[i][j]) * cc, yb = unp(tB[i][j]) * cc;
        ra[j] = pk_bf16(ya[0], ya[1]); rb[j] = pk_bf16(yb[0], yb[1]);
        u1a = dot2(ra[j], ONES, u1a); u1b = dot2(rb[j], ONES, u1b);
        u2a = dot2(tA[i][j], sb[j], u2a); u2b = dot2(tB[i][j], sb[j], u2b);
      }
      unsigned char* dst = stg + ((fp >> 3) * 20 + (e >> 2)) * 1024 + ((e & 3) * 16 + 2 * (fp & 7)) * 16;
      *(v4u*)dst = v4u{ra[0], ra[1], ra[2], ra[3]};
      *(v4u*)(dst + 16) = v4u{rb[0], rb[1], rb[2], rb[3]};
    }
    *(f32x4*)(red + (rgp * 16 + fp) * 4) = f32x4{u1a, u2a, u1b, u2b};
    __syncthreads();
#pragma unroll
    for (int i = 0; i < 10; ++i) bst1(rs, O_WINF + a * 40960 + i * 4096 + tid * 16, *(const v4u*)(stg + i * 4096 + tid * 16));
    if (tid < 16) {
      f32x4 s = {0.f, 0.f, 0.f, 0.f};
#pragma unroll
      for (int r = 0; r < 16; ++r) s += *(const f32x4*)(red + r * 64 + tid * 4);
      bst1(rs, O_U + a * 256 + tid * 16, __builtin_bit_cast(v4u, s));
    }
  } else if (a >= 56) {
    const int wi = (a - 56) * 4 + w;
    if (wi < 135) wout_unit(p, rs, smem + 16384 + w * 8192, wi, lane);
  }
#pragma unroll 1
  for (unsigned s = 0; s < SPIN; ++s) {
    const u64 v = __hip_atomic_load((GAS u64*)(p.ws + O_INIT), RLX_AGENT);
    if (__all((unsigned)(v >> 32) == tag)) break;
    __builtin_amdgcn_s_sleep(1);
  }
  ORDER();
  DRAIN();
  __syncthreads();
  arrive(p, C_A, tid);
  STAMP(2);
}

template <int CTRL> DEVI float dpp_add(float keep, float send) { return keep + dppf<CTRL>(send); }
DEVI float bfly16(const float (&v)[16], int i16) {
  float a[8], b[4], c[2];
  const bool h8 = i16 & 8, h4 = i16 & 4, h2 = i16 & 2, h1 = i16 & 1;
#pragma unroll
  for (int k = 0; k < 8; ++k) a[k] = dpp_add<0x128>(h8 ? v[k + 8] : v[k], h8 ? v[k] : v[k + 8]);
#pragma unroll
  for (int k = 0; k < 4; ++k) b[k] = dpp_add<0x141>(h4 ? a[k + 4] : a[k], h4 ? a[k] : a[k + 4]);
#pragma unroll
  for (int k = 0; k < 2; ++k) c[k] = dpp_add<0x4E>(h2 ? b[k + 2] : b[k], h2 ? b[k] : b[k + 2]);
  return dpp_add<0xB1>(h1 ? c[1] : c[0], h1 ? c[0] : c[1]);
}
DEVI f32x2 gelu2(f32x2 v) {
  const float c0 = -2.0f * 0.7978845608028654f * 1.4426950408889634f, c1 = c0 * 0.044715f;
  const f32x2 u = v * (v * v * c1 + c0);
  const f32x2 d = f32x2{__builtin_amdgcn_exp2f(u[0]), __builtin_amdgcn_exp2f(u[1])} + 1.0f;
  return v * f32x2{__builtin_amdgcn_rcpf(d[0]), __builtin_amdgcn_rcpf(d[1])};
}
__global__ void __launch_bounds__(256, 1) k_fused(Params p) {
  extern __shared__ __attribute__((aligned(16))) unsigned char smem[];
  const int tid = threadIdx.x, lane = tid & 63, w = __builtin_amdgcn_readfirstlane(tid >> 6);
  const int i16 = lane & 15, g = lane >> 4, l16 = lane * 16;
  const int bx = blockIdx.x & 7, bi = blockIdx.x >> 3;
  const rsrc_t rs = __builtin_amdgcn_make_buffer_rsrc((void*)p.ws, 0, O_END, 0x00020000);
  const rsrc_t rw = __builtin_amdgcn_make_buffer_rsrc((void*)p.w_sp, 0, T * T * 2, 0x00020000);
  u64 ts_[20];
  STAMP(0);
  const unsigned tag = __hip_atomic_load((GAS unsigned*)(p.ws + O_EPOCH), RLX_AGENT) + 1u;
  const unsigned lds0 = (unsigned)(size_t)(LAS unsigned char*)smem;
  u64 pc_; unsigned csz_;
  asm volatile("s_getpc_b64 %0\n.Lkf_beg:\n\ts_mov_b32 %1, .Lkf_end-.Lkf_beg" : "=s"(pc_), "=s"(csz_));
  auto code_prefetch = [&]() __attribute__((always_inline)) {
    const rsrc_t rc = __builtin_amdgcn_make_buffer_rsrc((void*)(pc_ & ~15ull), 0, (int)csz_, 0x00020000);
    if (w < 3) dma16(rc, l16, (bi * 3 + w) * 1024, lds0 + L_DUMMY + w * 1024);
  };
  const bool is_conv = bi < 20;
  const int q4 = bx * 20 + bi;
  const int panel = (bx >> 1) * 8 + (bi >> 2), j = (bx & 1) * 4 + (bi & 3);
  const int b0 = blk0(j);
  const int lb0 = (w < 3) ? 2 * w : 6, lb1 = 2 * w + 1;
  const bool has0 = (w < 3) || (j < 6), has1 = (w < 3);
  const int z2s0 = has0 ? O_WINF + (54 + b0 + lb0) * 20480 : OOB, z2s1 = has1 ? O_WINF + (54 + b0 + lb1) * 20480 : OOB;
  const int z1s0 = has0 ? O_WINF + (b0 + lb0) * 20480 : OOB, z1s1 = has1 ? O_WINF + (b0 + lb1) * 20480 : OOB;
  const int rot = ((panel & 7) * 2 + (j & 3) * 4) & 15;
  const int wvo = (8 * (lane >> 4) + ((lane >> 2) & 3)) * 4096 + (((lane & 3) ^ ((lane >> 4) & 3)) * 16);
  const int wso = (64 * panel) * 4096 + 16 * w * 64;
#define WSP_DMA(step, tb, ldsoff) dma16(rw, wvo, wso + (32 * ((tb) >> 1) + 4 * ((tb) & 1)) * 4096 + ((((step) + rot) & 15) * 64), lds0 + (ldsoff) + w * 16384 + ((((step) & 3) * 4 + (tb)) * 1024))
#define WSD_DMA(q) dma16(rw, wvo, wso + (32 * (((q) & 3) >> 1) + 4 * ((q) & 1)) * 4096 + (((12 + ((q) >> 2) + rot) & 15) * 64), lds0 + ((q) * 4 + w) * 1024)
  const int wrd = w * 16384 + (i16 * 4 + (g ^ ((i16 >> 2) & 3))) * 16;
  const int zu0 = has0 ? O_Z2N + (2 * panel) * 1024 + (b0 + lb0) * 65536 : OOB, zu1 = has1 ? O_Z2N + (2 * panel) * 1024 + (b0 + lb1) * 65536 : OOB;
  const int pu = O_P + ((4 * panel) * 27 + (w < 3 ? 3 * j + w : 24 + (j >> 1))) * 1024;
  const int pu16 = w < 3 ? pu : OOB, pu8 = (w == 3 && j < 6) ? pu + (j & 1) * 8 : OOB;
  v4u WR[32];
#define WR_RD(q, dst) WR[dst] = *(const v4u*)(smem + L_WL + wrd + (q) * 1024)
  v4u R[40];
#define R_LD(q, s0, s1) R[q] = bld1(rs, l16, (((q) & 1) ? (s1) : (s0)) + ((q) >> 1) * 1024)
  bool rdy = false;
  if (__builtin_expect(is_conv, 1)) {
    const int d = 4 * q4 + w;
    unsigned par[6];
    par[0] = *(const unsigned*)(p.a_re + d * NST + 2 * lane); par[1] = *(const unsigned*)(p.a_im + d * NST + 2 * lane);
    par[2] = *(const unsigned*)(p.b_re + d * NST + 2 * lane); par[3] = *(const unsigned*)(p.b_im + d * NST + 2 * lane);
    par[4] = *(const unsigned*)(p.c_re + d * NST + 2 * lane); par[5] = *(const unsigned*)(p.c_im + d * NST + 2 * lane);
    const float ldt = bf2f(p.log_dt[d]);
    const v2u dkv = *(const v2u*)(p.dskip + 4 * q4);
    v2u xa[8];
    v4u st[4][2]; u64 mg;
    const int mo = lane < 12 ? O_MODG + ((lane >> 2) * D + 4 * q4 + (lane & 3)) * 8 : O_INIT;
    auto gather = [&]() __attribute__((always_inline)) {
      mg = __hip_atomic_load((GAS u64*)(p.ws + mo), RLX_AGENT);
#pragma unroll
      for (int i = 0; i < 4; ++i) { st[i][0] = bld1(rs, O_STG + (256 * i + tid) * 32); st[i][1] = bld1(rs, O_STG + (256 * i + tid) * 32 + 16); }
    };
    const rsrc_t ru = __builtin_amdgcn_make_buffer_rsrc((void*)p.u, 0, T * D * 2, 0x00020000);
    auto xld = [&](int i) __attribute__((always_inline)) { xa[i] = __builtin_amdgcn_raw_buffer_load_b64(ru, tid * (4 * D), (512 * (i >> 1) + (i & 1)) * (2 * D) + 8 * q4, 0); };
    auto hook = [&](int i) __attribute__((always_inline)) {
      SB();
      if (i == 2) { xld(4); xld(5); }
      if (i == 3) { xld(6); xld(7); }
      if (i == 8) gather();
      SB();
    };
    SB();
    code_prefetch();
    xld(0); xld(1); xld(2); xld(3);
    SB();
    u16* himg = (u16*)(smem + L_HIMG);
    if (tid < 128) *(v4u*)(himg + (tid >> 5) * HL + (tid & 31) * 8) = v4u{0, 0, 0, 0};
    const float dt = rbf(__builtin_amdgcn_exp2f(ldt * 1.4426950408889634f));
    kgen_wave(par, dt, (unsigned*)(smem + L_KSCR) + w * 16 * RS, smem + L_KIMG + w * KIMG_B, lane, hook);
    STAMP(1);
#pragma unroll 1
    for (unsigned s = 0; s < SPIN; ++s) {
      unsigned bad = (unsigned)(mg >> 32) ^ tag;
#pragma unroll
      for (int i = 0; i < 4; ++i) bad |= (st[i][0][1] ^ tag) | (st[i][0][3] ^ tag) | (st[i][1][1] ^ tag) | (st[i][1][3] ^ tag);
      PIN(bad);
      if (__all(bad == 0u)) break;
      asm volatile("" ::: "memory");
      gather();
    }
    ORDER();
    STAMP(2);
    float sa[4], c1[4], ga[4];
#pragma unroll
    for (int c = 0; c < 4; ++c) {
      sa[c] = u2f(__builtin_amdgcn_readlane((unsigned)mg, c));
      c1[c] = rbf(1.0f + u2f(__builtin_amdgcn_readlane((unsigned)mg, 4 + c)));
      ga[c] = u2f(__builtin_amdgcn_readlane((unsigned)mg, 8 + c));
    }
    unsigned hv[4][4];
#pragma unroll
    for (int i = 0; i < 4; ++i) {
      const int pr = 256 * i + tid;
      const f32x2 m = {u2f(st[i][0][0]), u2f(st[i][1][0])}, r = {u2f(st[i][0][2]), u2f(st[i][1][2])};
      const f32x2 x[4] = {f32x2{bflo(xa[2 * i][0]), bflo(xa[2 * i + 1][0])}, f32x2{bfhi(xa[2 * i][0]), bfhi(xa[2 * i + 1][0])},
                          f32x2{bflo(xa[2 * i][1]), bflo(xa[2 * i + 1][1])}, f32x2{bfhi(xa[2 * i][1]), bfhi(xa[2 * i + 1][1])}};
#pragma unroll
      for (int c = 0; c < 4; ++c) {
        const f32x2 h = (x[c] - m) * r * c1[c] + sa[c];
        hv[i][c] = pk_bf16(h[0], h[1]);
        *(unsigned*)(himg + c * HL + 256 + 2 * pr) = hv[i][c];
      }
    }
    unsigned ab[8];
#pragma unroll
    for (int a = 0; a < 8; ++a) { ab[a] = lds0 + L_KIMG + w * KIMG_B + (i16 & 1) * (KL * 2) + (2032 - (i16 & ~1) - 2 * (i16 & 1) + 8 * g) * 2 - 64 * (8 * a + 7); PIN(ab[a]); }
    unsigned bb = lds0 + L_HIMG + (w * HL + 256 - 16 + 16 * i16 + 8 * g) * 2 - 64 * 7; PIN(bb);
    __syncthreads();
    STAMP(3);
    f32x4 acc[8];
#pragma unroll
    for (int c = 0; c < 8; ++c) acc[c] = f32x4{0.f, 0.f, 0.f, 0.f};
    v4u O[2][16];
#define CLOAD1(buf, r, k) { if constexpr (CL[k] < 8) { const LAS unsigned* ap = (const LAS unsigned*)(size_t)ab[CL[k] & 7] + 16 * (7 - (r)); O[buf][CL[k]] = v4u{ap[0], ap[1], ap[2], ap[3]}; } \
                            else O[buf][CL[k]] = *((const LAS v4u*)(size_t)bb + 4 * (8 * (CL[k] - 8) + 7 - (r))); }
    sfor<16>(LAM { constexpr int k = ic_; CLOAD1(0, 0, k); });
    SB();
    unsigned cav = 0;
    sfor<8 * 36>(LAM {
      constexpr int r = ic_ / 36, m = ic_ % 36;
      if constexpr (m == 0) TS(r);
      acc[CA[m] + CB[m]] = mfma_bf(O[r & 1][CA[m]], O[r & 1][8 + CB[m]], acc[CA[m] + CB[m]]);
      if constexpr (r < 7 && (m & 1) == 0 && m < 32) CLOAD1((r + 1) & 1, r + 1, m >> 1);
      if constexpr (r < 4 && (m & 7) == 5) WSP_DMA(r, m >> 3, L_WL);
      if constexpr (r == 7 && m == 21) cav = cnt_load(p, C_A, lane);
      SB();
    });
    TS(8); TSDUMP(0, 9);
    u16* ybuf = (u16*)(smem + L_KSCR);
#pragma unroll
    for (int c = 0; c < 8; ++c)
      *(v2u*)(ybuf + w * T + 256 * c + 16 * i16 + 4 * g) = v2u{pk_bf16(acc[c][0], acc[c][1]), pk_bf16(acc[c][2], acc[c][3])};
    __syncthreads();
    STAMP(4);
    const float dk[4] = {bflo(dkv[0]), bfhi(dkv[0]), bflo(dkv[1]), bfhi(dkv[1])};
#pragma unroll
    for (int i = 0; i < 4; ++i) {
      const int pr = 256 * i + tid;
      const f32x2 x[4] = {f32x2{bflo(xa[2 * i][0]), bflo(xa[2 * i + 1][0])}, f32x2{bfhi(xa[2 * i][0]), bfhi(xa[2 * i + 1][0])},
                          f32x2{bflo(xa[2 * i][1]), bflo(xa[2 * i + 1][1])}, f32x2{bfhi(xa[2 * i][1]), bfhi(xa[2 * i + 1][1])}};
      f32x2 o[4];
#pragma unroll
      for (int c = 0; c < 4; ++c) o[c] = x[c] + (unp(*(const unsigned*)(ybuf + c * T + 2 * pr)) + unp(hv[i][c]) * dk[c]) * ga[c];
      bst1(rs, O_X1G + (q4 * T + 2 * pr) * 8, v4u{(pk_bf16(o[0][0], o[1][0])), (pk_bf16(o[2][0], o[3][0])), (pk_bf16(o[0][1], o[1][1])), (pk_bf16(o[2][1], o[3][1]))});
    }
    SB();
    rdy = __all(cav >= 12u);
    ORDER();
    STAMP(5);
  } else {
    code_prefetch();
    aux_role(p, rs, smem, (bi - 20) * 8 + bx, tag);
#pragma unroll
    for (int q = 0; q < 16; ++q) WSP_DMA(q >> 2, q & 3, L_WL);
    DRAIN();
  }
#pragma unroll
  for (int q = 0; q < 16; ++q) WR_RD(q, q);
  asm volatile("s_waitcnt lgkmcnt(0)" ::: "memory");
#pragma unroll
  for (int q = 0; q < 6; ++q) WSP_DMA(4 + (q >> 2), q & 3, L_WL);
  if (is_conv) {
    asm volatile("s_waitcnt vmcnt(6)" ::: "memory");
    __syncthreads();
    arrive(p, C_X1, tid);
  }
#pragma unroll
  for (int q = 6; q < 16; ++q) WSP_DMA(4 + (q >> 2), q & 3, L_WL);
  if (!rdy) cnt_wait<4>(p, C_A, 12u, lane);
  {
    const rsrc_t rb = __builtin_amdgcn_make_buffer_rsrc((void*)p.b_sp, 0, T * 2, 0x00020000);
    if (w < 3) dma16s(rs, l16, w == 0 ? O_U + (FH + 16 * b0) * 8 : (w == 1 ? O_U + 16 * b0 * 8 : O_MODG + (5 * D + 80 * j) * 8), lds0 + L_DUMMY + w * 1024);
    else dma16(rb, l16, 128 * panel, lds0 + L_DUMMY + 3072);
  }
#pragma unroll
  for (int q = 0; q < 16; ++q) R_LD(q, z2s0, z2s1);
  SB();
  asm volatile("s_waitcnt vmcnt(16)" ::: "memory");
#pragma unroll
  for (int q = 0; q < 16; ++q) WR_RD(q, 16 + q);
  asm volatile("s_waitcnt lgkmcnt(0)" ::: "memory");
#pragma unroll
  for (int q = 0; q < 16; ++q) WSP_DMA(8 + (q >> 2), q & 3, L_WL);
  SB();
  STAMP(6);
  const int xoff = (8 * (lane >> 5) * T + 2 * (lane & 31)) * 8;
  const int xso = O_X1G + (2 * w * T + 64 * panel) * 8;
  v4u X[20];
#define X_LD(i) X[i] = bld1(rs, xoff, xso + ((i) >> 1) * (16 * T * 8) + ((i) & 1) * (T * 8))
  if (w == 3) cnt_wait<2>(p, C_X1, 20u, lane);
  __syncthreads();
  STAMP(7);
#pragma unroll
  for (int i = 0; i < 20; ++i) X_LD(i);
  SB();
  unsigned char* xs = smem;
  float* sxL = (float*)(smem + L_MISC);
  float* st1 = (float*)(smem + L_MISC + 512) + w * 128;
  float* gsum = (float*)(smem + L_MISC + 2560);
  float* st2 = (float*)(smem + L_MISC + 4608) + w * 128;
  int xw;
  {
    const int t = 2 * (lane & 31);
    const int tp = (2 * (t >> 5) + ((t >> 2) & 1)) * 16 + 4 * ((t >> 3) & 3) + (t & 3);
    xw = (((lane >> 5) * 4 + w) * 64 + tp) * 16;
  }
  const int aoff = (g * 64 + i16) * 16;
  f32x4 a0[4], a1[4], gram = {0.f, 0.f, 0.f, 0.f};
#pragma unroll
  for (int i = 0; i < 4; ++i) { a0[i] = f32x4{0.f, 0.f, 0.f, 0.f}; a1[i] = a0[i]; }
#define XW(i, h) *(v4u*)(xs + (i) * 8192 + xw + 16 * (h)) = v4u{X[2 * (i)][2 * (h)], X[2 * (i)][2 * (h) + 1], X[2 * (i) + 1][2 * (h)], X[2 * (i) + 1][2 * (h) + 1]}
#define LDA(ks, i) (*(const v4u*)(xs + (ks) * 4096 + aoff + ((i) < 4 ? (i) : w) * 256))
  XW(0, 0); XW(0, 1); XW(1, 0); XW(1, 1);
  __syncthreads();
  STAMP(8);
  {
    v4u A[2][5], bb1;
    const v4u ones = {0x3f803f80u, 0x3f803f80u, 0x3f803f80u, 0x3f803f80u};
#pragma unroll
    for (int i = 0; i < 5; ++i) A[0][i] = LDA(0, i);
    bb1 = (w == 3) ? ones : R[1];
    SB();
    sfor<20 * 9>(LAM {
      constexpr int ks = ic_ / 9, m = ic_ % 9;
      if constexpr (m == 0 && (ks & 1) == 0) TS(ks >> 1);
      if constexpr (m == 0 && (ks & 3) == 3 && ks < 19) { __syncthreads(); SB(); }
      if constexpr (m == 8) gram = mfma_bf(A[ks & 1][4], A[ks & 1][4], gram);
      else if constexpr (m & 1) a1[m >> 1] = mfma_bf(A[ks & 1][m >> 1], bb1, a1[m >> 1]);
      else a0[m >> 1] = mfma_bf(A[ks & 1][m >> 1], R[2 * ks], a0[m >> 1]);
      if constexpr (m < 5 && ks < 19) A[(ks + 1) & 1][m] = LDA(ks + 1, m);
      if constexpr (m == 5) { if constexpr (ks < 12) R_LD(16 + 2 * ks, z2s0, z2s1); else R_LD(2 * (ks - 12), z1s0, z1s1); }
      if constexpr (m == 8) { if constexpr (ks < 12) R_LD(17 + 2 * ks, z2s0, z2s1); else R_LD(2 * (ks - 12) + 1, z1s0, z1s1); }
      if constexpr (m == 8 && ks < 19) bb1 = (w == 3) ? ones : R[2 * ks + 3];
      if constexpr ((m == 6 || m == 7) && (ks & 3) < 2 && ks < 16) XW(2 * (ks >> 2) + 2 + (ks & 3), m - 6);
      SB();
    });
    TS(10); TSDUMP(16, 11);
  }
  if (w == 3) {
    if (i16 == 0) {
#pragma unroll
      for (int i = 0; i < 4; ++i) *(f32x4*)(sxL + i * 16 + 4 * g) = a1[i];
    }
#pragma unroll
    for (int i = 0; i < 4; ++i) a1[i] = f32x4{0.f, 0.f, 0.f, 0.f};
  }
  if ((i16 >> 2) == g) sxL[64 + w * 16 + i16] = (i16 & 3) == 0 ? gram[0] : (i16 & 3) == 1 ? gram[1] : (i16 & 3) == 2 ? gram[2] : gram[3];
  __syncthreads();
  STAMP(9);
  {
    const float m = sxL[lane] * (1.0f / D);
    const float var = fmaxf(sxL[64 + lane] * (1.0f / D) - m * m, 0.f);
    const float r = __builtin_amdgcn_rsqf(var + 1e-5f);
    st1[lane] = r; st1[64 + lane] = r * m;
  }
  v4u A1[2][4];
  {
    const f32x2* cU2 = (const f32x2*)(smem + L_DUMMY);
    f32x2 u0 = cU2[16 * lb0 + i16], u1 = cU2[16 * lb1 + i16];
    if (!has0) u0 = f32x2{0.f, 0.f};
    if (!has1) u1 = f32x2{0.f, 0.f};
    float s1[16], s2[16];
    f32x4 rr[4], rm[4];
#pragma unroll
    for (int i = 0; i < 4; ++i) { rr[i] = *(const f32x4*)(st1 + i * 16 + 4 * g); rm[i] = *(const f32x4*)(st1 + 64 + i * 16 + 4 * g); }
    sfor<8>(LAM {
      constexpr int i = ic_ >> 1, h = ic_ & 1;
      R_LD(16 + 2 * ic_, z1s0, z1s1); R_LD(17 + 2 * ic_, z1s0, z1s1);
      const f32x2 r2 = {rr[i][2 * h], rr[i][2 * h + 1]}, m2 = {rm[i][2 * h], rm[i][2 * h + 1]};
      const f32x2 v0 = gelu2(f32x2{a0[i][2 * h], a0[i][2 * h + 1]} * r2 + (u0[1] - m2 * u0[0]));
      const f32x2 v1 = gelu2(f32x2{a1[i][2 * h], a1[i][2 * h + 1]} * r2 + (u1[1] - m2 * u1[0]));
      a0[i][2 * h] = v0[0]; a0[i][2 * h + 1] = v0[1]; a1[i][2 * h] = v1[0]; a1[i][2 * h + 1] = v1[1];
      f32x2 q1 = v0 + v1, q2 = v0 * v0 + v1 * v1;
      PIN(q1); PIN(q2);
      s1[4 * i + 2 * h] = q1[0]; s1[4 * i + 2 * h + 1] = q1[1]; s2[4 * i + 2 * h] = q2[0]; s2[4 * i + 2 * h + 1] = q2[1];
      SB();
    });
#pragma unroll
    for (int i = 0; i < 4; ++i) A1[0][i] = LDA(0, i);
    const float t1 = bfly16(s1, i16), t2 = bfly16(s2, i16);
    const int tp = (i16 >> 2) * 16 + 4 * g + (i16 & 3);
    bst1(rs, O_ZST + (((panel * 4 + w) * 8 + j) * 64 + tp) * 16, v4u{f2u(t1), tag, f2u(t2), tag});
  }
  SB();
  STAMP(10);
  f32x4 c0[4], c1[4];
#pragma unroll
  for (int i = 0; i < 4; ++i) { c0[i] = f32x4{0.f, 0.f, 0.f, 0.f}; c1[i] = c0[i]; }
#define Z1M(ks, m) { if constexpr ((m) & 1) c1[(m) >> 1] = mfma_bf(R[2 * (ks) + 1], A1[(ks) & 1][(m) >> 1], c1[(m) >> 1]); else c0[(m) >> 1] = mfma_bf(R[2 * (ks)], A1[(ks) & 1][(m) >> 1], c0[(m) >> 1]); \
    if constexpr ((m) < 4 && (ks) < 19) A1[((ks) + 1) & 1][m] = LDA((ks) + 1, m); }
  constexpr int KA = 11;
  v4u zs[8];
  const int zsoff = O_ZST + ((panel * 4 + w) * 8 * 64) * 16;
  sfor<KA * 8>(LAM {
    constexpr int ks = ic_ / 8, m = ic_ % 8;
    Z1M(ks, m);
    if constexpr ((m == 4 || m == 7) && ks < 4) R_LD(32 + 2 * ks + (m == 7), z1s0, z1s1);
    if constexpr ((m == 4 || m == 7) && ks >= KA - 6 && ks < KA - 2) { constexpr int jj = 2 * (ks - (KA - 6)) + (m == 7); zs[jj] = bld1(rs, l16, zsoff + jj * 1024); }
    SB();
  });
  v2u x1v[5];
#pragma unroll
  for (int db = 0; db < 5; ++db) {
    const int d = 80 * j + 16 * db + 4 * g;
    x1v[db] = *(const v2u*)(xs + ((d >> 3) * 64 + 16 * w + i16) * 16 + ((d >> 2) & 1) * 8);
  }
  STAMP(11);
  {
#pragma unroll 1
    for (unsigned s = 0; s < SPIN; ++s) {
      unsigned bad = 0;
#pragma unroll
      for (int jj = 0; jj < 8; ++jj) bad |= (zs[jj][1] ^ tag) | (zs[jj][3] ^ tag);
      PIN(bad);
      if (__all(bad == 0u)) break;
      asm volatile("" ::: "memory");
#pragma unroll
      for (int jj = 0; jj < 8; ++jj) zs[jj] = bld1(rs, l16, zsoff + jj * 1024);
    }
    float a = 0.f, b = 0.f;
#pragma unroll
    for (int jj = 0; jj < 8; ++jj) { a += u2f(zs[jj][0]); b += u2f(zs[jj][2]); }
    *(f32x2*)(gsum + (w * 64 + lane) * 2) = f32x2{a, b};
  }
  __syncthreads();
  STAMP(12);
  {
    const f32x2 s = (*(const f32x2*)(gsum + lane * 2) + *(const f32x2*)(gsum + 128 + lane * 2)) + (*(const f32x2*)(gsum + 256 + lane * 2) + *(const f32x2*)(gsum + 384 + lane * 2));
    const float m = s[0] * (1.0f / FH);
    const float var = fmaxf(s[1] * (1.0f / FH) - m * m, 0.f);
    const float r = __builtin_amdgcn_rsqf(var + 1e-5f);
    st2[lane] = r; st2[64 + lane] = r * m;
  }
  {
    unsigned o0[4][2], o1[4][2];
#pragma unroll
    for (int i = 0; i < 4; ++i) {
      const f32x4 rr = *(const f32x4*)(st2 + i * 16 + 4 * g), rm = *(const f32x4*)(st2 + 64 + i * 16 + 4 * g);
      const f32x4 n0 = a0[i] * rr - rm, n1 = a1[i] * rr - rm;
      o0[i][0] = pk_bf16(n0[0], n0[1]); o0[i][1] = pk_bf16(n0[2], n0[3]);
      o1[i][0] = pk_bf16(n1[0], n1[1]); o1[i][1] = pk_bf16(n1[2], n1[3]);
    }
#pragma unroll
    for (int pp = 0; pp < 2; ++pp) {
      bst1(rs, zu0 + pp * 1024 + l16, v4u{o0[2 * pp][0], o0[2 * pp][1], o0[2 * pp + 1][0], o0[2 * pp + 1][1]});
      bst1(rs, zu1 + pp * 1024 + l16, v4u{o1[2 * pp][0], o1[2 * pp][1], o1[2 * pp + 1][0], o1[2 * pp + 1][1]});
    }
  }
  SB();
  STAMP(13);
  sfor<(20 - KA) * 8>(LAM {
    constexpr int ks = KA + ic_ / 8, m = ic_ % 8;
    Z1M(ks, m);
    if constexpr ((m == 4 || m == 7) && 2 * (ks - KA) + (m == 7) < KA) WSD_DMA(2 * (ks - KA) + (m == 7));
    SB();
  });
  static_assert(KA == 11, "count below");
  asm volatile("s_waitcnt vmcnt(11)" ::: "memory");
  __syncthreads();
  GAS unsigned* flags = (GAS unsigned*)(p.ws + O_FLAG);
  if (tid == 0) __hip_atomic_store(flags + j * 32 + panel, tag, RLX_AGENT);
  STAMP(14);
  int zof[7], rof[7];
#pragma unroll
  for (int lf = 0; lf < 7; ++lf) {
    int ab = 2 * w + lf; ab = ab >= 7 ? ab - 7 : ab;
    zof[lf] = (ab == 6 && j >= 6) ? OOB : O_Z2N + (b0 + ab) * 65536 + 16 * w * 1024;
    rof[lf] = ((ab * 3 + ((w - (ab >> 1) - 1) & 3)) * 4) * 1024;
    asm volatile("" : "+s"(zof[lf]), "+s"(rof[lf]));
  }
  constexpr int ZD = 4;
  v4u Z[16][7];
#define Z_LD(i, f) Z[i][f] = bld1(rs, l16, zof[f] + (((i) + rot) & 15) * 1024)
  unsigned z1p[2][4][2];
  {
    const v4u* cU1 = (const v4u*)(smem + L_DUMMY + 1024);
    const v4u ua0 = cU1[8 * lb0 + 2 * g], ua1 = cU1[8 * lb0 + 2 * g + 1], ub0 = cU1[8 * lb1 + 2 * g], ub1 = cU1[8 * lb1 + 2 * g + 1];
    float sr[4], sm[4];
#pragma unroll
    for (int i = 0; i < 4; ++i) { sr[i] = st1[16 * i + i16]; sm[i] = st1[64 + 16 * i + i16]; }
    sfor<8>(LAM {
      constexpr int i = ic_ >> 1, h = ic_ & 1;
      if constexpr (ic_ == 0) { WSD_DMA(11); WSD_DMA(12); WSD_DMA(13); }
      if constexpr (ic_ == 1) { WSD_DMA(14); WSD_DMA(15); }
      if constexpr (ic_ == ZPOLL) { STAMP(15); flag_wait8(flags + j * 32 + 8 * w, tag, lane); STAMP(16); }
      if constexpr (ic_ >= ZPOLL) {
        constexpr int n = 8 - ZPOLL, k = ic_ - ZPOLL;
#pragma unroll
        for (int q = 14 * k / n; q < 14 * (k + 1) / n; ++q) Z_LD(q / 7, q % 7);
        SB();
      }
      const v4u qa = h ? ua1 : ua0, qb = h ? ub1 : ub0;
      const f32x2 v0 = gelu2(f32x2{c0[i][2 * h], c0[i][2 * h + 1]} * sr[i] + (f32x2{u2f(qa[1]), u2f(qa[3])} - f32x2{u2f(qa[0]), u2f(qa[2])} * sm[i]));
      const f32x2 v1 = gelu2(f32x2{c1[i][2 * h], c1[i][2 * h + 1]} * sr[i] + (f32x2{u2f(qb[1]), u2f(qb[3])} - f32x2{u2f(qb[0]), u2f(qb[2])} * sm[i]));
      z1p[0][i][h] = pk_bf16(v0[0], v0[1]); z1p[1][i][h] = pk_bf16(v1[0], v1[1]);
      PIN(z1p[0][i][h]); PIN(z1p[1][i][h]);
    });
  }
  SB();
  f32x4 acc[7][4];
#pragma unroll
  for (int f = 0; f < 7; ++f)
#pragma unroll
    for (int t = 0; t < 4; ++t) acc[f][t] = f32x4{0.f, 0.f, 0.f, 0.f};
  const int woso = O_WOUTF + (5 * j) * 27 * 1024;
  v4u WO[35];
  v4u WL[2][4];
#define WO_LD(q) WO[q] = bld1(rs, l16, woso + (((q) % 5) * 27 + min(7 * w + (q) / 5, 26)) * 1024)
  sfor<16 * 28>(LAM {
    constexpr int i = ic_ / 28, f = (ic_ % 28) / 4, t = ic_ % 4;
    if constexpr (f == 0 && t == 0) TS(i);
    if constexpr (i < 8) acc[f][t] = mfma_bf(Z[i][f], WR[i * 4 + t], acc[f][t]);
    else acc[f][t] = mfma_bf(Z[i][f], WL[i & 1][t], acc[f][t]);
    if constexpr (i < 2 && (t == 0 || t == 2)) { constexpr int q = 2 * f + (t >> 1); Z_LD(2 + 2 * i + q / 7, q % 7); }
    if constexpr (i >= 2 && t == 0) { if constexpr (i + ZD < 16) Z_LD(i + ZD, f); else WO_LD(7 * (i + ZD - 16) + f); }
    if constexpr (t == 2 && f < 4 && i >= 7 && i < 15) WL[(i + 1) & 1][f] = *(const v4u*)(smem + (i + 1 < 12 ? L_WL + wrd + (((i + 1) & 3) * 4 + f) * 1024 : wrd - w * 15360 + (((i + 1) & 3) * 4 + f) * 4096));
    SB();
  });
  TS(16); TSDUMP(32, 17);
  __syncthreads();
  STAMP(17);
#pragma unroll
  for (int lf = 2; lf < 7; ++lf)
#pragma unroll
    for (int t = 0; t < 4; ++t) *(f32x4*)(smem + rof[lf] + t * 1024 + l16) = acc[lf][t];
  if (w == 3) {
#pragma unroll
    for (int t = 0; t < 4; ++t) *(f32x4*)(smem + rof[1] + t * 1024 + l16) = acc[1][t];
  }
  f32x4 o0[4], o1[4];
#pragma unroll
  for (int t = 0; t < 4; ++t) { const float bias = bf2f(((const u16*)(smem + L_DUMMY + 3072))[8 * (i16 >> 2) + (i16 & 3) + 32 * (t >> 1) + 4 * (t & 1)]); o0[t] = acc[0][t] + bias; o1[t] = acc[1][t] + bias; PIN(o0[t]); PIN(o1[t]); }
  __syncthreads();
  {
    const unsigned char* r0 = smem + (2 * w * 12) * 1024 + l16;
    const unsigned char* r1 = smem + ((w < 3 ? 2 * w + 1 : 6) * 12) * 1024 + l16;
    f32x4 pa[4][3], pb[4][3];
#pragma unroll
    for (int t = 0; t < 4; ++t)
#pragma unroll
      for (int s = 0; s < 3; ++s) { pa[t][s] = *(const f32x4*)(r0 + (s * 4 + t) * 1024); pb[t][s] = *(const f32x4*)(r1 + (s * 4 + t) * 1024); }
    SB();
#pragma unroll
    for (int t = 0; t < 4; ++t) {
      const f32x4 x0 = (o0[t] + pa[t][0]) + (pa[t][1] + pa[t][2]), x1 = (o1[t] + pb[t][0]) + (pb[t][1] + pb[t][2]);
      const f32x2 qa = unp(z1p[0][t][0]) * f32x2{x0[0], x0[1]}, qb = unp(z1p[0][t][1]) * f32x2{x0[2], x0[3]};
      const f32x2 qc = unp(z1p[1][t][0]) * f32x2{x1[0], x1[1]}, qd = unp(z1p[1][t][1]) * f32x2{x1[2], x1[3]};
      const unsigned q0 = (pk_bf16(qa[0], qa[1])), q1 = (pk_bf16(qb[0], qb[1])), q2 = (pk_bf16(qc[0], qc[1])), q3 = (pk_bf16(qd[0], qd[1]));
      bst1(rs, pu16 + t * 27 * 1024 + l16, v4u{q0, q1, q2, q3});
      bst1_64(rs, pu8 + t * 27 * 1024 + l16, v2u{q0, q1});
    }
  }
  SB();
  STAMP(18);
#pragma unroll
  for (int q = 7 * ZD; q < 35; ++q) WO_LD(q);
  SB();
  asm volatile("s_waitcnt vmcnt(7)" ::: "memory");
  __syncthreads();
  if (tid == 0) __hip_atomic_store(flags + 256 + panel * 32 + j, tag, RLX_AGENT);
  const int trow = 64 * panel + 8 * (i16 >> 2) + (i16 & 3) + 32 * (w >> 1) + 4 * (w & 1);
  int pof[4], eof[4];
#pragma unroll
  for (int lt = 0; lt < 4; ++lt) {
    const int tb = (w + lt) & 3;
    pof[lt] = O_P + ((4 * panel + tb) * 27 + 7 * w) * 1024;
    eof[lt] = L_WL + ((tb * 3 + ((w - tb - 1) & 3)) * 5) * 1024;
  }
  const int k6 = w < 3 ? 6 * 1024 : OOB;
  STAMP(19);
  v4u P[7][4];
  f32x4 e[5][4];
#define P_LD(q) P[(q) >> 2][(q) & 3] = bld1(rs, l16, pof[(q) & 3] + (((q) >> 2) < 6 ? ((q) >> 2) * 1024 : k6))
  constexpr int PDN = 12;
  flag_wait8(flags + 256 + panel * 32, tag, lane);
  STAMP(20);
#pragma unroll
  for (int q = 0; q < PDN; ++q) P_LD(q);
  SB();
#pragma unroll
  for (int d = 0; d < 5; ++d)
#pragma unroll
    for (int t = 0; t < 4; ++t) e[d][t] = f32x4{0.f, 0.f, 0.f, 0.f};
  sfor<28 * 5>(LAM {
    constexpr int q = ic_ / 5, d = ic_ % 5;
    e[d][q & 3] = mfma_bf(WO[5 * (q >> 2) + d], P[q >> 2][q & 3], e[d][q & 3]);
    if constexpr (d == 0 && q + PDN < 28) P_LD(q + PDN);
    SB();
  });
  STAMP(21);
#pragma unroll
  for (int lt = 1; lt < 4; ++lt)
#pragma unroll
    for (int d = 0; d < 5; ++d) *(f32x4*)(smem + eof[lt] + d * 1024 + l16) = e[d][lt];
  __syncthreads();
  u16* orow = p.out + (size_t)trow * D + 80 * j + 4 * g;
  f32x4 ep[5][3];
  v4u gb0[5], gb1[5];
#pragma unroll
  for (int d = 0; d < 5; ++d) { gb0[d] = *(const v4u*)(smem + L_DUMMY + 2048 + (16 * d + 4 * g) * 8); gb1[d] = *(const v4u*)(smem + L_DUMMY + 2048 + (16 * d + 4 * g) * 8 + 16); }
#pragma unroll
  for (int d = 0; d < 5; ++d)
#pragma unroll
    for (int s = 0; s < 3; ++s) ep[d][s] = *(const f32x4*)(smem + L_WL + ((w * 3 + s) * 5 + d) * 1024 + l16);
  SB();
#pragma unroll
  for (int d = 0; d < 5; ++d) {
    const f32x4 eo = (e[d][0] + ep[d][0]) + (ep[d][1] + ep[d][2]);
    const float r0 = bflo(x1v[d][0]) + u2f(gb0[d][0]) * eo[0], r1 = bfhi(x1v[d][0]) + u2f(gb0[d][2]) * eo[1];
    const float r2 = bflo(x1v[d][1]) + u2f(gb1[d][0]) * eo[2], r3 = bfhi(x1v[d][1]) + u2f(gb1[d][2]) * eo[3];
    *(v2u*)(orow + 16 * d) = v2u{pk_bf16(r0, r1), pk_bf16(r2, r3)};
  }
  if (blockIdx.x == 0 && tid == 0) __hip_atomic_store((GAS unsigned*)(p.ws + O_EPOCH), tag, RLX_AGENT);
  STAMP(22);
  asm volatile(".Lkf_end:");
}

extern "C" void kernel_launch(void* const* d_in, const int* in_sizes, int n_in, void* d_out, int out_size, void* d_ws,
                              size_t ws_size, hipStream_t stream) {
  Params p;
  p.u = (const u16*)d_in[0]; p.a_re = (const u16*)d_in[1]; p.a_im = (const u16*)d_in[2];
  p.b_re = (const u16*)d_in[3]; p.b_im = (const u16*)d_in[4]; p.c_re = (const u16*)d_in[5]; p.c_im = (const u16*)d_in[6];
  p.log_dt = (const u16*)d_in[7]; p.dskip = (const u16*)d_in[8];
  p.w_in = (const u16*)d_in[10]; p.w_sp = (const u16*)d_in[11]; p.b_sp = (const u16*)d_in[12]; p.w_out = (const u16*)d_in[13];
  p.cvec = (const u16*)d_in[14]; p.w_mod = (const u16*)d_in[15];
  p.out = (u16*)d_out; p.ws = (unsigned char*)d_ws;
  (void)hipFuncSetAttribute((const void*)k_fused, hipFuncAttributeMaxDynamicSharedMemorySize, L_TOTAL);
  k_fused<<<256, 256, L_TOTAL, stream>>>(p);
}
```

```cpp
#include <hip/hip_runtime.h>
#include <hip/hip_bf16.h>
#include <stdint.h>
#include <utility>

typedef unsigned v4u __attribute__((ext_vector_type(4)));
typedef unsigned v2u __attribute__((ext_vector_type(2)));
typedef float f32x4 __attribute__((ext_vector_type(4)));
typedef float f32x2 __attribute__((ext_vector_type(2)));
typedef short bf16x8 __attribute__((ext_vector_type(8)));
typedef _Float16 f16x8 __attribute__((ext_vector_type(8)));
typedef _Float16 f16x2 __attribute__((ext_vector_type(2)));
typedef __bf16 bf16x2_t __attribute__((ext_vector_type(2)));
typedef unsigned short u16;
typedef unsigned long long u64;
#define GAS __attribute__((address_space(1)))
#define LAS __attribute__((address_space(3)))
#define RLX_AGENT __ATOMIC_RELAXED, __HIP_MEMORY_SCOPE_AGENT
#define DEVI __device__ __forceinline__
#define SB() __builtin_amdgcn_sched_barrier(0)
template <int... I, class F> __device__ __forceinline__ void sfor_impl(std::integer_sequence<int, I...>, F&& f) { (f(std::integral_constant<int, I>{}), ...); }
template <int N, class F> __device__ __forceinline__ void sfor(F&& f) { sfor_impl(std::make_integer_sequence<int, N>{}, f); }
#define LAM [&](auto ic_) __attribute__((always_inline))
#define NTL(p) __builtin_nontemporal_load(p)
#define DRAIN() asm volatile("s_waitcnt vmcnt(0)" ::: "memory")

constexpr int T = 2048, D = 640, NST = 128, FH = 864, F2 = 1728;
constexpr int KL = 2080, KIMG_B = 2 * KL * 2, HL = 2304;
constexpr unsigned SPIN = 1u << 15;
constexpr int AUX_SC1 = 16, AUX_NT = 2;
constexpr int O_EPOCH = 0;
constexpr int O_INIT = 256;
constexpr int O_CNT = 4096;
constexpr int O_FLAG = O_CNT + 3 * 64 * 256;
constexpr int O_MODG = O_FLAG + 256 * 256;
constexpr int O_MODP = O_MODG + 3840 * 8 + 2048;
constexpr int O_STG = O_MODP + 4 * 2560 * 8;
constexpr int O_U = O_STG + 2048 * 16;
constexpr int O_ZST = O_U + 16384;
constexpr int O_X1G = O_ZST + 32 * 32 * 64 * 16;
constexpr int O_WINF = O_X1G + T * D * 2;
constexpr int O_WOUTF = O_WINF + 109 * 20 * 1024;
constexpr int O_Z2N = O_WOUTF + 40 * 27 * 1024;
constexpr int O_P = O_Z2N + 54 * 64 * 1024;
constexpr int O_END = O_P + 128 * 27 * 1024;
constexpr int C_X1 = 0, C_W = 1, C_S = 2;

constexpr size_t O_STAMP = 200u << 20;
#define TS(k) do {} while (0)
#define TSDUMP(base, n) do {} while (0)
#define STAMP(idx) do {} while (0)

DEVI float u2f(unsigned u) { return __builtin_bit_cast(float, u); }
DEVI unsigned f2u(float f) { return __builtin_bit_cast(unsigned, f); }
DEVI float bf2f(unsigned b) { return u2f(b << 16); }
DEVI float bflo(unsigned u) { return u2f(u << 16); }
DEVI float bfhi(unsigned u) { return u2f(u & 0xffff0000u); }
DEVI unsigned pk_bf16(float a, float b) {
  f32x2 v = {a, b};
  bf16x2_t r = __builtin_convertvector(v, bf16x2_t);
  return __builtin_bit_cast(unsigned, r);
}
DEVI float rbf(float a) { return bflo(pk_bf16(a, 0.f)); }
DEVI unsigned pk_f16(float a, float b) {
  f32x2 v = {a, b};
  f16x2 r = __builtin_convertvector(v, f16x2);
  return __builtin_bit_cast(unsigned, r);
}
DEVI f32x4 mfma_bf(v4u a, v4u b, f32x4 c) {
  return __builtin_amdgcn_mfma_f32_16x16x32_bf16(__builtin_bit_cast(bf16x8, a), __builtin_bit_cast(bf16x8, b), c, 0, 0, 0);
}
DEVI f32x4 mfma_h(v4u a, v4u b, f32x4 c) {
  return __builtin_amdgcn_mfma_f32_16x16x32_f16(__builtin_bit_cast(f16x8, a), __builtin_bit_cast(f16x8, b), c, 0, 0, 0);
}
typedef __amdgpu_buffer_rsrc_t rsrc_t;
DEVI v4u bld1(rsrc_t rs, int voff, int soff = 0) { return __builtin_amdgcn_raw_buffer_load_b128(rs, voff, soff, AUX_SC1); }
DEVI void bst1(rsrc_t rs, int voff, v4u v) { __builtin_amdgcn_raw_buffer_store_b128(v, rs, voff, 0, AUX_SC1); }
DEVI void bst1_64(rsrc_t rs, int voff, v2u v) { __builtin_amdgcn_raw_buffer_store_b64(v, rs, voff, 0, AUX_SC1); }
template <int CTRL> DEVI float dppf(float x) { return u2f((unsigned)__builtin_amdgcn_update_dpp(0, (int)f2u(x), CTRL, 0xf, 0xf, true)); }
DEVI float dot2(unsigned a, unsigned b, float c) { return __builtin_amdgcn_fdot2_f32_bf16(__builtin_bit_cast(bf16x2_t, a), __builtin_bit_cast(bf16x2_t, b), c, false); }
#define PIN(x) asm volatile("" : "+v"(x))
#define ORDER() __builtin_amdgcn_fence(__ATOMIC_ACQUIRE, "wavefront")

struct Params {
  const u16 *u, *a_re, *a_im, *b_re, *b_im, *c_re, *c_im, *log_dt, *dskip, *w_in, *w_sp, *b_sp, *w_out, *cvec, *w_mod;
  u16* out;
  unsigned char* ws;
};

DEVI int blk0(int j) { return j < 6 ? 7 * j : 42 + 6 * (j - 6); }
DEVI int pair_blk(int ks3, int h) {
  if (ks3 < 24) { const int j = ks3 / 3, w = ks3 - 3 * j; return blk0(j) + 2 * w + h; }
  return 7 * (2 * (ks3 - 24) + h) + 6;
}
DEVI void arrive(const Params& p, int c, int tid) {
  if (tid < 8) __hip_atomic_fetch_add((GAS unsigned*)(p.ws + O_CNT) + ((c * 8 + tid) * 8 + (blockIdx.x & 7)) * 64, 1u, RLX_AGENT);
}
DEVI unsigned cnt_load(const Params& p, int c, int lane) {
  return __hip_atomic_load((GAS unsigned*)(p.ws + O_CNT) + ((c * 8 + ((blockIdx.x >> 3) & 7)) * 8 + (lane & 7)) * 64, RLX_AGENT);
}
template <int SLP> DEVI void cnt_wait(const Params& p, int c, unsigned want, int lane) {
#pragma unroll 1
  for (unsigned s = 0; s < SPIN; ++s) {
    if (__all(cnt_load(p, c, lane) >= want)) break;
    __builtin_amdgcn_s_sleep(SLP);
  }
  ORDER();
}
struct C2 { f32x2 re, im; };
DEVI C2 cmul2(C2 a, C2 b) { return C2{a.re * b.re - a.im * b.im, a.re * b.im + a.im * b.re}; }
DEVI C2 cexp2(f32x2 are2, f32x2 arev, float l) {
  const f32x2 e = are2 * l, q = arev * l;
  const f32x2 m = {__builtin_amdgcn_exp2f(e[0]), __builtin_amdgcn_exp2f(e[1])};
  const float p0 = __builtin_amdgcn_fractf(q[0]), p1 = __builtin_amdgcn_fractf(q[1]);
  return C2{m * f32x2{__builtin_amdgcn_cosf(p0), __builtin_amdgcn_cosf(p1)}, m * f32x2{__builtin_amdgcn_sinf(p0), __builtin_amdgcn_sinf(p1)}};
}
constexpr int RS = 132;
DEVI void put_row(unsigned* buf, int row, int lane, C2 v) { *(v2u*)(buf + row * RS + 2 * lane) = v2u{pk_f16(v.re[0], v.im[0]), pk_f16(v.re[1], v.im[1])}; }
DEVI f32x2 unp(unsigned u) { return f32x2{bflo(u), bfhi(u)}; }

template <class HK> DEVI void kgen_wave(unsigned par[6], float dt, unsigned* buf0, unsigned char* img, int lane, HK&& hook) {
  unsigned* buf1 = (unsigned*)img;
  u16* tab = (u16*)buf0;
  const int c = lane & 15, g = lane >> 4;
  const f32x2 are2 = unp(par[0]) * (dt * 1.4426950408889634f), arev = unp(par[1]) * (dt * 0.15915494309189535f), nrev = -arev;
  C2 W = cmul2(C2{unp(par[4]), unp(par[5])}, C2{unp(par[2]) * dt, unp(par[3]) * dt});
  float mx = fmaxf(fmaxf(fabsf(W.re[0]), fabsf(W.re[1])), fmaxf(fabsf(W.im[0]), fabsf(W.im[1])));
  unsigned mu = f2u(mx);
  mu = max(mu, f2u(dppf<0x128>(u2f(mu)))); mu = max(mu, f2u(dppf<0x124>(u2f(mu)))); mu = max(mu, f2u(dppf<0x122>(u2f(mu)))); mu = max(mu, f2u(dppf<0x121>(u2f(mu))));
  const unsigned m0 = __builtin_amdgcn_readlane(mu, 0), m1 = __builtin_amdgcn_readlane(mu, 16), m2 = __builtin_amdgcn_readlane(mu, 32), m3 = __builtin_amdgcn_readlane(mu, 48);
  unsigned eb = (max(max(m0, m1), max(m2, m3)) >> 23) & 0xffu;
  eb = eb < 2u ? 2u : (eb > 250u ? 250u : eb);
  const float sc = u2f((eb + 1u) << 23), isc = u2f((253u - eb) << 23);
  W.re *= isc; W.im *= isc;
  {
    const C2 e8 = cexp2(are2, arev, 8.f);
    C2 W8 = cmul2(W, cexp2(are2, arev, 64.f));
#pragma unroll
    for (int b = 0; b < 8; ++b) {
      put_row(buf0, b, lane, W); put_row(buf0, 8 + b, lane, W8);
      if (b < 7) { W = cmul2(W, e8); W8 = cmul2(W8, e8); }
      if (b == 0) hook(2);
      if (b == 4) hook(3);
    }
  }
  v4u A[8];
#pragma unroll
  for (int ks = 0; ks < 8; ++ks) A[ks] = *(const v4u*)(buf0 + c * RS + 16 * ks + 4 * g);
  C2 er[8];
  er[0] = C2{f32x2{1.f, 1.f}, f32x2{0.f, 0.f}};
  er[1] = cexp2(are2, nrev, 1.f);
  er[2] = cmul2(er[1], er[1]); er[3] = cmul2(er[2], er[1]); er[4] = cmul2(er[2], er[2]);
  er[5] = cmul2(er[4], er[1]); er[6] = cmul2(er[3], er[3]); er[7] = cmul2(er[4], er[3]);
  hook(4);
  const C2 e128 = cexp2(are2, nrev, 128.f);
#pragma unroll
  for (int r = 0; r < 8; ++r) { put_row(buf0, r, lane, er[r]); put_row(buf0, 8 + r, lane, r ? cmul2(e128, er[r]) : e128); }
  f32x4 acc0 = {0.f, 0.f, 0.f, 0.f}, acc1 = acc0, acc2 = acc0;
#pragma unroll
  for (int ks = 0; ks < 8; ++ks) acc0 = mfma_h(A[ks], *(const v4u*)(buf0 + c * RS + 16 * ks + 4 * g), acc0);
  hook(5);
  C2 e = cmul2(e128, e128);
  C2 f = cexp2(are2, nrev, 1024.f);
#pragma unroll
  for (int a = 0; a < 2; ++a) {
#pragma unroll
    for (int r = 0; r < 4; ++r) put_row(buf0, 4 * a + r, lane, r ? cmul2(e, er[2 * r]) : e);
    e = cmul2(e, e128);
  }
  hook(6);
#pragma unroll
  for (int a = 0; a < 4; ++a) {
    put_row(buf0, 8 + 2 * a, lane, e); put_row(buf0, 9 + 2 * a, lane, cmul2(e, er[4])); if (a < 3) e = cmul2(e, e128);
    put_row(buf1, 2 * a, lane, f); f = cmul2(f, e128); put_row(buf1, 2 * a + 1, lane, f); f = cmul2(f, e128);
    if (a == 1) hook(7);
  }
  put_row(buf1, 8, lane, f);
  hook(8);
  {
    v4u B1[8], B2[8];
#pragma unroll
    for (int ks = 0; ks < 8; ++ks) { B1[ks] = *(const v4u*)(buf0 + c * RS + 16 * ks + 4 * g); B2[ks] = *(const v4u*)(buf1 + c * RS + 16 * ks + 4 * g); }
    SB();
#pragma unroll
    for (int ks = 0; ks < 8; ++ks) { acc1 = mfma_h(A[ks], B1[ks], acc1); acc2 = mfma_h(A[ks], B2[ks], acc2); }
  }
  hook(9);
  const int cv0 = 128 * (c >> 3) + (c & 7);
  const int cv1 = c < 8 ? 256 + 128 * (c >> 2) + 2 * (c & 3) : 512 + 128 * ((c - 8) >> 1) + 4 * (c & 1);
  const int cv2 = c < 9 ? 1024 + 128 * c : 2048;
#pragma unroll
  for (int r = 0; r < 4; ++r) {
    const int lb = 8 * (4 * g + r);
    const unsigned q01 = pk_bf16(acc0[r] * sc, acc1[r] * sc);
    tab[lb + cv0] = (u16)q01;
    tab[lb + cv1] = (u16)(q01 >> 16);
    tab[lb + cv2 + (c > 8 ? 1 : 0)] = (u16)pk_bf16(acc2[r] * sc, 0.f);
  }
  {
    const u16* t0 = tab + 2048 - 8 * lane;
    const int o = lane < 32 ? 1 : 0;
    const unsigned H0 = t0[0], L0 = t0[-8], H1 = t0[-512], L1 = t0[-520], H2 = t0[-1024], M2 = t0[-1028], L2 = t0[-1032];
    const u16* t3 = t0 - 1536;
    const unsigned v0 = t3[0], v1 = t3[-1 + o], v2 = t3[-2], v3 = t3[-3 - o], v4 = t3[-4], v5 = t3[-5 + o], v6 = t3[-6], v7 = t3[-7 - o], v8 = t3[-8];
    const unsigned k0 = tab[0];
    SB();
    unsigned char* i0 = img + lane * 16;
    unsigned char* i1 = i0 + KL * 2;
    {
      const unsigned X = (lane & 1) ? L0 : H0, HH = H0 | (H0 << 16), LL = L0 | (L0 << 16);
      *(v4u*)i0 = v4u{HH, HH, X | (L0 << 16), LL};
      *(v4u*)i1 = v4u{HH, H0 | (X << 16), LL, LL};
    }
    {
      const unsigned X = (lane & 1) ? L1 : H1, HH = H1 | (H1 << 16), LL = L1 | (L1 << 16);
      *(v4u*)(i0 + 1024) = v4u{HH, HH, X | (L1 << 16), LL};
      *(v4u*)(i1 + 1024) = v4u{HH, H1 | (X << 16), LL, LL};
    }
    {
      const unsigned HH = H2 | (H2 << 16), MM = M2 | (M2 << 16), LL = L2 | (L2 << 16);
      *(v4u*)(i0 + 2048) = v4u{HH, H2 | (M2 << 16), MM, LL};
      *(v4u*)(i1 + 2048) = v4u{HH, MM, M2 | (L2 << 16), LL};
    }
    *(v4u*)(i0 + 3072) = v4u{v0 | (v1 << 16), v2 | (v3 << 16), v4 | (v5 << 16), v6 | (v7 << 16)};
    *(v4u*)(i1 + 3072) = v4u{v1 | (v2 << 16), v3 | (v4 << 16), v5 | (v6 << 16), v7 | (v8 << 16)};
    if (lane < 8) *(v4u*)(img + (lane >> 2) * (KL * 2) + (256 + (lane & 3)) * 16) = v4u{lane == 0 ? k0 : 0u, 0u, 0u, 0u};
  }
}

constexpr int L_KIMG = 0, L_HIMG = 4 * KIMG_B, L_KSCR = L_HIMG + 4 * HL * 2;
constexpr int L_MISC = 86016, L_WL = L_MISC + 8192, L_DUMMY = L_WL + 65536, L_TOTAL = L_DUMMY + 4096;
static_assert(L_TOTAL <= 163840 && L_KSCR + 4 * 16 * RS * 4 <= L_MISC, "lds");
#define XAUX 0
#define MODHOOK 4
#define ASP 16
#define ROTM 0
#define ROTS 0
#define KAV 12
#define ZFIRST 1u
#define NB0 0
#define WNT ""
#define ZPOLL 7
constexpr int OOB = 64 << 20;
constexpr int CA[36] = {3,3,3,3,3,4,4,4,4, 2,2,2,2,2,2,5,5,5, 1,1,1,1,6,6,1,1,1, 0,0,0,0,7,0,0,0,0};
constexpr int CB[36] = {4,3,2,1,0,3,2,1,0, 5,4,3,2,1,0,2,1,0, 6,5,4,3,1,0,2,1,0, 7,6,5,4,0,3,2,1,0};
constexpr int CL[16] = {8+4,3,8+3,8+2,8+1,8+0,4,8+5,2,5,8+6,1,6,8+7,0,7};

DEVI void dma16s(rsrc_t r, int voff, int soff, unsigned ldsaddr) {
  asm volatile("s_mov_b32 m0, %2\n\ts_nop 0\n\tbuffer_load_dwordx4 %0, %1, %3 offen sc1 lds" :: "v"(voff), "s"(r), "s"(ldsaddr), "s"(soff) : "memory");
}
DEVI void dma16(rsrc_t r, int voff, int soff, unsigned ldsaddr) {
  asm volatile("s_mov_b32 m0, %2\n\ts_nop 0\n\tbuffer_load_dwordx4 %0, %1, %3 offen " WNT " lds" :: "v"(voff), "s"(r), "s"(ldsaddr), "s"(soff) : "memory");
}

DEVI unsigned plo(unsigned a, unsigned b) { return __builtin_amdgcn_perm(b, a, 0x05040100u); }
DEVI unsigned phi(unsigned a, unsigned b) { return __builtin_amdgcn_perm(b, a, 0x07060302u); }
DEVI unsigned silu_bf(float c) { return pk_bf16(c * __builtin_amdgcn_rcpf(1.0f + __builtin_amdgcn_exp2f(-1.4426950408889634f * c)), 0.f); }
DEVI float col_reduce(float* fl, const float (&acc)[8], int tid) {
  const int rg = tid >> 3, cl = tid & 7;
  *(f32x4*)(fl + rg * 68 + cl * 8) = f32x4{acc[0], acc[1], acc[2], acc[3]};
  *(f32x4*)(fl + rg * 68 + cl * 8 + 4) = f32x4{acc[4], acc[5], acc[6], acc[7]};
  __syncthreads();
  float s = 0.f;
#pragma unroll
  for (int r = 0; r < 8; ++r) s += fl[(8 * (tid & 3) + r) * 68 + (tid >> 2)];
  s += dppf<0xB1>(s); s += dppf<0x4E>(s);
  return s;
}
DEVI void wout_unit(const Params& p, rsrc_t rs, unsigned char* lw, int unit, int lane) {
  const int ks = unit / 5, dc = unit - 5 * ks, d = 128 * dc + 2 * lane;
  const int f0 = 16 * pair_blk(ks, 0), f1 = 16 * pair_blk(ks, 1);
  unsigned wv[4][8];
#pragma unroll
  for (int g = 0; g < 4; ++g)
#pragma unroll
    for (int j = 0; j < 8; ++j) wv[g][j] = NTL((const unsigned*)(p.w_out + (size_t)((j < 4 ? f0 : f1) + 4 * g + (j & 3)) * D + d));
#pragma unroll
  for (int g = 0; g < 4; ++g) {
    unsigned char* dst = lw + (lane >> 3) * 1024 + (g * 16 + 2 * (lane & 7)) * 16;
    *(v4u*)dst = v4u{plo(wv[g][0], wv[g][1]), plo(wv[g][2], wv[g][3]), plo(wv[g][4], wv[g][5]), plo(wv[g][6], wv[g][7])};
    *(v4u*)(dst + 16) = v4u{phi(wv[g][0], wv[g][1]), phi(wv[g][2], wv[g][3]), phi(wv[g][4], wv[g][5]), phi(wv[g][6], wv[g][7])};
  }
#pragma unroll
  for (int i = 0; i < 8; ++i) bst1(rs, O_WOUTF + ((8 * dc + i) * 27 + ks) * 1024 + lane * 16, *(const v4u*)(lw + i * 1024 + lane * 16));
}

DEVI void aux_role(const Params& p, rsrc_t rs, unsigned char* smem, int a, unsigned& tag) {
  const int tid = threadIdx.x, lane = tid & 63, w = __builtin_amdgcn_readfirstlane(tid >> 6);
  float* fl = (float*)smem;
  constexpr unsigned ONES = 0x3f803f80u;
  unsigned wv[5][8];
  if (a < 64) {
    const int row = 32 * a + 8 * w + (lane >> 3), c = lane & 7;
    v4u xv[10];
#pragma unroll
    for (int i = 0; i < 10; ++i) xv[i] = *(const v4u*)(p.u + (size_t)row * D + 64 * i + 8 * c);
    tag = __hip_atomic_load((GAS unsigned*)(p.ws + O_EPOCH), RLX_AGENT);
    SB();
    const u16* wsrc = p.w_in + (size_t)(8 * (tid >> 4)) * F2 + 32 * min(a, 53) + 2 * (tid & 15);
#pragma unroll
    for (int i = 0; i < 2; ++i)
#pragma unroll
      for (int j = 0; j < 8; ++j) wv[i][j] = NTL((const unsigned*)(wsrc + (size_t)(128 * i + j) * F2));
    SB();
    float s0 = 0.f, s1 = 0.f, q0 = 0.f, q1 = 0.f;
#pragma unroll
    for (int i = 0; i < 10; ++i)
#pragma unroll
      for (int j = 0; j < 4; j += 2) {
        s0 = dot2(xv[i][j], ONES, s0); q0 = dot2(xv[i][j], xv[i][j], q0);
        s1 = dot2(xv[i][j + 1], ONES, s1); q1 = dot2(xv[i][j + 1], xv[i][j + 1], q1);
      }
    float m = s0 + s1, v = q0 + q1;
    m += dppf<0xB1>(m); v += dppf<0xB1>(v); m += dppf<0x4E>(m); v += dppf<0x4E>(v); m += dppf<0x141>(m); v += dppf<0x141>(v);
    m *= (1.0f / D);
    const float r = __builtin_amdgcn_rsqf(fmaxf(v * (1.0f / D) - m * m, 0.f) + 1e-5f);
    tag += 1u;
    if (c == 0) bst1(rs, O_STG + row * 16, v4u{f2u(m), tag, f2u(r), tag});
    SB();
#pragma unroll
    for (int i = 2; i < 5; ++i)
#pragma unroll
      for (int j = 0; j < 8; ++j) wv[i][j] = NTL((const unsigned*)(wsrc + (size_t)(128 * i + j) * F2));
  } else if (a < 84) {
    const int rg = tid >> 3, cl = tid & 7, col0 = (a < 74 ? 2 * D + 64 * (a - 64) : 5 * D + 64 * (a - 74));
    unsigned* sL = (unsigned*)(smem + 12288);
    v4u mv[20];
    const v2u cv = *(const v2u*)(p.cvec + 4 * min(tid, 159));
#pragma unroll
    for (int i = 0; i < 20; ++i) mv[i] = __builtin_nontemporal_load((const v4u*)(p.w_mod + (size_t)(20 * rg + i) * 3840 + col0 + 8 * cl));
    tag = __hip_atomic_load((GAS unsigned*)(p.ws + O_EPOCH), RLX_AGENT);
    SB();
    if (tid < 160) *(v4u*)(sL + 4 * tid) = v4u{silu_bf(bflo(cv[0])), silu_bf(bfhi(cv[0])), silu_bf(bflo(cv[1])), silu_bf(bfhi(cv[1]))};
    __syncthreads();
    v4u s4[5];
#pragma unroll
    for (int i = 0; i < 5; ++i) s4[i] = *(const v4u*)(sL + 20 * rg + 4 * i);
    float acc[8];
#pragma unroll
    for (int j = 0; j < 8; ++j) acc[j] = 0.f;
#pragma unroll
    for (int i = 0; i < 20; ++i) {
      const unsigned slo = s4[i >> 2][i & 3], shi = slo << 16;
#pragma unroll
      for (int j = 0; j < 4; ++j) { acc[2 * j] = dot2(mv[i][j], slo, acc[2 * j]); acc[2 * j + 1] = dot2(mv[i][j], shi, acc[2 * j + 1]); }
    }
    const float s = col_reduce(fl, acc, tid);
    tag += 1u;
    if ((tid & 3) == 0) bst1_64(rs, O_MODG + (col0 + (tid >> 2)) * 8, v2u{f2u(rbf(s)), tag});
  } else {
    tag = __hip_atomic_load((GAS unsigned*)(p.ws + O_EPOCH), RLX_AGENT) + 1u;
    if (a == 92) {
      if (tid < 192) __hip_atomic_store((GAS unsigned*)(p.ws + O_CNT) + tid * 64, 0u, RLX_AGENT);
      __hip_atomic_store((GAS unsigned*)(p.ws + O_FLAG) + tid * 64, 0u, RLX_AGENT);
      DRAIN();
      __syncthreads();
      if (tid == 0) bst1_64(rs, O_INIT, v2u{0u, tag});
    }
  }
  STAMP(1);
  if (a < 54) {
    float* c1L = (float*)(smem + 61440);
    u16* sbL = (u16*)(smem + 61440 + 2560);
    float* red = (float*)(smem + 57344);
    unsigned char* stg = smem + 16384;
    const int fp = tid & 15, rgp = tid >> 4;
    unsigned tA[5][4], tB[5][4];
#pragma unroll
    for (int i = 0; i < 5; ++i)
#pragma unroll
      for (int j = 0; j < 4; ++j) { tA[i][j] = plo(wv[i][2 * j], wv[i][2 * j + 1]); tB[i][j] = phi(wv[i][2 * j], wv[i][2 * j + 1]); PIN(tA[i][j]); PIN(tB[i][j]); }
#pragma unroll 1
    for (unsigned s = 0; s < SPIN; ++s) {
      asm volatile("" ::: "memory");
      v4u v[3][4];
#pragma unroll
      for (int i = 0; i < 3; ++i)
#pragma unroll
        for (int k = 0; k < 4; ++k) v[i][k] = bld1(rs, min(256 * i + tid, 639) * 16, O_MODP + (k * 2560 + 1280) * 8);
      const u64 vi = __hip_atomic_load((GAS u64*)(p.ws + O_INIT), RLX_AGENT);
      unsigned bad = (unsigned)(vi >> 32) ^ tag;
#pragma unroll
      for (int i = 0; i < 3; ++i) {
        const int e = min(256 * i + tid, 639);
#pragma unroll
        for (int k = 0; k < 4; ++k) bad |= (v[i][k][1] ^ tag) | (v[i][k][3] ^ tag);
        const unsigned pr = pk_bf16((u2f(v[i][0][0]) + u2f(v[i][1][0])) + (u2f(v[i][2][0]) + u2f(v[i][3][0])), (u2f(v[i][0][2]) + u2f(v[i][1][2])) + (u2f(v[i][2][2]) + u2f(v[i][3][2])));
        if (e < 320) *(unsigned*)(sbL + 2 * e) = pr; else *(f32x2*)(c1L + 2 * (e - 320)) = f32x2{rbf(1.0f + bflo(pr)), rbf(1.0f + bfhi(pr))};
      }
      PIN(bad);
      if (__all(bad == 0u)) break;
      __builtin_amdgcn_s_sleep(1);
    }
    ORDER();
    __syncthreads();
    float u1a = 0.f, u1b = 0.f, u2a = 0.f, u2b = 0.f;
#pragma unroll
    for (int i = 0; i < 5; ++i) {
      const int e = rgp + 16 * i;
      const f32x4 ca = *(const f32x4*)(c1L + 8 * e), cb = *(const f32x4*)(c1L + 8 * e + 4);
      const v4u sb = *(const v4u*)(sbL + 8 * e);
      unsigned ra[4], rb[4];
#pragma unroll
      for (int j = 0; j < 4; ++j) {
        const f32x2 cc = j == 0 ? f32x2{ca[0], ca[1]} : j == 1 ? f32x2{ca[2], ca[3]} : j == 2 ? f32x2{cb[0], cb[1]} : f32x2{cb[2], cb[3]};
        const f32x2 ya = unp(tA[i][j]) * cc, yb = unp(tB[i][j]) * cc;
        ra[j] = pk_bf16(ya[0], ya[1]); rb[j] = pk_bf16(yb[0], yb[1]);
        u1a = dot2(ra[j], ONES, u1a); u1b = dot2(rb[j], ONES, u1b);
        u2a = dot2(tA[i][j], sb[j], u2a); u2b = dot2(tB[i][j], sb[j], u2b);
      }
      unsigned char* dst = stg + ((fp >> 3) * 20 + (e >> 2)) * 1024 + ((e & 3) * 16 + 2 * (fp & 7)) * 16;
      *(v4u*)dst = v4u{ra[0], ra[1], ra[2], ra[3]};
      *(v4u*)(dst + 16) = v4u{rb[0], rb[1], rb[2], rb[3]};
    }
    *(f32x4*)(red + (rgp * 16 + fp) * 4) = f32x4{u1a, u2a, u1b, u2b};
    __syncthreads();
#pragma unroll
    for (int i = 0; i < 10; ++i) bst1(rs, O_WINF + a * 40960 + i * 4096 + tid * 16, *(const v4u*)(stg + i * 4096 + tid * 16));
    if (tid < 16) {
      f32x4 s = {0.f, 0.f, 0.f, 0.f};
#pragma unroll
      for (int r = 0; r < 16; ++r) s += *(const f32x4*)(red + r * 64 + tid * 4);
      bst1(rs, O_U + a * 256 + tid * 16, __builtin_bit_cast(v4u, s));
    }
  } else {
#pragma unroll 1
    for (unsigned s = 0; s < SPIN; ++s) {
      const u64 v = __hip_atomic_load((GAS u64*)(p.ws + O_INIT), RLX_AGENT);
      if (__all((unsigned)(v >> 32) == tag)) break;
      __builtin_amdgcn_s_sleep(1);
    }
    ORDER();
    if (a == 54) {
#pragma unroll
      for (int i = 0; i < 5; ++i) bst1(rs, O_WINF + 108 * 20480 + i * 4096 + tid * 16, v4u{ONES, ONES, ONES, ONES});
    }
    const int wi = (a - 56) * 4 + w;
    if (a >= 56 && wi < 135) wout_unit(p, rs, smem + 16384 + w * 8192, wi, lane);
  }
  DRAIN();
  __syncthreads();
  arrive(p, a < 56 ? C_W : C_S, tid);
  STAMP(2);
}

template <int CTRL> DEVI float dpp_add(float keep, float send) { return keep + dppf<CTRL>(send); }
DEVI float bfly16(const float (&v)[16], int i16) {
  float a[8], b[4], c[2];
  const bool h8 = i16 & 8, h4 = i16 & 4, h2 = i16 & 2, h1 = i16 & 1;
#pragma unroll
  for (int k = 0; k < 8; ++k) a[k] = dpp_add<0x128>(h8 ? v[k + 8] : v[k], h8 ? v[k] : v[k + 8]);
#pragma unroll
  for (int k = 0; k < 4; ++k) b[k] = dpp_add<0x141>(h4 ? a[k + 4] : a[k], h4 ? a[k] : a[k + 4]);
#pragma unroll
  for (int k = 0; k < 2; ++k) c[k] = dpp_add<0x4E>(h2 ? b[k + 2] : b[k], h2 ? b[k] : b[k + 2]);
  return dpp_add<0xB1>(h1 ? c[1] : c[0], h1 ? c[0] : c[1]);
}
DEVI f32x2 gelu2(f32x2 v) {
  const float c0 = -2.0f * 0.7978845608028654f * 1.4426950408889634f, c1 = c0 * 0.044715f;
  const f32x2 u = v * (v * v * c1 + c0);
  const f32x2 d = f32x2{__builtin_amdgcn_exp2f(u[0]), __builtin_amdgcn_exp2f(u[1])} + 1.0f;
  return v * f32x2{__builtin_amdgcn_rcpf(d[0]), __builtin_amdgcn_rcpf(d[1])};
}
__global__ void __launch_bounds__(256, 1) k_fused(Params p) {
  extern __shared__ __attribute__((aligned(16))) unsigned char smem[];
  const int tid = threadIdx.x, lane = tid & 63, w = __builtin_amdgcn_readfirstlane(tid >> 6);
  const int i16 = lane & 15, g = lane >> 4, l16 = lane * 16;
  const int bx = blockIdx.x & 7, bi = blockIdx.x >> 3;
  const rsrc_t rs = __builtin_amdgcn_make_buffer_rsrc((void*)p.ws, 0, O_END, 0x00020000);
  const rsrc_t rw = __builtin_amdgcn_make_buffer_rsrc((void*)p.w_sp, 0, T * T * 2, 0x00020000);
  u64 ts_[20];
  STAMP(0);
  unsigned tag;
  const unsigned lds0 = (unsigned)(size_t)(LAS unsigned char*)smem;
  u64 pc_; unsigned csz_;
  asm volatile("s_getpc_b64 %0\n.Lkf_beg:\n\ts_mov_b32 %1, .Lkf_end-.Lkf_beg" : "=s"(pc_), "=s"(csz_));
  auto code_prefetch = [&]() __attribute__((always_inline)) {
    const rsrc_t rc = __builtin_amdgcn_make_buffer_rsrc((void*)(pc_ & ~15ull), 0, (int)csz_, 0x00020000);
    if (w < 3) dma16(rc, l16, (bi * 3 + w) * 1024, lds0 + L_DUMMY + w * 1024);
  };
  const bool is_conv = bi < 20;
  const int q4 = bx * 20 + bi;
  const int panel = (bx >> 1) * 8 + (bi >> 2), j = (bx & 1) * 4 + (bi & 3);
  const int b0 = blk0(j);
  const int lb0 = (w < 3) ? 2 * w : 6, lb1 = 2 * w + 1;
  const bool has0 = (w < 3) || (j < 6), has1 = (w < 3);
  const int z2s0 = has0 ? O_WINF + (54 + b0 + lb0) * 20480 : OOB, z2s1 = O_WINF + (has1 ? 54 + b0 + lb1 : 108) * 20480;
  const int z1s0 = has0 ? O_WINF + (b0 + lb0) * 20480 : OOB, z1s1 = has1 ? O_WINF + (b0 + lb1) * 20480 : OOB;
  const int rot = ((panel & ROTM) * ROTS + (j & 3) * 4) & 15;
  const int wvo = (8 * (lane >> 4) + ((lane >> 2) & 3)) * 4096 + (((lane & 3) ^ ((lane >> 4) & 3)) * 16);
  const int wso = (64 * panel + 32 * (w >> 1) + 4 * (w & 1)) * 4096 + 16 * w * 64;
#define WSP_DMA(step, tb, ldsoff) dma16(rw, wvo, (wso ^ ((32 * ((tb) >> 1) + 4 * ((tb) & 1)) * 4096)) + ((((step) + rot) & 15) * 64), lds0 + (ldsoff) + w * 16384 + ((((step) & 3) * 4 + (tb)) * 1024))
#define WSD_DMA(q) dma16(rw, wvo, (wso ^ ((32 * (((q) & 3) >> 1) + 4 * ((q) & 1)) * 4096)) + (((12 + ((q) >> 2) + rot) & 15) * 64), lds0 + ((q) * 4 + w) * 1024)
  const int wrd = w * 16384 + (i16 * 4 + (g ^ ((i16 >> 2) & 3))) * 16;
  const int zu0 = has0 ? O_Z2N + (2 * panel) * 1024 + (b0 + lb0) * 65536 : OOB, zu1 = has1 ? O_Z2N + (2 * panel) * 1024 + (b0 + lb1) * 65536 : OOB;
  const int pu = O_P + ((4 * panel) * 27 + (w < 3 ? 3 * j + w : 24 + (j >> 1))) * 1024;
  const int pu16 = w < 3 ? pu : OOB, pu8 = (w == 3 && j < 6) ? pu + (j & 1) * 8 : OOB;
  v4u WR[32];
#define WR_RD(q, dst) WR[dst] = *(const v4u*)(smem + L_WL + wrd + (q) * 1024)
  v4u R[40];
#define R_LD(q, s0, s1) R[q] = bld1(rs, l16 + (((q) >> 1) & 3) * 1024, (((q) & 1) ? (s1) : (s0)) + ((q) >> 3) * 4096)
  bool rdy = false;
  auto ld_w24 = [&](int s0, int s1, int su) __attribute__((always_inline)) {
    const rsrc_t rb = __builtin_amdgcn_make_buffer_rsrc((void*)p.b_sp, 0, T * 2, 0x00020000);
    if (w < 3) dma16s(rs, l16, su, lds0 + L_DUMMY + w * 1024);
    else dma16(rb, l16, 128 * panel, lds0 + L_DUMMY + 3072);
#pragma unroll
    for (int q = 0; q < 24; ++q) R_LD(q, s0, s1);
    SB();
  };
  const int uso = w == 0 ? O_U + (FH + 16 * b0) * 8 : (w == 1 ? O_U + 16 * b0 * 8 : OOB);
  if (__builtin_expect(is_conv, 1)) {
    const int d = 4 * q4 + w;
    unsigned par[6];
    par[0] = NTL((const unsigned*)(p.a_re + d * NST + 2 * lane)); par[1] = NTL((const unsigned*)(p.a_im + d * NST + 2 * lane));
    par[2] = NTL((const unsigned*)(p.b_re + d * NST + 2 * lane)); par[3] = NTL((const unsigned*)(p.b_im + d * NST + 2 * lane));
    par[4] = NTL((const unsigned*)(p.c_re + d * NST + 2 * lane)); par[5] = NTL((const unsigned*)(p.c_im + d * NST + 2 * lane));
    const float ldt = bf2f(p.log_dt[d]);
    SB();
    v4u mv[5]; unsigned cv[5];
    const int mcg = q4 >> 2, mkq = q4 & 3;
#pragma unroll
    for (int i = 0; i < 5; ++i) {
      const int row = 160 * mkq + 32 * i + (tid >> 3);
      mv[i] = __builtin_nontemporal_load((const v4u*)(p.w_mod + (size_t)row * 3840 + 64 * mcg + (mcg >= 20 ? D : 0) + 8 * (tid & 7)));
      cv[i] = p.cvec[row];
    }
    const unsigned ep = __hip_atomic_load((GAS unsigned*)(p.ws + O_EPOCH), RLX_AGENT);
    const v2u dkv = *(const v2u*)(p.dskip + 4 * q4);
    v2u xa[8];
    v4u st[4][2]; u64 mg;
    const int mo = lane < 32 ? O_MODP + ((lane & 3) * 2560 + (lane >> 4) * D + 4 * q4 + ((lane >> 2) & 3)) * 8 : O_INIT;
    auto gather = [&]() __attribute__((always_inline)) {
      mg = __hip_atomic_load((GAS u64*)(p.ws + mo), RLX_AGENT);
#pragma unroll
      for (int i = 0; i < 4; ++i) { st[i][0] = bld1(rs, O_STG + (256 * i + tid) * 32); st[i][1] = bld1(rs, O_STG + (256 * i + tid) * 32 + 16); }
    };
    const rsrc_t ru = __builtin_amdgcn_make_buffer_rsrc((void*)p.u, 0, T * D * 2, 0x00020000);
    auto xld = [&](int i) __attribute__((always_inline)) { xa[i] = __builtin_amdgcn_raw_buffer_load_b64(ru, tid * (4 * D), (512 * (i >> 1) + (i & 1)) * (2 * D) + 8 * q4, 0); };
    auto hook = [&](int i) __attribute__((always_inline)) {
      SB();
      if (i == MODHOOK) {
        tag = ep + 1u;
        float acc[8];
#pragma unroll
        for (int k = 0; k < 8; ++k) acc[k] = 0.f;
#pragma unroll
        for (int q = 0; q < 5; ++q) {
          const unsigned slo = silu_bf(bf2f(cv[q])), shi = slo << 16;
#pragma unroll
          for (int k = 0; k < 4; ++k) { acc[2 * k] = dot2(mv[q][k], slo, acc[2 * k]); acc[2 * k + 1] = dot2(mv[q][k], shi, acc[2 * k + 1]); }
        }
        const float s = col_reduce((float*)(smem + L_WL), acc, tid);
        if ((tid & 3) == 0) bst1_64(rs, O_MODP + (mkq * 2560 + 64 * mcg + (tid >> 2)) * 8, v2u{f2u(s), tag});
        SB();
        code_prefetch();
      }
      if (i == 2) xld(2);
      if (i == 4) { xld(3); xld(4); }
      if (i >= 5 && i <= 7) xld(i);
      if (i == 9) gather();
      SB();
    };
    SB();
    xld(0); xld(1);
    SB();
    u16* himg = (u16*)(smem + L_HIMG);
    if (tid < 128) *(v4u*)(himg + (tid >> 5) * HL + (tid & 31) * 8) = v4u{0, 0, 0, 0};
    const float dt = rbf(__builtin_amdgcn_exp2f(ldt * 1.4426950408889634f));
    kgen_wave(par, dt, (unsigned*)(smem + L_KSCR) + w * 16 * RS, smem + L_KIMG + w * KIMG_B, lane, hook);
    STAMP(1);
#pragma unroll 1
    for (unsigned s = 0; s < SPIN; ++s) {
      unsigned bad = (unsigned)(mg >> 32) ^ tag;
#pragma unroll
      for (int i = 0; i < 4; ++i) bad |= (st[i][0][1] ^ tag) | (st[i][0][3] ^ tag) | (st[i][1][1] ^ tag) | (st[i][1][3] ^ tag);
      PIN(bad);
      if (__all(bad == 0u)) break;
      asm volatile("" ::: "memory");
      gather();
    }
    ORDER();
    STAMP(2);
    float sa[4], c1[4];
    {
      float ms = u2f((unsigned)mg);
      ms += dppf<0xB1>(ms); ms += dppf<0x4E>(ms);
      const unsigned mr = f2u(rbf(ms));
#pragma unroll
      for (int c = 0; c < 4; ++c) {
        sa[c] = u2f(__builtin_amdgcn_readlane(mr, 4 * c));
        c1[c] = rbf(1.0f + u2f(__builtin_amdgcn_readlane(mr, 16 + 4 * c)));
      }
    }
    unsigned hv[4][4];
#pragma unroll
    for (int i = 0; i < 4; ++i) {
      const int pr = 256 * i + tid;
      const f32x2 m = {u2f(st[i][0][0]), u2f(st[i][1][0])}, r = {u2f(st[i][0][2]), u2f(st[i][1][2])};
      const f32x2 x[4] = {f32x2{bflo(xa[2 * i][0]), bflo(xa[2 * i + 1][0])}, f32x2{bfhi(xa[2 * i][0]), bfhi(xa[2 * i + 1][0])},
                          f32x2{bflo(xa[2 * i][1]), bflo(xa[2 * i + 1][1])}, f32x2{bfhi(xa[2 * i][1]), bfhi(xa[2 * i + 1][1])}};
#pragma unroll
      for (int c = 0; c < 4; ++c) {
        const f32x2 h = (x[c] - m) * r * c1[c] + sa[c];
        hv[i][c] = pk_bf16(h[0], h[1]);
        *(unsigned*)(himg + c * HL + 256 + 2 * pr) = hv[i][c];
      }
    }
    unsigned ab[8];
#pragma unroll
    for (int a = 0; a < 8; ++a) { ab[a] = lds0 + L_KIMG + w * KIMG_B + (i16 & 1) * (KL * 2) + (2032 - (i16 & ~1) - 2 * (i16 & 1) + 8 * g) * 2 - 64 * (8 * a + 7); PIN(ab[a]); }
    unsigned bb = lds0 + L_HIMG + (w * HL + 256 - 16 + 16 * i16 + 8 * g) * 2 - 64 * 7; PIN(bb);
    __syncthreads();
    STAMP(3);
    f32x4 acc[8];
#pragma unroll
    for (int c = 0; c < 8; ++c) acc[c] = f32x4{0.f, 0.f, 0.f, 0.f};
    v4u O[2][16];
#define CLOAD1(buf, r, k) { if constexpr (CL[k] < 8) { const LAS unsigned* ap = (const LAS unsigned*)(size_t)ab[CL[k] & 7] + 16 * (7 - (r)); O[buf][CL[k]] = v4u{ap[0], ap[1], ap[2], ap[3]}; } \
                            else O[buf][CL[k]] = *((const LAS v4u*)(size_t)bb + 4 * (8 * (CL[k] - 8) + 7 - (r))); }
    sfor<16>(LAM { constexpr int k = ic_; CLOAD1(0, 0, k); });
    SB();
    unsigned cav = 0;
    u64 gav = 0;
    GAS u64* gap = (GAS u64*)(p.ws + O_MODG) + 2 * D + 4 * q4 + (lane & 3);
    sfor<8 * 36>(LAM {
      constexpr int r = ic_ / 36, m = ic_ % 36;
      if constexpr (m == 0) TS(r);
      acc[CA[m] + CB[m]] = mfma_bf(O[r & 1][CA[m]], O[r & 1][8 + CB[m]], acc[CA[m] + CB[m]]);
      if constexpr (r < 7 && (m & 1) == 0 && m < 32) CLOAD1((r + 1) & 1, r + 1, m >> 1);
      if constexpr ((ic_ % ASP) == 5 && ic_ / ASP < 16) WSP_DMA((ic_ / ASP) >> 2, (ic_ / ASP) & 3, L_WL);
      if constexpr (r == 6 && m == 21) gav = __hip_atomic_load(gap, RLX_AGENT);
      if constexpr (r == 7 && m == 33) cav = cnt_load(p, C_W, lane);
      SB();
    });
    TS(8); TSDUMP(0, 9);
    u16* ybuf = (u16*)(smem + L_KSCR);
#pragma unroll
    for (int c = 0; c < 8; ++c)
      *(v2u*)(ybuf + w * T + 256 * c + 16 * i16 + 4 * g) = v2u{pk_bf16(acc[c][0], acc[c][1]), pk_bf16(acc[c][2], acc[c][3])};
    __syncthreads();
    STAMP(4);
#pragma unroll 1
    for (unsigned s = 0; s < SPIN; ++s) {
      if (__all((unsigned)(gav >> 32) == tag)) break;
      gav = __hip_atomic_load(gap, RLX_AGENT);
    }
    float ga[4];
#pragma unroll
    for (int c = 0; c < 4; ++c) ga[c] = u2f(__builtin_amdgcn_readlane((unsigned)gav, c));
    const float dk[4] = {bflo(dkv[0]), bfhi(dkv[0]), bflo(dkv[1]), bfhi(dkv[1])};
#pragma unroll
    for (int i = 0; i < 4; ++i) {
      const int pr = 256 * i + tid;
      const f32x2 x[4] = {f32x2{bflo(xa[2 * i][0]), bflo(xa[2 * i + 1][0])}, f32x2{bfhi(xa[2 * i][0]), bfhi(xa[2 * i + 1][0])},
                          f32x2{bflo(xa[2 * i][1]), bflo(xa[2 * i + 1][1])}, f32x2{bfhi(xa[2 * i][1]), bfhi(xa[2 * i + 1][1])}};
      f32x2 o[4];
#pragma unroll
      for (int c = 0; c < 4; ++c) o[c] = x[c] + (unp(*(const unsigned*)(ybuf + c * T + 2 * pr)) + unp(hv[i][c]) * dk[c]) * ga[c];
      bst1(rs, O_X1G + (q4 * T + 2 * pr) * 8, v4u{(pk_bf16(o[0][0], o[1][0])), (pk_bf16(o[2][0], o[3][0])), (pk_bf16(o[0][1], o[1][1])), (pk_bf16(o[2][1], o[3][1]))});
    }
    SB();
    rdy = __all(cav >= 7u);
    ORDER();
    STAMP(5);
  } else {
    aux_role(p, rs, smem, (bi - 20) * 8 + bx, tag);
    code_prefetch();
#pragma unroll
    for (int q = 0; q < 16; ++q) WSP_DMA(q >> 2, q & 3, L_WL);
    cnt_wait<4>(p, C_W, 7u, lane);
    ld_w24(z2s0, z2s1, uso);
  }
#pragma unroll
  for (int q = 0; q < 16; ++q) WR_RD(q, q);
  int otab;
  {
    const int lf = lane & 7;
    int ab = 2 * w + lf; ab = ab >= 7 ? ab - 7 : ab;
    const int zo = (ab == 6 && j >= 6) ? OOB : O_Z2N + (b0 + ab) * 65536 + 16 * w * 1024;
    const int sl = (ab * 3 + ((w - (ab >> 1) - 1) & 3)) * 4096;
    const int ro = (sl < 65536 ? L_WL + sl : sl) + ((w ^ (ab >> 1)) << 10);
    const int n = 6 * w + lf - 1;
    const int ko = (lf == 0 ? 3 * j + min(w, 2) : (n < 3 * j ? n : n + 3)) * 1024;
    const int lt = lane & 3, tb = lt ^ w;
    const int po = (lane & 4) ? (tb * 3 + lt - 1) * 5120 : O_P + (4 * panel + tb) * (27 * 1024);
    const int sel = (lane >> 3) & 3;
    otab = sel == 0 ? zo : (sel == 1 ? ro : (sel == 2 ? ko : po));
    PIN(otab);
  }
  asm volatile("s_waitcnt lgkmcnt(0)" ::: "memory");
#pragma unroll
  for (int q = 0; q < NB0; ++q) WSP_DMA(4 + (q >> 2), q & 3, L_WL);
  if (is_conv) {
    asm volatile("s_waitcnt vmcnt(%0)" :: "n"(NB0) : "memory");
    __syncthreads();
    arrive(p, C_X1, tid);
    if (!rdy) cnt_wait<4>(p, C_W, 7u, lane);
    ld_w24(z2s0, z2s1, uso);
  }
#pragma unroll
  for (int q = NB0; q < 16; ++q) WSP_DMA(4 + (q >> 2), q & 3, L_WL);
  SB();
  STAMP(6);
  const int xoff = ((lane >> 5) * T + ((2 * (lane & 31)) ^ (4 * (w & 1)))) * 8;
  const int xso = O_X1G + (2 * w * T + 64 * panel) * 8;
#define X_DMA(ks) dma16s(rs, xoff, xso + (ks) * (8 * T * 8), lds0 + ((ks) * 4 + w) * 1024)
  float* sxL = (float*)(smem + L_MISC);
  float* st1 = (float*)(smem + L_MISC + 512) + w * 128;
  float* gsum = (float*)(smem + L_MISC + 2560);
  float* st2 = (float*)(smem + L_MISC + 4608) + w * 128;
  const unsigned xrb = lds0 + g * 1024 + (8 * (i16 >> 2) + (i16 & 3) + 4 * (g & 1)) * 8;
  unsigned xl[4], xh[4];
#pragma unroll
  for (int i = 0; i < 4; ++i) { xl[i] = xrb ^ (((i ^ w) & 1) * 32) ^ (((i ^ w) >> 1) * 256); xh[i] = xl[i] + 512; PIN(xl[i]); PIN(xh[i]); }
  auto ldx = [&](unsigned lo, unsigned hi, int off) __attribute__((always_inline)) {
    const v2u a = *(const LAS v2u*)(size_t)(lo + off), c = *(const LAS v2u*)(size_t)(hi + off);
    return v4u{a[0], a[1], c[0], c[1]};
  };
  f32x4 a0[4], a1[4], gram = {0.f, 0.f, 0.f, 0.f};
#pragma unroll
  for (int i = 0; i < 4; ++i) { a0[i] = f32x4{0.f, 0.f, 0.f, 0.f}; a1[i] = a0[i]; }
  if (w == 3) cnt_wait<2>(p, C_X1, 20u, lane);
  __syncthreads();
  STAMP(7);
#pragma unroll
  for (int ks = 0; ks < 8; ++ks) X_DMA(ks);
  asm volatile("s_waitcnt vmcnt(4)\n\ts_barrier" ::: "memory");
  STAMP(8);
  {
    v4u A[3][4];
    sfor<8>(LAM { A[ic_ >> 2][ic_ & 3] = ldx(xl[ic_ & 3], xh[ic_ & 3], (ic_ >> 2) * 4096); SB(); });
    sfor<20 * 9>(LAM {
      constexpr int ks = ic_ / 9, m = ic_ % 9;
      if constexpr (m == 0 && (ks & 1) == 0) TS(ks >> 1);
      if constexpr (m == 0 && ks == 2) asm volatile("s_waitcnt vmcnt(6)\n\ts_barrier" ::: "memory");
      if constexpr (m == 0 && (ks == 6 || ks == 10)) asm volatile("s_waitcnt vmcnt(12)\n\ts_barrier" ::: "memory");
      if constexpr (m == 0 && ks == 8) { sfor<4>([&](auto i_) __attribute__((always_inline)) { xl[i_] += 40960; xh[i_] += 40960; PIN(xl[i_]); PIN(xh[i_]); }); }
      if constexpr (m == 0) __builtin_amdgcn_s_waitcnt(0xC87F);
      if constexpr (m == 2) gram = mfma_bf(A[ks % 3][0], A[ks % 3][0], gram);
      else {
        constexpr int mm = m < 2 ? m : m - 1;
        if constexpr (mm & 1) a1[mm >> 1] = mfma_bf(A[ks % 3][mm >> 1], R[2 * ks + 1], a1[mm >> 1]);
        else a0[mm >> 1] = mfma_bf(A[ks % 3][mm >> 1], R[2 * ks], a0[mm >> 1]);
      }
      if constexpr ((m & 1) == 1 && ks < 18) A[(ks + 2) % 3][m >> 1] = ldx(xl[m >> 1], xh[m >> 1], (ks + 2 - (ks >= 8 ? 10 : 0)) * 4096);
      if constexpr (ks >= 18 && m < 8) WR_RD(8 * (ks - 18) + m, 16 + 8 * (ks - 18) + m);
      if constexpr ((m == 2 || m == 4 || m == 8) && ks < 4) X_DMA(8 + 3 * ks + (m >> 2));
      if constexpr (m == 4 && ks >= 4) { if constexpr (ks < 12) R_LD(16 + 2 * ks, z2s0, z2s1); else R_LD(2 * (ks - 12), z1s0, z1s1); }
      if constexpr (m == 8 && ks >= 4) { if constexpr (ks < 12) R_LD(17 + 2 * ks, z2s0, z2s1); else R_LD(2 * (ks - 12) + 1, z1s0, z1s1); }
      SB();
    });
    TS(10); TSDUMP(16, 11);
  }
  if (w == 3) {
    if (i16 == 0) {
#pragma unroll
      for (int i = 0; i < 4; ++i) *(f32x4*)(sxL + (i ^ 3) * 16 + 4 * g) = a1[i];
    }
#pragma unroll
    for (int i = 0; i < 4; ++i) a1[i] = f32x4{0.f, 0.f, 0.f, 0.f};
  }
  if ((i16 >> 2) == g) sxL[64 + w * 16 + i16] = (i16 & 3) == 0 ? gram[0] : (i16 & 3) == 1 ? gram[1] : (i16 & 3) == 2 ? gram[2] : gram[3];
  __syncthreads();
  STAMP(9);
  {
    const float m = sxL[lane] * (1.0f / D);
    const float var = fmaxf(sxL[64 + lane] * (1.0f / D) - m * m, 0.f);
    const float r = __builtin_amdgcn_rsqf(var + 1e-5f);
    st1[lane] = r; st1[64 + lane] = r * m;
  }
  v4u A1[3][4];
  {
    const f32x2* cU2 = (const f32x2*)(smem + L_DUMMY);
    f32x2 u0 = cU2[16 * lb0 + i16], u1 = cU2[16 * lb1 + i16];
    if (!has0) u0 = f32x2{0.f, 0.f};
    if (!has1) u1 = f32x2{0.f, 0.f};
    float s1[16], s2[16];
    f32x4 rr[4], rm[4];
#pragma unroll
    for (int i = 0; i < 4; ++i) { rr[i] = *(const f32x4*)(st1 + (i ^ w) * 16 + 4 * g); rm[i] = *(const f32x4*)(st1 + 64 + (i ^ w) * 16 + 4 * g); }
    sfor<8>(LAM {
      constexpr int i = ic_ >> 1, h = ic_ & 1;
      R_LD(16 + 3 * ic_, z1s0, z1s1); R_LD(17 + 3 * ic_, z1s0, z1s1); R_LD(18 + 3 * ic_, z1s0, z1s1);
      const f32x2 r2 = {rr[i][2 * h], rr[i][2 * h + 1]}, m2 = {rm[i][2 * h], rm[i][2 * h + 1]};
      const f32x2 v0 = gelu2(f32x2{a0[i][2 * h], a0[i][2 * h + 1]} * r2 + (u0[1] - m2 * u0[0]));
      const f32x2 v1 = gelu2(f32x2{a1[i][2 * h], a1[i][2 * h + 1]} * r2 + (u1[1] - m2 * u1[0]));
      a0[i][2 * h] = v0[0]; a0[i][2 * h + 1] = v0[1]; a1[i][2 * h] = v1[0]; a1[i][2 * h + 1] = v1[1];
      f32x2 q1 = v0 + v1, q2 = v0 * v0 + v1 * v1;
      PIN(q1); PIN(q2);
      s1[4 * i + 2 * h] = q1[0]; s1[4 * i + 2 * h + 1] = q1[1]; s2[4 * i + 2 * h] = q2[0]; s2[4 * i + 2 * h + 1] = q2[1];
      SB();
    });
    sfor<4>(LAM { xl[ic_] -= 40960; xh[ic_] -= 40960; PIN(xl[ic_]); PIN(xh[ic_]); A1[0][ic_] = ldx(xl[ic_], xh[ic_], 0); SB(); A1[1][ic_] = ldx(xl[ic_], xh[ic_], 4096); SB(); });
    const float t1 = bfly16(s1, i16), t2 = bfly16(s2, i16);
    const int tp = ((i16 >> 2) ^ w) * 16 + 4 * g + (i16 & 3);
    bst1(rs, O_ZST + (((panel * 4 + w) * 8 + j) * 64 + tp) * 16, v4u{f2u(t1), tag, f2u(t2), tag});
  }
  SB();
  STAMP(10);
  f32x4 c0[4], c1[4];
#pragma unroll
  for (int i = 0; i < 4; ++i) { c0[i] = f32x4{0.f, 0.f, 0.f, 0.f}; c1[i] = c0[i]; }
#define Z1M(ks, m) { if constexpr ((m) == 0 && (ks) >= 2 && (ks) < 19 && (ks) != KA && (ks) != KA + 1) __builtin_amdgcn_s_waitcnt(0xC87F); \
    if constexpr ((m) & 1) c1[(m) >> 1] = mfma_bf(R[2 * (ks) + 1], A1[(ks) % 3][(m) >> 1], c1[(m) >> 1]); else c0[(m) >> 1] = mfma_bf(R[2 * (ks)], A1[(ks) % 3][(m) >> 1], c0[(m) >> 1]); \
    if constexpr (((m) & 1) == 1 && (ks) < 18) A1[((ks) + 2) % 3][(m) >> 1] = ldx(xl[(m) >> 1], xh[(m) >> 1], ((ks) + 2 - ((ks) >= KA ? KA : 0)) * 4096); }
  constexpr int KA = KAV;
  v4u zs[8];
  unsigned csv = 0;
  const int zsoff = O_ZST + ((panel * 4 + w) * 8 * 64) * 16;
  sfor<KA * 8>(LAM {
    constexpr int ks = ic_ / 8, m = ic_ % 8;
    Z1M(ks, m);
    if constexpr (m == 4 && ks == 4) csv = cnt_load(p, C_S, lane);
    if constexpr ((m == 2 || m == 6) && ks < 8) { constexpr int q = 2 * ks + (m == 6); WSP_DMA(8 + (q >> 2), q & 3, L_WL); }
    if constexpr ((m & 1) == 0 && ks >= 8 && ks < 10) { constexpr int jj = 4 * (ks - 8) + (m >> 1); zs[jj] = bld1(rs, l16 + (jj & 3) * 1024, zsoff + (jj >> 2) * 4096); }
    SB();
  });
  v2u x1v[5];
#pragma unroll
  for (int db = 0; db < 5; ++db) {
    const int d = 80 * j + 16 * db + 4 * g;
    x1v[db] = *(const v2u*)(smem + (d >> 2) * 512 + ((32 * (w >> 1) + 8 * (i16 >> 2) + 4 * (w & 1) + (i16 & 3)) ^ (4 * ((d >> 3) & 1))) * 8);
  }
  STAMP(11);
  {
#pragma unroll 1
    for (unsigned s = 0; s < SPIN; ++s) {
      unsigned bad = 0;
#pragma unroll
      for (int jj = 0; jj < 8; ++jj) bad |= (zs[jj][1] ^ tag) | (zs[jj][3] ^ tag);
      PIN(bad);
      if (__all(bad == 0u)) break;
      asm volatile("" ::: "memory");
#pragma unroll
      for (int jj = 0; jj < 8; ++jj) zs[jj] = bld1(rs, l16, zsoff + jj * 1024);
    }
    float a = 0.f, b = 0.f;
#pragma unroll
    for (int jj = 0; jj < 8; ++jj) { a += u2f(zs[jj][0]); b += u2f(zs[jj][2]); }
    *(f32x2*)(gsum + (w * 64 + lane) * 2) = f32x2{a, b};
  }
  if (!__all(csv >= 5u)) cnt_wait<1>(p, C_S, 5u, lane);
  ORDER();
  if (w == 2) dma16s(rs, l16, O_MODG + (5 * D + 80 * j) * 8, lds0 + L_DUMMY + 2048);
  __syncthreads();
  STAMP(12);
  {
    const f32x2 s = (*(const f32x2*)(gsum + lane * 2) + *(const f32x2*)(gsum + 128 + lane * 2)) + (*(const f32x2*)(gsum + 256 + lane * 2) + *(const f32x2*)(gsum + 384 + lane * 2));
    const float m = s[0] * (1.0f / FH);
    const float var = fmaxf(s[1] * (1.0f / FH) - m * m, 0.f);
    const float r = __builtin_amdgcn_rsqf(var + 1e-5f);
    st2[lane] = r; st2[64 + lane] = r * m;
  }
  {
    unsigned o0[4][2], o1[4][2];
#pragma unroll
    for (int i = 0; i < 4; ++i) {
      const f32x4 rr = *(const f32x4*)(st2 + (i ^ w) * 16 + 4 * g), rm = *(const f32x4*)(st2 + 64 + (i ^ w) * 16 + 4 * g);
      const f32x4 n0 = a0[i] * rr - rm, n1 = a1[i] * rr - rm;
      o0[i][0] = pk_bf16(n0[0], n0[1]); o0[i][1] = pk_bf16(n0[2], n0[3]);
      o1[i][0] = pk_bf16(n1[0], n1[1]); o1[i][1] = pk_bf16(n1[2], n1[3]);
    }
    const bool sw = w & 1;
    const int ph = (w >> 1) * 1024;
#pragma unroll
    for (int pp = 0; pp < 2; ++pp) {
      const int e = 2 * pp, o = 2 * pp + 1;
      bst1(rs, zu0 + ((pp * 1024) ^ ph) + l16, sw ? v4u{o0[o][0], o0[o][1], o0[e][0], o0[e][1]} : v4u{o0[e][0], o0[e][1], o0[o][0], o0[o][1]});
      bst1(rs, zu1 + ((pp * 1024) ^ ph) + l16, sw ? v4u{o1[o][0], o1[o][1], o1[e][0], o1[e][1]} : v4u{o1[e][0], o1[e][1], o1[o][0], o1[o][1]});
    }
  }
  sfor<4>(LAM { xl[ic_] += KA * 4096; xh[ic_] += KA * 4096; PIN(xl[ic_]); PIN(xh[ic_]); });
  SB();
  STAMP(13);
  sfor<(20 - KA) * 8>(LAM {
    constexpr int ks = KA + ic_ / 8, m = ic_ % 8;
    Z1M(ks, m);
    SB();
  });
  DRAIN();
  __syncthreads();
  GAS unsigned* flags = (GAS unsigned*)(p.ws + O_FLAG);
  if (tid < 4) __hip_atomic_fetch_or(flags + ((j * 4 + (panel >> 3)) * 4 + tid) * 64, 0x101u << (panel & 7), RLX_AGENT);
  STAMP(14);
  int zof[7], rof[7], kof[7];
#pragma unroll
  for (int q = 0; q < 7; ++q) { zof[q] = __builtin_amdgcn_readlane(otab, q); rof[q] = __builtin_amdgcn_readlane(otab, 8 + q); kof[q] = __builtin_amdgcn_readlane(otab, 16 + q); }
  constexpr int ZD = 4;
  v4u Z[16][7];
#define Z_LD(i, f) Z[i][f] = bld1(rs, l16, zof[f] + (((i) + rot) & 15) * 1024)
  unsigned zmask = 0u, zfv = 0u;
  GAS unsigned* zfp = flags + ((j * 4 + w) * 4 + (panel >> 3)) * 64;
  auto zneed = [&](unsigned bits) __attribute__((always_inline)) {
    if (__builtin_expect((zmask & bits) != bits, 0)) {
#pragma unroll 1
      for (unsigned s = 0; s < SPIN; ++s) {
        zmask = __builtin_amdgcn_readfirstlane(__hip_atomic_load(zfp, RLX_AGENT)) >> (rot >> 1);
        if ((zmask & bits) == bits) break;
        __builtin_amdgcn_s_sleep(1);
      }
    }
    ORDER();
  };
  unsigned z1p[2][4][2];
  {
    const v4u* cU1 = (const v4u*)(smem + L_DUMMY + 1024);
    const v4u ua0 = cU1[8 * lb0 + 2 * g], ua1 = cU1[8 * lb0 + 2 * g + 1], ub0 = cU1[8 * lb1 + 2 * g], ub1 = cU1[8 * lb1 + 2 * g + 1];
    float sr[4], sm[4];
#pragma unroll
    for (int i = 0; i < 4; ++i) { sr[i] = st1[16 * (i ^ w) + i16]; sm[i] = st1[64 + 16 * (i ^ w) + i16]; }
    sfor<8>(LAM {
      constexpr int i = ic_ >> 1, h = ic_ & 1;
      if constexpr (ic_ < 4) { WSD_DMA(4 * ic_); WSD_DMA(4 * ic_ + 1); WSD_DMA(4 * ic_ + 2); WSD_DMA(4 * ic_ + 3); }
      if constexpr (ic_ == ZPOLL - 2) { zfv = __hip_atomic_load(zfp, RLX_AGENT); SB(); }
      if constexpr (ic_ == ZPOLL) { STAMP(15); zmask = __builtin_amdgcn_readfirstlane(zfv) >> (rot >> 1); zneed(ZFIRST); STAMP(16); }
      if constexpr (ic_ >= ZPOLL) {
        constexpr int n = 8 - ZPOLL, k = ic_ - ZPOLL;
#pragma unroll
        for (int q = 14 * k / n; q < 14 * (k + 1) / n; ++q) Z_LD(q / 7, q % 7);
        SB();
      }
      const v4u qa = h ? ua1 : ua0, qb = h ? ub1 : ub0;
      const f32x2 v0 = gelu2(f32x2{c0[i][2 * h], c0[i][2 * h + 1]} * sr[i] + (f32x2{u2f(qa[1]), u2f(qa[3])} - f32x2{u2f(qa[0]), u2f(qa[2])} * sm[i]));
      const f32x2 v1 = gelu2(f32x2{c1[i][2 * h], c1[i][2 * h + 1]} * sr[i] + (f32x2{u2f(qb[1]), u2f(qb[3])} - f32x2{u2f(qb[0]), u2f(qb[2])} * sm[i]));
      z1p[0][i][h] = pk_bf16(v0[0], v0[1]); z1p[1][i][h] = pk_bf16(v1[0], v1[1]);
      PIN(z1p[0][i][h]); PIN(z1p[1][i][h]);
    });
  }
  SB();
  f32x4 acc[7][4];
#pragma unroll
  for (int f = 0; f < 7; ++f)
#pragma unroll
    for (int t = 0; t < 4; ++t) acc[f][t] = f32x4{0.f, 0.f, 0.f, 0.f};
  const int woso = O_WOUTF + (5 * j) * 27 * 1024;
  v4u WO[35];
  v4u WL[4][4];
#define WO_LD(q) WO[q] = bld1(rs, l16, woso + ((q) % 5) * (27 * 1024) + kof[(q) / 5])
#define WL_RD(i, t) WL[(i) & 3][t] = *(const v4u*)(smem + ((i) < 12 ? L_WL + wrd + (((i) & 3) * 4 + (t)) * 1024 : wrd - w * 15360 + (((i) & 3) * 4 + (t)) * 4096))
  sfor<13 * 28>(LAM {
    constexpr int i = ic_ / 28, f = (ic_ % 28) / 4, t = ic_ % 4;
    if constexpr (f == 0 && t == 0) TS(i);
    if constexpr (f == 0 && t == 0 && i < 2) zneed(2u << i);
    if constexpr (f == 0 && t == 0 && i >= 2 && i + ZD < 16 && (i & 1) == 0) zneed(1u << ((i + ZD) >> 1));
    if constexpr (i < 8) acc[f][t] = mfma_bf(Z[i][f], WR[i * 4 + t], acc[f][t]);
    else acc[f][t] = mfma_bf(Z[i][f], WL[i & 3][t], acc[f][t]);
    if constexpr (i < 2 && (t == 0 || t == 2)) { constexpr int q = 2 * f + (t >> 1); Z_LD(2 + 2 * i + q / 7, q % 7); }
    if constexpr (i >= 2 && t == 0) { if constexpr (i + ZD < 16) Z_LD(i + ZD, f); else WO_LD(f); }
    if constexpr (t == 2 && f < 4 && i >= 7 && i < 12) WL_RD(i + 1, f);
    if constexpr (i == 12 && t >= 2 && f < 6) { constexpr int q = 2 * f + t - 2; WL_RD(13 + (q >> 2), q & 3); }
    SB();
  });
  TS(13);
  asm volatile("s_barrier" ::: "memory");
  sfor<7 * 12>(LAM {
    constexpr int fo = ic_ / 12, f = (fo + 2) % 7, i = 13 + (ic_ % 12) / 4, t = ic_ % 4, m = ic_ % 12;
    acc[f][t] = mfma_bf(Z[i][f], WL[i & 3][t], acc[f][t]);
    if constexpr ((m & 3) == 0) WO_LD(7 + 3 * fo + (m >> 2));
    if constexpr (fo >= 1 && fo <= 5 && m % 3 == 2) { constexpr int pf = fo + 1, pt = m / 3; *(f32x4*)(smem + ((rof[pf] + l16) ^ (pt << 10))) = acc[pf][pt]; }
    SB();
  });
  TS(14); TSDUMP(32, 15);
  if (w == 3) {
#pragma unroll
    for (int t = 0; t < 4; ++t) *(f32x4*)(smem + ((rof[1] + l16) ^ (t << 10))) = acc[1][t];
  }
  f32x4 o0[4], o1[4];
#pragma unroll
  for (int t = 0; t < 4; ++t) {
    const float bias = bf2f(((const u16*)(smem + L_DUMMY + 3072))[8 * (i16 >> 2) + (i16 & 3) + 32 * ((t ^ w) >> 1) + 4 * ((t ^ w) & 1)]);
    o0[t] = acc[0][t] + bias; o1[t] = acc[1][t] + bias; PIN(o0[t]); PIN(o1[t]);
  }
  __syncthreads();
  STAMP(17);
  v4u PO[4];
  {
    f32x4 pa[4][3], pb[4][3];
#pragma unroll
    for (int s = 0; s < 3; ++s) {
      const int s0 = (2 * w * 3 + s) * 4096, s1 = ((w < 3 ? 2 * w + 1 : 6) * 3 + s) * 4096;
      const unsigned char* r0 = smem + (s0 < 65536 ? L_WL + s0 : s0) + l16;
      const unsigned char* r1 = smem + (s1 < 65536 ? L_WL + s1 : s1) + l16;
#pragma unroll
      for (int t = 0; t < 4; ++t) { pa[t][s] = *(const f32x4*)(r0 + t * 1024); pb[t][s] = *(const f32x4*)(r1 + t * 1024); }
    }
    SB();
#pragma unroll
    for (int t = 0; t < 4; ++t) {
      const f32x4 x0 = (o0[t] + pa[t][0]) + (pa[t][1] + pa[t][2]), x1 = (o1[t] + pb[t][0]) + (pb[t][1] + pb[t][2]);
      const f32x2 qa = unp(z1p[0][t][0]) * f32x2{x0[0], x0[1]}, qb = unp(z1p[0][t][1]) * f32x2{x0[2], x0[3]};
      const f32x2 qc = unp(z1p[1][t][0]) * f32x2{x1[0], x1[1]}, qd = unp(z1p[1][t][1]) * f32x2{x1[2], x1[3]};
      PO[t] = v4u{pk_bf16(qa[0], qa[1]), pk_bf16(qb[0], qb[1]), pk_bf16(qc[0], qc[1]), pk_bf16(qd[0], qd[1])};
      bst1(rs, pu16 + (t ^ w) * (27 * 1024) + l16, PO[t]);
      bst1_64(rs, pu8 + (t ^ w) * (27 * 1024) + l16, v2u{PO[t][0], PO[t][1]});
    }
  }
  SB();
  STAMP(18);
  f32x4 e[5][4];
#pragma unroll
  for (int d = 0; d < 5; ++d)
#pragma unroll
    for (int t = 0; t < 4; ++t) e[d][t] = f32x4{0.f, 0.f, 0.f, 0.f};
  if (w == 3) {
#pragma unroll
    for (int t = 0; t < 4; ++t) PO[t] = v4u{0u, 0u, 0u, 0u};
  }
#pragma unroll
  for (int t = 0; t < 4; ++t)
#pragma unroll
    for (int d = 0; d < 5; ++d) e[d][t] = mfma_bf(WO[d], PO[t], e[d][t]);
  SB();
  DRAIN();
  __syncthreads();
  if (tid < 4) __hip_atomic_fetch_or(flags + (128 + panel * 4 + tid) * 64, 1u << j, RLX_AGENT);
  SB();
#pragma unroll
  for (int q = 28; q < 35; ++q) WO_LD(q);
  SB();
  const int trow = 64 * panel + 8 * (i16 >> 2) + (i16 & 3) + 32 * (w >> 1) + 4 * (w & 1);
  int pof[4], eof[4];
#pragma unroll
  for (int q = 0; q < 4; ++q) { pof[q] = __builtin_amdgcn_readlane(otab, 24 + q); eof[q] = __builtin_amdgcn_readlane(otab, 28 + q); }
  v4u gb0[5], gb1[5];
#pragma unroll
  for (int d = 0; d < 5; ++d) { gb0[d] = *(const v4u*)(smem + L_DUMMY + 2048 + (16 * d + 4 * g) * 8); gb1[d] = *(const v4u*)(smem + L_DUMMY + 2048 + (16 * d + 4 * g) * 8 + 16); }
  STAMP(19);
  v4u P[24];
#define P_LD(q) P[q] = bld1(rs, l16, pof[((q) / 6 + 1) & 3] + kof[1 + (q) % 6])
  constexpr int PDN = 12;
#pragma unroll 1
  for (unsigned s = 0; s < SPIN; ++s) {
    if (__builtin_amdgcn_readfirstlane(__hip_atomic_load(flags + (128 + panel * 4 + w) * 64, RLX_AGENT)) == 0xffu) break;
    __builtin_amdgcn_s_sleep(1);
  }
  ORDER();
  STAMP(20);
#pragma unroll
  for (int q = 0; q < PDN; ++q) P_LD(q);
  SB();
  sfor<24 * 5>(LAM {
    constexpr int q = ic_ / 5, d = ic_ % 5, lo = q / 6, lt = (lo + 1) & 3, kk = 1 + q % 6;
    e[d][lt] = mfma_bf(WO[5 * kk + d], P[q], e[d][lt]);
    if constexpr (d == 0 && q + PDN < 24) P_LD(q + PDN);
    if constexpr (lo >= 1 && d == 3 && q % 6 < 5) *(f32x4*)(smem + eof[lo] + (q % 6) * 1024 + l16) = e[q % 6][lo];
    SB();
  });
  STAMP(21);
  __syncthreads();
  u16* orow = p.out + (size_t)trow * D + 80 * j + 4 * g;
  f32x4 ep[5][3];
#pragma unroll
  for (int d = 0; d < 5; ++d)
#pragma unroll
    for (int s = 0; s < 3; ++s) ep[d][s] = *(const f32x4*)(smem + ((w * 3 + s) * 5 + d) * 1024 + l16);
  SB();
#pragma unroll
  for (int d = 0; d < 5; ++d) {
    const f32x4 eo = (e[d][0] + ep[d][0]) + (ep[d][1] + ep[d][2]);
    const float r0 = bflo(x1v[d][0]) + u2f(gb0[d][0]) * eo[0], r1 = bfhi(x1v[d][0]) + u2f(gb0[d][2]) * eo[1];
    const float r2 = bflo(x1v[d][1]) + u2f(gb1[d][0]) * eo[2], r3 = bfhi(x1v[d][1]) + u2f(gb1[d][2]) * eo[3];
    *(v2u*)(orow + 16 * d) = v2u{pk_bf16(r0, r1), pk_bf16(r2, r3)};
  }
  if (blockIdx.x == 0 && tid == 0) __hip_atomic_store((GAS unsigned*)(p.ws + O_EPOCH), tag, RLX_AGENT);
  STAMP(22);
  asm volatile(".Lkf_end:");
}

extern "C" void kernel_launch(void* const* d_in, const int* in_sizes, int n_in, void* d_out, int out_size, void* d_ws,
                              size_t ws_size, hipStream_t stream) {
  Params p;
  p.u = (const u16*)d_in[0]; p.a_re = (const u16*)d_in[1]; p.a_im = (const u16*)d_in[2];
  p.b_re = (const u16*)d_in[3]; p.b_im = (const u16*)d_in[4]; p.c_re = (const u16*)d_in[5]; p.c_im = (const u16*)d_in[6];
  p.log_dt = (const u16*)d_in[7]; p.dskip = (const u16*)d_in[8];
  p.w_in = (const u16*)d_in[10]; p.w_sp = (const u16*)d_in[11]; p.b_sp = (const u16*)d_in[12]; p.w_out = (const u16*)d_in[13];
  p.cvec = (const u16*)d_in[14]; p.w_mod = (const u16*)d_in[15];
  p.out = (u16*)d_out; p.ws = (unsigned char*)d_ws;
  (void)hipFuncSetAttribute((const void*)k_fused, hipFuncAttributeMaxDynamicSharedMemorySize, L_TOTAL);
  k_fused<<<256, 256, L_TOTAL, stream>>>(p);
}
```

```cpp
#include <hip/hip_runtime.h>
#include <hip/hip_bf16.h>
#include <stdint.h>
#include <utility>

typedef unsigned v4u __attribute__((ext_vector_type(4)));
typedef unsigned v2u __attribute__((ext_vector_type(2)));
typedef float f32x4 __attribute__((ext_vector_type(4)));
typedef float f32x2 __attribute__((ext_vector_type(2)));
typedef short bf16x8 __attribute__((ext_vector_type(8)));
typedef _Float16 f16x8 __attribute__((ext_vector_type(8)));
typedef _Float16 f16x2 __attribute__((ext_vector_type(2)));
typedef __bf16 bf16x2_t __attribute__((ext_vector_type(2)));
typedef unsigned short u16;
typedef unsigned long long u64;
#define GAS __attribute__((address_space(1)))
#define LAS __attribute__((address_space(3)))
#define RLX_AGENT __ATOMIC_RELAXED, __HIP_MEMORY_SCOPE_AGENT
#define DEVI __device__ __forceinline__
#define SB() __builtin_amdgcn_sched_barrier(0)
template <int... I, class F> __device__ __forceinline__ void sfor_impl(std::integer_sequence<int, I...>, F&& f) { (f(std::integral_constant<int, I>{}), ...); }
template <int N, class F> __device__ __forceinline__ void sfor(F&& f) { sfor_impl(std::make_integer_sequence<int, N>{}, f); }
#define LAM [&](auto ic_) __attribute__((always_inline))
#define NTL(p) __builtin_nontemporal_load(p)
#define DRAIN() asm volatile("s_waitcnt vmcnt(0)" ::: "memory")

constexpr int T = 2048, D = 640, NST = 128, FH = 864, F2 = 1728;
constexpr int KL = 2080, KIMG_B = 2 * KL * 2, HL = 2304;
constexpr unsigned SPIN = 1u << 15;
constexpr int AUX_SC1 = 16, AUX_NT = 2;
constexpr int O_EPOCH = 0;
constexpr int O_INIT = 256;
constexpr int O_CNT = 4096;
constexpr int O_FLAG = O_CNT + 3 * 64 * 256;
constexpr int O_MODG = O_FLAG + 256 * 256;
constexpr int O_MODP = O_MODG + 3840 * 8 + 2048;
constexpr int O_STG = O_MODP + 4 * 2560 * 8;
constexpr int O_U = O_STG + 2048 * 16;
constexpr int O_ZST = O_U + 16384;
constexpr int O_X1G = O_ZST + 32 * 32 * 64 * 16;
constexpr int O_WINF = O_X1G + T * D * 2;
constexpr int O_WOUTF = O_WINF + 109 * 20 * 1024;
constexpr int O_Z2N = O_WOUTF + 40 * 27 * 1024;
constexpr int O_P = O_Z2N + 54 * 64 * 1024;
constexpr int O_END = O_P + 128 * 27 * 1024;
constexpr int C_X1 = 0, C_S = 1, C_F = 2;
constexpr unsigned SENT = 0xffffffffu;

constexpr size_t O_STAMP = 200u << 20;
#define TS(k) do {} while (0)
#define TSDUMP(base, n) do {} while (0)
#define STAMP(idx) do {} while (0)

DEVI float u2f(unsigned u) { return __builtin_bit_cast(float, u); }
DEVI unsigned f2u(float f) { return __builtin_bit_cast(unsigned, f); }
DEVI float bf2f(unsigned b) { return u2f(b << 16); }
DEVI float bflo(unsigned u) { return u2f(u << 16); }
DEVI float bfhi(unsigned u) { return u2f(u & 0xffff0000u); }
DEVI unsigned pk_bf16(float a, float b) {
  f32x2 v = {a, b};
  bf16x2_t r = __builtin_convertvector(v, bf16x2_t);
  return __builtin_bit_cast(unsigned, r);
}
DEVI float rbf(float a) { return bflo(pk_bf16(a, 0.f)); }
DEVI unsigned pk_f16(float a, float b) {
  f32x2 v = {a, b};
  f16x2 r = __builtin_convertvector(v, f16x2);
  return __builtin_bit_cast(unsigned, r);
}
DEVI f32x4 mfma_bf(v4u a, v4u b, f32x4 c) {
  return __builtin_amdgcn_mfma_f32_16x16x32_bf16(__builtin_bit_cast(bf16x8, a), __builtin_bit_cast(bf16x8, b), c, 0, 0, 0);
}
DEVI f32x4 mfma_h(v4u a, v4u b, f32x4 c) {
  return __builtin_amdgcn_mfma_f32_16x16x32_f16(__builtin_bit_cast(f16x8, a), __builtin_bit_cast(f16x8, b), c, 0, 0, 0);
}
template <bool FIRST, bool BA> DEVI void mfma_pin(f32x4& acc, const v4u& a, const v4u& bb) {
  if constexpr (FIRST) {
    if constexpr (BA) asm volatile("v_mfma_f32_16x16x32_bf16 %0, %1, %2, 0" : "=a"(acc) : "v"(a), "a"(bb));
    else asm volatile("v_mfma_f32_16x16x32_bf16 %0, %1, %2, 0" : "=a"(acc) : "v"(a), "v"(bb));
  } else {
    if constexpr (BA) asm volatile("v_mfma_f32_16x16x32_bf16 %0, %1, %2, %0" : "+a"(acc) : "v"(a), "a"(bb));
    else asm volatile("v_mfma_f32_16x16x32_bf16 %0, %1, %2, %0" : "+a"(acc) : "v"(a), "v"(bb));
  }
}
typedef __amdgpu_buffer_rsrc_t rsrc_t;
DEVI v4u bld1(rsrc_t rs, int voff, int soff = 0) { return __builtin_amdgcn_raw_buffer_load_b128(rs, voff, soff, AUX_SC1); }
DEVI void bst1(rsrc_t rs, int voff, v4u v) { __builtin_amdgcn_raw_buffer_store_b128(v, rs, voff, 0, AUX_SC1); }
DEVI void bst1_64(rsrc_t rs, int voff, v2u v) { __builtin_amdgcn_raw_buffer_store_b64(v, rs, voff, 0, AUX_SC1); }
template <int CTRL> DEVI float dppf(float x) { return u2f((unsigned)__builtin_amdgcn_update_dpp(0, (int)f2u(x), CTRL, 0xf, 0xf, true)); }
DEVI float dot2(unsigned a, unsigned b, float c) { return __builtin_amdgcn_fdot2_f32_bf16(__builtin_bit_cast(bf16x2_t, a), __builtin_bit_cast(bf16x2_t, b), c, false); }
DEVI unsigned mx4(unsigned m, v4u v) { return max(max(max(m, v[0]), v[1]), max(v[2], v[3])); }
#define PIN(x) asm volatile("" : "+v"(x))
#define ORDER() __builtin_amdgcn_fence(__ATOMIC_ACQUIRE, "wavefront")

struct Params {
  const u16 *u, *a_re, *a_im, *b_re, *b_im, *c_re, *c_im, *log_dt, *dskip, *w_in, *w_sp, *b_sp, *w_out, *cvec, *w_mod;
  u16* out;
  unsigned char* ws;
};

DEVI int blk0(int j) { return j < 6 ? 7 * j : 42 + 6 * (j - 6); }
DEVI int pair_blk(int ks3, int h) {
  if (ks3 < 24) { const int j = ks3 / 3, w = ks3 - 3 * j; return blk0(j) + 2 * w + h; }
  return 7 * (2 * (ks3 - 24) + h) + 6;
}
DEVI void arrive(const Params& p, int c, int tid) {
  if (tid < 8) __hip_atomic_fetch_add((GAS unsigned*)(p.ws + O_CNT) + ((c * 8 + tid) * 8 + (blockIdx.x & 7)) * 64, 1u, RLX_AGENT);
}
DEVI unsigned cnt_load(const Params& p, int c, int lane) {
  return __hip_atomic_load((GAS unsigned*)(p.ws + O_CNT) + ((c * 8 + ((blockIdx.x >> 3) & 7)) * 8 + (lane & 7)) * 64, RLX_AGENT);
}
template <int SLP> DEVI void cnt_wait(const Params& p, int c, unsigned want, int lane) {
#pragma unroll 1
  for (unsigned s = 0; s < SPIN; ++s) {
    if (__all(cnt_load(p, c, lane) >= want)) break;
    __builtin_amdgcn_s_sleep(SLP);
  }
  ORDER();
}
struct C2 { f32x2 re, im; };
DEVI C2 cmul2(C2 a, C2 b) { return C2{a.re * b.re - a.im * b.im, a.re * b.im + a.im * b.re}; }
DEVI C2 cexp2(f32x2 are2, f32x2 arev, float l) {
  const f32x2 e = are2 * l, q = arev * l;
  const f32x2 m = {__builtin_amdgcn_exp2f(e[0]), __builtin_amdgcn_exp2f(e[1])};
  const float p0 = __builtin_amdgcn_fractf(q[0]), p1 = __builtin_amdgcn_fractf(q[1]);
  return C2{m * f32x2{__builtin_amdgcn_cosf(p0), __builtin_amdgcn_cosf(p1)}, m * f32x2{__builtin_amdgcn_sinf(p0), __builtin_amdgcn_sinf(p1)}};
}
constexpr int RS = 132;
DEVI void put_row(unsigned* buf, int row, int lane, C2 v) { *(v2u*)(buf + row * RS + 2 * lane) = v2u{pk_f16(v.re[0], v.im[0]), pk_f16(v.re[1], v.im[1])}; }
DEVI f32x2 unp(unsigned u) { return f32x2{bflo(u), bfhi(u)}; }

template <class HK> DEVI void kgen_wave(unsigned par[6], float dt, unsigned* buf0, unsigned char* img, int lane, HK&& hook) {
  unsigned* buf1 = (unsigned*)img;
  u16* tab = (u16*)buf0;
  const int c = lane & 15, g = lane >> 4;
  const f32x2 are2 = unp(par[0]) * (dt * 1.4426950408889634f), arev = unp(par[1]) * (dt * 0.15915494309189535f), nrev = -arev;
  C2 W = cmul2(C2{unp(par[4]), unp(par[5])}, C2{unp(par[2]) * dt, unp(par[3]) * dt});
  float mx = fmaxf(fmaxf(fabsf(W.re[0]), fabsf(W.re[1])), fmaxf(fabsf(W.im[0]), fabsf(W.im[1])));
  unsigned mu = f2u(mx);
  mu = max(mu, f2u(dppf<0x128>(u2f(mu)))); mu = max(mu, f2u(dppf<0x124>(u2f(mu)))); mu = max(mu, f2u(dppf<0x122>(u2f(mu)))); mu = max(mu, f2u(dppf<0x121>(u2f(mu))));
  const unsigned m0 = __builtin_amdgcn_readlane(mu, 0), m1 = __builtin_amdgcn_readlane(mu, 16), m2 = __builtin_amdgcn_readlane(mu, 32), m3 = __builtin_amdgcn_readlane(mu, 48);
  unsigned eb = (max(max(m0, m1), max(m2, m3)) >> 23) & 0xffu;
  eb = eb < 2u ? 2u : (eb > 250u ? 250u : eb);
  const float sc = u2f((eb + 1u) << 23), isc = u2f((253u - eb) << 23);
  W.re *= isc; W.im *= isc;
  {
    const C2 e8 = cexp2(are2, arev, 8.f);
    C2 W8 = cmul2(W, cexp2(are2, arev, 64.f));
#pragma unroll
    for (int b = 0; b < 8; ++b) {
      put_row(buf0, b, lane, W); put_row(buf0, 8 + b, lane, W8);
      if (b < 7) { W = cmul2(W, e8); W8 = cmul2(W8, e8); }
      if (b == 0) hook(2);
      if (b == 4) hook(3);
    }
  }
  v4u A[8];
#pragma unroll
  for (int ks = 0; ks < 8; ++ks) A[ks] = *(const v4u*)(buf0 + c * RS + 16 * ks + 4 * g);
  C2 er[8];
  er[0] = C2{f32x2{1.f, 1.f}, f32x2{0.f, 0.f}};
  er[1] = cexp2(are2, nrev, 1.f);
  er[2] = cmul2(er[1], er[1]); er[3] = cmul2(er[2], er[1]); er[4] = cmul2(er[2], er[2]);
  er[5] = cmul2(er[4], er[1]); er[6] = cmul2(er[3], er[3]); er[7] = cmul2(er[4], er[3]);
  hook(4);
  const C2 e128 = cexp2(are2, nrev, 128.f);
#pragma unroll
  for (int r = 0; r < 8; ++r) { put_row(buf0, r, lane, er[r]); put_row(buf0, 8 + r, lane, r ? cmul2(e128, er[r]) : e128); }
  f32x4 acc0 = {0.f, 0.f, 0.f, 0.f}, acc1 = acc0, acc2 = acc0;
#pragma unroll
  for (int ks = 0; ks < 8; ++ks) acc0 = mfma_h(A[ks], *(const v4u*)(buf0 + c * RS + 16 * ks + 4 * g), acc0);
  hook(5);
  C2 e = cmul2(e128, e128);
  C2 f = cexp2(are2, nrev, 1024.f);
#pragma unroll
  for (int a = 0; a < 2; ++a) {
#pragma unroll
    for (int r = 0; r < 4; ++r) put_row(buf0, 4 * a + r, lane, r ? cmul2(e, er[2 * r]) : e);
    e = cmul2(e, e128);
  }
  hook(6);
#pragma unroll
  for (int a = 0; a < 4; ++a) {
    put_row(buf0, 8 + 2 * a, lane, e); put_row(buf0, 9 + 2 * a, lane, cmul2(e, er[4])); if (a < 3) e = cmul2(e, e128);
    put_row(buf1, 2 * a, lane, f); f = cmul2(f, e128); put_row(buf1, 2 * a + 1, lane, f); f = cmul2(f, e128);
    if (a == 1) hook(7);
  }
  put_row(buf1, 8, lane, f);
  hook(8);
  {
    v4u B1[8], B2[8];
#pragma unroll
    for (int ks = 0; ks < 8; ++ks) { B1[ks] = *(const v4u*)(buf0 + c * RS + 16 * ks + 4 * g); B2[ks] = *(const v4u*)(buf1 + c * RS + 16 * ks + 4 * g); }
    SB();
#pragma unroll
    for (int ks = 0; ks < 8; ++ks) { acc1 = mfma_h(A[ks], B1[ks], acc1); acc2 = mfma_h(A[ks], B2[ks], acc2); }
  }
  hook(9);
  const int cv0 = 128 * (c >> 3) + (c & 7);
  const int cv1 = c < 8 ? 256 + 128 * (c >> 2) + 2 * (c & 3) : 512 + 128 * ((c - 8) >> 1) + 4 * (c & 1);
  const int cv2 = c < 9 ? 1024 + 128 * c : 2048;
#pragma unroll
  for (int r = 0; r < 4; ++r) {
    const int lb = 8 * (4 * g + r);
    const unsigned q01 = pk_bf16(acc0[r] * sc, acc1[r] * sc);
    tab[lb + cv0] = (u16)q01;
    tab[lb + cv1] = (u16)(q01 >> 16);
    tab[lb + cv2 + (c > 8 ? 1 : 0)] = (u16)pk_bf16(acc2[r] * sc, 0.f);
  }
  {
    const u16* t0 = tab + 2048 - 8 * lane;
    const int o = lane < 32 ? 1 : 0;
    const unsigned H0 = t0[0], L0 = t0[-8], H1 = t0[-512], L1 = t0[-520], H2 = t0[-1024], M2 = t0[-1028], L2 = t0[-1032];
    const u16* t3 = t0 - 1536;
    const unsigned v0 = t3[0], v1 = t3[-1 + o], v2 = t3[-2], v3 = t3[-3 - o], v4 = t3[-4], v5 = t3[-5 + o], v6 = t3[-6], v7 = t3[-7 - o], v8 = t3[-8];
    const unsigned k0 = tab[0];
    SB();
    unsigned char* i0 = img + lane * 16;
    unsigned char* i1 = i0 + KL * 2;
    {
      const unsigned X = (lane & 1) ? L0 : H0, HH = H0 | (H0 << 16), LL = L0 | (L0 << 16);
      *(v4u*)i0 = v4u{HH, HH, X | (L0 << 16), LL};
      *(v4u*)i1 = v4u{HH, H0 | (X << 16), LL, LL};
    }
    {
      const unsigned X = (lane & 1) ? L1 : H1, HH = H1 | (H1 << 16), LL = L1 | (L1 << 16);
      *(v4u*)(i0 + 1024) = v4u{HH, HH, X | (L1 << 16), LL};
      *(v4u*)(i1 + 1024) = v4u{HH, H1 | (X << 16), LL, LL};
    }
    {
      const unsigned HH = H2 | (H2 << 16), MM = M2 | (M2 << 16), LL = L2 | (L2 << 16);
      *(v4u*)(i0 + 2048) = v4u{HH, H2 | (M2 << 16), MM, LL};
      *(v4u*)(i1 + 2048) = v4u{HH, MM, M2 | (L2 << 16), LL};
    }
    *(v4u*)(i0 + 3072) = v4u{v0 | (v1 << 16), v2 | (v3 << 16), v4 | (v5 << 16), v6 | (v7 << 16)};
    *(v4u*)(i1 + 3072) = v4u{v1 | (v2 << 16), v3 | (v4 << 16), v5 | (v6 << 16), v7 | (v8 << 16)};
    if (lane < 8) *(v4u*)(img + (lane >> 2) * (KL * 2) + (256 + (lane & 3)) * 16) = v4u{lane == 0 ? k0 : 0u, 0u, 0u, 0u};
  }
}

constexpr int L_KIMG = 0, L_HIMG = 4 * KIMG_B, L_KSCR = L_HIMG + 4 * HL * 2;
constexpr int L_MISC = 86016, L_WL = L_MISC + 8192, L_DUMMY = L_WL + 65536, L_TOTAL = L_DUMMY + 4096;
static_assert(L_TOTAL <= 163840 && L_KSCR + 4 * 16 * RS * 4 <= L_MISC, "lds");
#define XAUX 0
#define MODHOOK 4
#define ASP 8
#define ROTM 0
#define ROTS 0
#define KAV 12
#define ZFIRST 1u
#define NR1 24
#define ZDV 3
#define NWO 21
#define WNT ""
#define PDNV 8
#define PF0 171
#define A0 0
#define ZPOLL 7
constexpr int OOB = 64 << 20;
constexpr int CA[36] = {3,3,3,3,3,4,4,4,4, 2,2,2,2,2,2,5,5,5, 1,1,1,1,6,6,1,1,1, 0,0,0,0,7,0,0,0,0};
constexpr int CB[36] = {4,3,2,1,0,3,2,1,0, 5,4,3,2,1,0,2,1,0, 6,5,4,3,1,0,2,1,0, 7,6,5,4,0,3,2,1,0};
constexpr int CL[16] = {8+4,3,8+3,8+2,8+1,8+0,4,8+5,2,5,8+6,1,6,8+7,0,7};

constexpr bool cs_dma(int ic) { return ic >= A0 && ((ic - A0) % ASP) == 5 && (ic - A0) / ASP < 16; }
constexpr int zs_lo(int i, int zd) {
  int v = i + zd; if (v > 2 + 2 * i) v = 2 + 2 * i; if (i >= 13 || v > 16) v = 16; return v;
}
DEVI void dma16s(rsrc_t r, int voff, int soff, unsigned ldsaddr) {
  asm volatile("s_mov_b32 m0, %2\n\ts_nop 0\n\tbuffer_load_dwordx4 %0, %1, %3 offen sc1 lds" :: "v"(voff), "s"(r), "s"(ldsaddr), "s"(soff) : "memory");
}
template <int X> DEVI void dma16x(rsrc_t r, int voff, int soff, unsigned ldsaddr) {
  int tmp;
  asm volatile("s_mov_b32 m0, %3\n\tv_xor_b32 %0, %5, %1\n\tbuffer_load_dwordx4 %0, %2, %4 offen lds" : "=&v"(tmp) : "v"(voff), "s"(r), "s"(ldsaddr), "s"(soff), "n"(X) : "memory");
}
DEVI void dma16(rsrc_t r, int voff, int soff, unsigned ldsaddr) {
  asm volatile("s_mov_b32 m0, %2\n\ts_nop 0\n\tbuffer_load_dwordx4 %0, %1, %3 offen " WNT " lds" :: "v"(voff), "s"(r), "s"(ldsaddr), "s"(soff) : "memory");
}

DEVI unsigned plo(unsigned a, unsigned b) { return __builtin_amdgcn_perm(b, a, 0x05040100u); }
DEVI unsigned phi(unsigned a, unsigned b) { return __builtin_amdgcn_perm(b, a, 0x07060302u); }
DEVI unsigned silu_bf(float c) { return pk_bf16(c * __builtin_amdgcn_rcpf(1.0f + __builtin_amdgcn_exp2f(-1.4426950408889634f * c)), 0.f); }
DEVI float col_reduce(float* fl, const float (&acc)[8], int tid) {
  const int rg = tid >> 3, cl = tid & 7;
  *(f32x4*)(fl + rg * 68 + cl * 8) = f32x4{acc[0], acc[1], acc[2], acc[3]};
  *(f32x4*)(fl + rg * 68 + cl * 8 + 4) = f32x4{acc[4], acc[5], acc[6], acc[7]};
  __syncthreads();
  float s = 0.f;
#pragma unroll
  for (int r = 0; r < 8; ++r) s += fl[(8 * (tid & 3) + r) * 68 + (tid >> 2)];
  s += dppf<0xB1>(s); s += dppf<0x4E>(s);
  return s;
}
DEVI void wout_unit(const Params& p, rsrc_t rs, unsigned char* lw, int unit, int lane) {
  const int ks = unit / 5, dc = unit - 5 * ks, d = 128 * dc + 2 * lane;
  const int f0 = 16 * pair_blk(ks, 0), f1 = 16 * pair_blk(ks, 1);
  unsigned wv[4][8];
#pragma unroll
  for (int g = 0; g < 4; ++g)
#pragma unroll
    for (int j = 0; j < 8; ++j) wv[g][j] = NTL((const unsigned*)(p.w_out + (size_t)((j < 4 ? f0 : f1) + 2 * ((4 * g + (j & 3)) & 7) + (g >> 1)) * D + d));
#pragma unroll
  for (int g = 0; g < 4; ++g) {
    unsigned char* dst = lw + (lane >> 3) * 1024 + (g * 16 + 2 * (lane & 7)) * 16;
    *(v4u*)dst = v4u{plo(wv[g][0], wv[g][1]), plo(wv[g][2], wv[g][3]), plo(wv[g][4], wv[g][5]), plo(wv[g][6], wv[g][7])};
    *(v4u*)(dst + 16) = v4u{phi(wv[g][0], wv[g][1]), phi(wv[g][2], wv[g][3]), phi(wv[g][4], wv[g][5]), phi(wv[g][6], wv[g][7])};
  }
#pragma unroll
  for (int i = 0; i < 8; ++i) bst1(rs, O_WOUTF + ((8 * dc + i) * 27 + ks) * 1024 + lane * 16, *(const v4u*)(lw + i * 1024 + lane * 16));
}

DEVI void aux_role(const Params& p, rsrc_t rs, unsigned char* smem, int a, unsigned& tag) {
  const int tid = threadIdx.x, lane = tid & 63, w = __builtin_amdgcn_readfirstlane(tid >> 6);
  float* fl = (float*)smem;
  constexpr unsigned ONES = 0x3f803f80u;
  unsigned wv[5][8];
  const int blk = (a & 7) * 7 + (a >> 3), fp = tid & 15, rgp = tid >> 4;
  const u16* wsrc = p.w_in + (size_t)(8 * rgp) * F2 + 32 * min(blk, 53) + 2 * fp;
#define WIN_LD(i) { _Pragma("unroll") for (int j_ = 0; j_ < 8; ++j_) wv[i][j_] = NTL((const unsigned*)(wsrc + (128 * (i) + j_) * F2)); }
  if (a < 64) {
    const int row = 32 * a + 8 * w + (lane >> 3), c = lane & 7;
    v4u xv[10];
#pragma unroll
    for (int i = 0; i < 10; ++i) xv[i] = *(const v4u*)(p.u + (size_t)row * D + 64 * i + 8 * c);
    tag = __hip_atomic_load((GAS unsigned*)(p.ws + O_EPOCH), RLX_AGENT);
    SB();
    WIN_LD(0); WIN_LD(1);
    SB();
    float s0 = 0.f, s1 = 0.f, q0 = 0.f, q1 = 0.f;
#pragma unroll
    for (int i = 0; i < 10; ++i)
#pragma unroll
      for (int j = 0; j < 4; j += 2) {
        s0 = dot2(xv[i][j], ONES, s0); q0 = dot2(xv[i][j], xv[i][j], q0);
        s1 = dot2(xv[i][j + 1], ONES, s1); q1 = dot2(xv[i][j + 1], xv[i][j + 1], q1);
      }
    float m = s0 + s1, v = q0 + q1;
    m += dppf<0xB1>(m); v += dppf<0xB1>(v); m += dppf<0x4E>(m); v += dppf<0x4E>(v); m += dppf<0x141>(m); v += dppf<0x141>(v);
    m *= (1.0f / D);
    const float r = __builtin_amdgcn_rsqf(fmaxf(v * (1.0f / D) - m * m, 0.f) + 1e-5f);
    tag += 1u;
    if (c == 0) bst1(rs, O_STG + row * 16, v4u{f2u(m), tag, f2u(r), tag});
    SB();
    WIN_LD(2); WIN_LD(3); WIN_LD(4);
  } else if (a < 84) {
    const int rg = tid >> 3, cl = tid & 7, col0 = (a < 74 ? 2 * D + 64 * (a - 64) : 5 * D + 64 * (a - 74));
    unsigned* sL = (unsigned*)(smem + 12288);
    v4u mv[20];
    const v2u cv = *(const v2u*)(p.cvec + 4 * min(tid, 159));
#pragma unroll
    for (int i = 0; i < 20; ++i) mv[i] = __builtin_nontemporal_load((const v4u*)(p.w_mod + (size_t)(20 * rg + i) * 3840 + col0 + 8 * cl));
    tag = __hip_atomic_load((GAS unsigned*)(p.ws + O_EPOCH), RLX_AGENT);
    SB();
    if (tid < 160) *(v4u*)(sL + 4 * tid) = v4u{silu_bf(bflo(cv[0])), silu_bf(bfhi(cv[0])), silu_bf(bflo(cv[1])), silu_bf(bfhi(cv[1]))};
    __syncthreads();
    v4u s4[5];
#pragma unroll
    for (int i = 0; i < 5; ++i) s4[i] = *(const v4u*)(sL + 20 * rg + 4 * i);
    float acc[8];
#pragma unroll
    for (int j = 0; j < 8; ++j) acc[j] = 0.f;
#pragma unroll
    for (int i = 0; i < 20; ++i) {
      const unsigned slo = s4[i >> 2][i & 3], shi = slo << 16;
#pragma unroll
      for (int j = 0; j < 4; ++j) { acc[2 * j] = dot2(mv[i][j], slo, acc[2 * j]); acc[2 * j + 1] = dot2(mv[i][j], shi, acc[2 * j + 1]); }
    }
    const float s = col_reduce(fl, acc, tid);
    tag += 1u;
    if ((tid & 3) == 0) bst1_64(rs, O_MODG + (col0 + (tid >> 2)) * 8, v2u{f2u(rbf(s)), tag});
  } else {
    tag = __hip_atomic_load((GAS unsigned*)(p.ws + O_EPOCH), RLX_AGENT) + 1u;
    if (a == 92) {
      if (tid < 192) __hip_atomic_store((GAS unsigned*)(p.ws + O_CNT) + tid * 64, 0u, RLX_AGENT);
      __hip_atomic_store((GAS unsigned*)(p.ws + O_FLAG) + tid * 64, 0u, RLX_AGENT);
      DRAIN();
      __syncthreads();
      if (tid == 0) bst1_64(rs, O_INIT, v2u{0u, tag});
    }
  }
  STAMP(1);
  if (a < 56) {
    float* c1L = (float*)(smem + 61440);
    u16* sbL = (u16*)(smem + 61440 + 2560);
    float* red = (float*)(smem + 16384);
#pragma unroll 1
    for (unsigned s = 0; s < SPIN; ++s) {
      asm volatile("" ::: "memory");
      v4u v[3][4];
#pragma unroll
      for (int i = 0; i < 3; ++i)
#pragma unroll
        for (int k = 0; k < 4; ++k) v[i][k] = bld1(rs, min(256 * i + tid, 639) * 16, O_MODP + (k * 2560 + 1280) * 8);
      const u64 vi = __hip_atomic_load((GAS u64*)(p.ws + O_INIT), RLX_AGENT);
      unsigned bad = (unsigned)(vi >> 32) ^ tag;
#pragma unroll
      for (int i = 0; i < 3; ++i) {
        const int e = min(256 * i + tid, 639);
#pragma unroll
        for (int k = 0; k < 4; ++k) bad |= (v[i][k][1] ^ tag) | (v[i][k][3] ^ tag);
        const unsigned pr = pk_bf16((u2f(v[i][0][0]) + u2f(v[i][1][0])) + (u2f(v[i][2][0]) + u2f(v[i][3][0])), (u2f(v[i][0][2]) + u2f(v[i][1][2])) + (u2f(v[i][2][2]) + u2f(v[i][3][2])));
        if (e < 320) *(unsigned*)(sbL + 2 * e) = pr; else *(f32x2*)(c1L + 2 * (e - 320)) = f32x2{rbf(1.0f + bflo(pr)), rbf(1.0f + bfhi(pr))};
      }
      PIN(bad);
      if (__all(bad == 0u)) break;
      __builtin_amdgcn_s_sleep(1);
    }
    ORDER();
    __syncthreads();
    f32x4 us[5];
    const int so = blk < 54 ? O_WINF + (2 * blk + (fp >> 3)) * 20480 + (rgp >> 2) * 1024 + ((rgp & 3) * 16 + (fp & 7)) * 16 : OOB;
    auto unit = [&](auto ic_) __attribute__((always_inline)) {
      constexpr int i = ic_;
      const int e = rgp + 16 * i, uo = so + i * 4096;
      const f32x4 ca = *(const f32x4*)(c1L + 8 * e), cb = *(const f32x4*)(c1L + 8 * e + 4);
      const v4u sb = *(const v4u*)(sbL + 8 * e);
      unsigned ta[4], tb[4], ra[4], rb[4];
#pragma unroll
      for (int j = 0; j < 4; ++j) {
        const f32x2 cc = j == 0 ? f32x2{ca[0], ca[1]} : j == 1 ? f32x2{ca[2], ca[3]} : j == 2 ? f32x2{cb[0], cb[1]} : f32x2{cb[2], cb[3]};
        const f32x2 ya = f32x2{bflo(wv[i][2 * j]), bflo(wv[i][2 * j + 1])} * cc, yb = f32x2{bfhi(wv[i][2 * j]), bfhi(wv[i][2 * j + 1])} * cc;
        ra[j] = pk_bf16(ya[0], ya[1]); rb[j] = pk_bf16(yb[0], yb[1]);
      }
      bst1(rs, uo, v4u{ra[0], ra[1], ra[2], ra[3]});
      bst1(rs, uo + 128, v4u{rb[0], rb[1], rb[2], rb[3]});
      float u1a = 0.f, u1b = 0.f, u2a = 0.f, u2b = 0.f;
#pragma unroll
      for (int j = 0; j < 4; ++j) {
        ta[j] = plo(wv[i][2 * j], wv[i][2 * j + 1]); tb[j] = phi(wv[i][2 * j], wv[i][2 * j + 1]);
        u1a = dot2(ra[j], ONES, u1a); u1b = dot2(rb[j], ONES, u1b);
        u2a = dot2(ta[j], sb[j], u2a); u2b = dot2(tb[j], sb[j], u2b);
      }
      us[i] = f32x4{u1a, u2a, u1b, u2b};
    };
    sfor<5>(unit);
    if (blk == 54) {
#pragma unroll
      for (int i = 0; i < 5; ++i) bst1(rs, O_WINF + 108 * 20480 + i * 4096 + tid * 16, v4u{ONES, ONES, ONES, ONES});
    }
    *(f32x4*)(red + tid * 4) = ((us[0] + us[1]) + (us[2] + us[3])) + us[4];
    __syncthreads();
    if (tid < 64) {
      const int c = (tid >> 1) & 15;
      float s = 0.f;
#pragma unroll
      for (int r = 0; r < 16; ++r) s += red[r * 64 + ((tid >> 5) * 8 + (c & 7)) * 4 + (c >> 3) * 2 + (tid & 1)];
      if (blk < 54) __builtin_amdgcn_raw_buffer_store_b32(f2u(s), rs, O_U + 32 * blk * 8 + tid * 4, 0, AUX_SC1);
    }
  } else {
    const int wi = (a >= 84 ? a - 84 : a - 44) * 4 + w;
    if (wi < 135) wout_unit(p, rs, smem + 16384 + w * 8192, wi, lane);
    if (a >= 84) {
#pragma unroll 2
      for (int q = a - 84; q < 864; q += 12) bst1(rs, O_P + q * 4096 + tid * 16, v4u{SENT, SENT, SENT, SENT});
    }
#pragma unroll 1
    for (unsigned s = 0; s < SPIN; ++s) {
      const u64 v = __hip_atomic_load((GAS u64*)(p.ws + O_INIT), RLX_AGENT);
      if (__all((unsigned)(v >> 32) == tag)) break;
      __builtin_amdgcn_s_sleep(1);
    }
    ORDER();
  }
  DRAIN();
  __syncthreads();
  arrive(p, a < 56 ? C_S : C_F, tid);
  STAMP(2);
}

template <int CTRL> DEVI float dpp_add(float keep, float send) { return keep + dppf<CTRL>(send); }
DEVI float bfly16(const float (&v)[16], int i16) {
  float a[8], b[4], c[2];
  const bool h8 = i16 & 8, h4 = i16 & 4, h2 = i16 & 2, h1 = i16 & 1;
#pragma unroll
  for (int k = 0; k < 8; ++k) a[k] = dpp_add<0x128>(h8 ? v[k + 8] : v[k], h8 ? v[k] : v[k + 8]);
#pragma unroll
  for (int k = 0; k < 4; ++k) b[k] = dpp_add<0x141>(h4 ? a[k + 4] : a[k], h4 ? a[k] : a[k + 4]);
#pragma unroll
  for (int k = 0; k < 2; ++k) c[k] = dpp_add<0x4E>(h2 ? b[k + 2] : b[k], h2 ? b[k] : b[k + 2]);
  return dpp_add<0xB1>(h1 ? c[1] : c[0], h1 ? c[0] : c[1]);
}
DEVI f32x2 gelu2(f32x2 v) {
  const float c0 = -2.0f * 0.7978845608028654f * 1.4426950408889634f, c1 = c0 * 0.044715f;
  const f32x2 u = v * (v * v * c1 + c0);
  const f32x2 d = f32x2{__builtin_amdgcn_exp2f(u[0]), __builtin_amdgcn_exp2f(u[1])} + 1.0f;
  return v * f32x2{__builtin_amdgcn_rcpf(d[0]), __builtin_amdgcn_rcpf(d[1])};
}
__global__ void __launch_bounds__(256, 1) k_fused(Params p) {
  extern __shared__ __attribute__((aligned(16))) unsigned char smem[];
  const int tid = threadIdx.x, lane = tid & 63, w = __builtin_amdgcn_readfirstlane(tid >> 6);
  const int i16 = lane & 15, g = lane >> 4, l16 = lane * 16;
  const int bx = blockIdx.x & 7, bi = blockIdx.x >> 3;
  const rsrc_t rs = __builtin_amdgcn_make_buffer_rsrc((void*)p.ws, 0, O_END, 0x00020000);
  const rsrc_t rw = __builtin_amdgcn_make_buffer_rsrc((void*)p.w_sp, 0, T * T * 2, 0x00020000);
  u64 ts_[20];
  STAMP(0);
  unsigned tag;
  const unsigned lds0 = (unsigned)(size_t)(LAS unsigned char*)smem;
  u64 pc_; unsigned csz_;
  asm volatile("s_getpc_b64 %0\n.Lkf_beg:\n\ts_mov_b32 %1, .Lkf_end-.Lkf_beg" : "=s"(pc_), "=s"(csz_));
  auto code_prefetch = [&]() __attribute__((always_inline)) {
    const rsrc_t rc = __builtin_amdgcn_make_buffer_rsrc((void*)(pc_ & ~15ull), 0, (int)csz_, 0x00020000);
    if (w < 3) dma16(rc, l16, (bi * 3 + w) * 1024, lds0 + L_DUMMY + w * 1024);
  };
  const bool is_conv = bi < 20;
  const int q4 = bx * 20 + bi;
  const int panel = (bx >> 1) * 8 + (bi >> 2), j = (bx & 1) * 4 + (bi & 3);
  const int b0 = blk0(j);
  const int lb0 = (w < 3) ? 2 * w : 6, lb1 = 2 * w + 1;
  const bool has0 = (w < 3) || (j < 6), has1 = (w < 3);
  const int z2s0 = has0 ? O_WINF + (54 + b0 + lb0) * 20480 : OOB, z2s1 = O_WINF + (has1 ? 54 + b0 + lb1 : 108) * 20480;
  const int z1s0 = has0 ? O_WINF + (b0 + lb0) * 20480 : OOB, z1s1 = has1 ? O_WINF + (b0 + lb1) * 20480 : OOB;
  const int wx = w ^ (j & 3);
  const int wso = (64 * panel + 32 * (wx >> 1) + 4 * (wx & 1)) * 4096 + w * 1024;
#define WSP_DMA(lt, r, ldsoff) dma16x<(r) * 16>(rw, l16, (wso ^ ((32 * ((lt) >> 1) + 4 * ((lt) & 1)) * 4096)) + (8 * ((r) >> 2) + ((r) & 3)) * 4096, lds0 + (ldsoff) + w * 16384 + (r) * 1024)
  const int wrd = w * 16384 + i16 * 1024 + (g ^ (i16 & 3)) * 16;
#define W_ADDR(s) (wrq[(s) & 3] + ((s) >> 2) * 256)
  const int zu0 = has0 ? O_Z2N + (2 * panel) * 1024 + (b0 + lb0) * 65536 : OOB, zu1 = has1 ? O_Z2N + (2 * panel) * 1024 + (b0 + lb1) * 65536 : OOB;
  const int pu = O_P + ((4 * panel) * 27 + (w < 3 ? 3 * j + w : 24 + (j >> 1))) * 1024;
  const int pu16 = w < 3 ? pu : OOB, pu8 = (w == 3 && j < 6) ? pu + (j & 1) * 8 : OOB;
  v4u WR[32];
  int wrq[4];
#pragma unroll
  for (int q = 0; q < 4; ++q) wrq[q] = wrd + ((q ^ (i16 >> 2)) * 64);
#define WR_RD(q, dst) WR[dst] = *(const v4u*)(smem + L_WL + W_ADDR(q))
  v4u R[40];
#define R_LD(q, s0, s1) R[q] = bld1(rs, l16 + (((q) >> 1) & 3) * 1024, (((q) & 1) ? (s1) : (s0)) + ((q) >> 3) * 4096)
  bool oks = false;
  if (__builtin_expect(is_conv, 1)) {
    const int d = 4 * q4 + w;
    unsigned par[6];
    par[0] = NTL((const unsigned*)(p.a_re + d * NST + 2 * lane)); par[1] = NTL((const unsigned*)(p.a_im + d * NST + 2 * lane));
    par[2] = NTL((const unsigned*)(p.b_re + d * NST + 2 * lane)); par[3] = NTL((const unsigned*)(p.b_im + d * NST + 2 * lane));
    par[4] = NTL((const unsigned*)(p.c_re + d * NST + 2 * lane)); par[5] = NTL((const unsigned*)(p.c_im + d * NST + 2 * lane));
    const float ldt = bf2f(p.log_dt[d]);
    SB();
    v4u mv[5]; unsigned cv[5];
    const int mcg = q4 >> 2, mkq = q4 & 3;
#pragma unroll
    for (int i = 0; i < 5; ++i) {
      const int row = 160 * mkq + 32 * i + (tid >> 3);
      mv[i] = __builtin_nontemporal_load((const v4u*)(p.w_mod + (size_t)row * 3840 + 64 * mcg + (mcg >= 20 ? D : 0) + 8 * (tid & 7)));
      cv[i] = p.cvec[row];
    }
    const unsigned ep = __hip_atomic_load((GAS unsigned*)(p.ws + O_EPOCH), RLX_AGENT);
    const v2u dkv = *(const v2u*)(p.dskip + 4 * q4);
    v2u xa[8];
    v4u st[4][2]; u64 mg;
    const int mo = lane < 32 ? O_MODP + ((lane & 3) * 2560 + (lane >> 4) * D + 4 * q4 + ((lane >> 2) & 3)) * 8 : O_INIT;
    auto gather = [&]() __attribute__((always_inline)) {
      mg = __hip_atomic_load((GAS u64*)(p.ws + mo), RLX_AGENT);
#pragma unroll
      for (int i = 0; i < 4; ++i) { st[i][0] = bld1(rs, O_STG + (256 * i + tid) * 32); st[i][1] = bld1(rs, O_STG + (256 * i + tid) * 32 + 16); }
    };
    const rsrc_t ru = __builtin_amdgcn_make_buffer_rsrc((void*)p.u, 0, T * D * 2, 0x00020000);
    auto xld = [&](int i) __attribute__((always_inline)) { xa[i] = __builtin_amdgcn_raw_buffer_load_b64(ru, tid * (4 * D), (512 * (i >> 1) + (i & 1)) * (2 * D) + 8 * q4, 0); };
    auto hook = [&](int i) __attribute__((always_inline)) {
      SB();
      if (i == MODHOOK) {
        tag = ep + 1u;
        float acc[8];
#pragma unroll
        for (int k = 0; k < 8; ++k) acc[k] = 0.f;
#pragma unroll
        for (int q = 0; q < 5; ++q) {
          const unsigned slo = silu_bf(bf2f(cv[q])), shi = slo << 16;
#pragma unroll
          for (int k = 0; k < 4; ++k) { acc[2 * k] = dot2(mv[q][k], slo, acc[2 * k]); acc[2 * k + 1] = dot2(mv[q][k], shi, acc[2 * k + 1]); }
        }
        const float s = col_reduce((float*)(smem + L_WL), acc, tid);
        if ((tid & 3) == 0) bst1_64(rs, O_MODP + (mkq * 2560 + 64 * mcg + (tid >> 2)) * 8, v2u{f2u(s), tag});
        SB();
        code_prefetch();
      }
      if (i == 2) xld(2);
      if (i == 4) { xld(3); xld(4); }
      if (i >= 5 && i <= 7) xld(i);
      if (i == 9) gather();
      SB();
    };
    SB();
    xld(0); xld(1);
    SB();
    u16* himg = (u16*)(smem + L_HIMG);
    if (tid < 128) *(v4u*)(himg + (tid >> 5) * HL + (tid & 31) * 8) = v4u{0, 0, 0, 0};
    const float dt = rbf(__builtin_amdgcn_exp2f(ldt * 1.4426950408889634f));
    kgen_wave(par, dt, (unsigned*)(smem + L_KSCR) + w * 16 * RS, smem + L_KIMG + w * KIMG_B, lane, hook);
    STAMP(1);
#pragma unroll 1
    for (unsigned s = 0; s < SPIN; ++s) {
      unsigned bad = (unsigned)(mg >> 32) ^ tag;
#pragma unroll
      for (int i = 0; i < 4; ++i) bad |= (st[i][0][1] ^ tag) | (st[i][0][3] ^ tag) | (st[i][1][1] ^ tag) | (st[i][1][3] ^ tag);
      PIN(bad);
      if (__all(bad == 0u)) break;
      asm volatile("" ::: "memory");
      gather();
    }
    ORDER();
    STAMP(2);
    float sa[4], c1[4];
    {
      float ms = u2f((unsigned)mg);
      ms += dppf<0xB1>(ms); ms += dppf<0x4E>(ms);
      const unsigned mr = f2u(rbf(ms));
#pragma unroll
      for (int c = 0; c < 4; ++c) {
        sa[c] = u2f(__builtin_amdgcn_readlane(mr, 4 * c));
        c1[c] = rbf(1.0f + u2f(__builtin_amdgcn_readlane(mr, 16 + 4 * c)));
      }
    }
    unsigned hv[4][4];
#pragma unroll
    for (int i = 0; i < 4; ++i) {
      const int pr = 256 * i + tid;
      const f32x2 m = {u2f(st[i][0][0]), u2f(st[i][1][0])}, r = {u2f(st[i][0][2]), u2f(st[i][1][2])};
      const f32x2 x[4] = {f32x2{bflo(xa[2 * i][0]), bflo(xa[2 * i + 1][0])}, f32x2{bfhi(xa[2 * i][0]), bfhi(xa[2 * i + 1][0])},
                          f32x2{bflo(xa[2 * i][1]), bflo(xa[2 * i + 1][1])}, f32x2{bfhi(xa[2 * i][1]), bfhi(xa[2 * i + 1][1])}};
#pragma unroll
      for (int c = 0; c < 4; ++c) {
        const f32x2 h = (x[c] - m) * r * c1[c] + sa[c];
        hv[i][c] = pk_bf16(h[0], h[1]);
        *(unsigned*)(himg + c * HL + 256 + 2 * pr) = hv[i][c];
      }
    }
    unsigned ab[8];
#pragma unroll
    for (int a = 0; a < 8; ++a) { ab[a] = lds0 + L_KIMG + w * KIMG_B + (i16 & 1) * (KL * 2) + (2032 - (i16 & ~1) - 2 * (i16 & 1) + 8 * g) * 2 - 64 * (8 * a + 7); PIN(ab[a]); }
    unsigned bb = lds0 + L_HIMG + (w * HL + 256 - 16 + 16 * i16 + 8 * g) * 2 - 64 * 7; PIN(bb);
    __syncthreads();
    STAMP(3);
    f32x4 acc[8];
#pragma unroll
    for (int c = 0; c < 8; ++c) acc[c] = f32x4{0.f, 0.f, 0.f, 0.f};
    v4u O[2][16];
#define CLOAD1(buf, r, k) { if constexpr (CL[k] < 8) { const LAS unsigned* ap = (const LAS unsigned*)(size_t)ab[CL[k] & 7] + 16 * (7 - (r)); O[buf][CL[k]] = v4u{ap[0], ap[1], ap[2], ap[3]}; } \
                            else O[buf][CL[k]] = *((const LAS v4u*)(size_t)bb + 4 * (8 * (CL[k] - 8) + 7 - (r))); }
    sfor<16>(LAM { constexpr int k = ic_; CLOAD1(0, 0, k); });
    SB();
    unsigned cav = 0;
    u64 gav = 0;
    GAS u64* gap = (GAS u64*)(p.ws + O_MODG) + 2 * D + 4 * q4 + (lane & 3);
    sfor<8 * 36>(LAM {
      constexpr int r = ic_ / 36, m = ic_ % 36;
      if constexpr (m == 0) TS(r);
      acc[CA[m] + CB[m]] = mfma_bf(O[r & 1][CA[m]], O[r & 1][8 + CB[m]], acc[CA[m] + CB[m]]);
      if constexpr (r < 7 && (m & 1) == 0 && m < 32) CLOAD1((r + 1) & 1, r + 1, m >> 1);
      if constexpr (cs_dma(ic_)) WSP_DMA(0, (ic_ - A0) / ASP, L_WL);
      if constexpr (r == 6 && m == 22) gav = __hip_atomic_load(gap, RLX_AGENT);
      if constexpr (r == 7 && m == 34) cav = cnt_load(p, C_S, lane);
      SB();
    });
    TS(8); TSDUMP(0, 9);
    u16* ybuf = (u16*)(smem + L_KSCR);
#pragma unroll
    for (int c = 0; c < 8; ++c)
      *(v2u*)(ybuf + w * T + 256 * c + 16 * i16 + 4 * g) = v2u{pk_bf16(acc[c][0], acc[c][1]), pk_bf16(acc[c][2], acc[c][3])};
    __syncthreads();
    STAMP(4);
#pragma unroll 1
    for (unsigned s = 0; s < SPIN; ++s) {
      if (__all((unsigned)(gav >> 32) == tag)) break;
      gav = __hip_atomic_load(gap, RLX_AGENT);
    }
    float ga[4];
#pragma unroll
    for (int c = 0; c < 4; ++c) ga[c] = u2f(__builtin_amdgcn_readlane((unsigned)gav, c));
    const float dk[4] = {bflo(dkv[0]), bfhi(dkv[0]), bflo(dkv[1]), bfhi(dkv[1])};
#pragma unroll
    for (int i = 0; i < 4; ++i) {
      const int pr = 256 * i + tid;
      const f32x2 x[4] = {f32x2{bflo(xa[2 * i][0]), bflo(xa[2 * i + 1][0])}, f32x2{bfhi(xa[2 * i][0]), bfhi(xa[2 * i + 1][0])},
                          f32x2{bflo(xa[2 * i][1]), bflo(xa[2 * i + 1][1])}, f32x2{bfhi(xa[2 * i][1]), bfhi(xa[2 * i + 1][1])}};
      f32x2 o[4];
#pragma unroll
      for (int c = 0; c < 4; ++c) o[c] = x[c] + (unp(*(const unsigned*)(ybuf + c * T + 2 * pr)) + unp(hv[i][c]) * dk[c]) * ga[c];
      bst1(rs, O_X1G + (q4 * T + 2 * pr) * 8, v4u{(pk_bf16(o[0][0], o[1][0])), (pk_bf16(o[2][0], o[3][0])), (pk_bf16(o[0][1], o[1][1])), (pk_bf16(o[2][1], o[3][1]))});
    }
    SB();
    oks = __all(cav >= 7u);
    ORDER();
    STAMP(5);
  } else {
    aux_role(p, rs, smem, (bi - 20) * 8 + bx, tag);
    code_prefetch();
    sfor<16>(LAM { WSP_DMA(0, ic_, L_WL); });
    DRAIN();
  }
#pragma unroll
  for (int q = 0; q < 16; ++q) WR_RD(q, q);
  int otab;
  {
    const int lf = lane & 7;
    int ab = 2 * w + lf; ab = ab >= 7 ? ab - 7 : ab;
    const int zo = (ab == 6 && j >= 6) ? OOB : O_Z2N + (b0 + ab) * 65536 + 16 * w * 1024;
    const int sl = (ab * 3 + ((w - (ab >> 1) - 1) & 3)) * 4096;
    const int ro = (sl < 65536 ? L_WL + sl : sl) + ((w ^ (ab >> 1)) << 10);
    const int n = 6 * w + lf - 1;
    const int ko = (lf == 0 ? 3 * j + min(w, 2) : (n < 3 * j ? n : n + 3)) * 1024;
    const int lt = lane & 3;
    const int po = (lane & 4) ? ((lt ^ w) * 3 + lt - 1) * 5120 : O_P + (4 * panel + (lt ^ w)) * (27 * 1024);
    const int sel = (lane >> 3) & 3;
    otab = sel == 0 ? zo : (sel == 1 ? ro : (sel == 2 ? ko : po));
    PIN(otab);
  }
  asm volatile("s_waitcnt lgkmcnt(0)" ::: "memory");
  if (is_conv) {
    DRAIN();
    __syncthreads();
    arrive(p, C_X1, tid);
  }
  if (!oks) cnt_wait<4>(p, C_S, 7u, lane);
  {
    const rsrc_t rb = __builtin_amdgcn_make_buffer_rsrc((void*)p.b_sp, 0, T * 2, 0x00020000);
    if (w < 3) dma16s(rs, l16, w == 0 ? O_U + (FH + 16 * b0) * 8 : (w == 1 ? O_U + 16 * b0 * 8 : OOB), lds0 + L_DUMMY + w * 1024);
    else dma16(rb, l16, 128 * panel, lds0 + L_DUMMY + 3072);
  }
#pragma unroll
  for (int q = 0; q < NR1; ++q) R_LD(q, z2s0, z2s1);
  SB();
  sfor<16>(LAM { WSP_DMA(1, ic_, L_WL); });
  SB();
  STAMP(6);
  const int xoff = ((lane >> 5) * T + ((2 * (lane & 31)) ^ (4 * (w & 1)))) * 8;
  const int xso = O_X1G + (2 * w * T + 64 * panel) * 8;
#define X_DMA(ks) dma16s(rs, xoff, xso + (ks) * (8 * T * 8), lds0 + ((ks) * 4 + w) * 1024)
  float* sxL = (float*)(smem + L_MISC);
  float* st1 = (float*)(smem + L_MISC + 512) + w * 128;
  float* gsum = (float*)(smem + L_MISC + 2560);
  float* st2 = (float*)(smem + L_MISC + 4608) + w * 128;
  const unsigned xrb = lds0 + g * 1024 + (8 * (i16 >> 2) + (i16 & 3) + 4 * (g & 1)) * 8;
  unsigned xl[4], xh[4];
#pragma unroll
  for (int i = 0; i < 4; ++i) { xl[i] = xrb ^ (((i ^ wx) & 1) * 32) ^ (((i ^ wx) >> 1) * 256); xh[i] = xl[i] + 512; PIN(xl[i]); PIN(xh[i]); }
  auto ldx = [&](unsigned lo, unsigned hi, int off) __attribute__((always_inline)) {
    const v2u a = *(const LAS v2u*)(size_t)(lo + off), c = *(const LAS v2u*)(size_t)(hi + off);
    return v4u{a[0], a[1], c[0], c[1]};
  };
  f32x4 a0[4], a1[4], gram = {0.f, 0.f, 0.f, 0.f};
#pragma unroll
  for (int i = 0; i < 4; ++i) { a0[i] = f32x4{0.f, 0.f, 0.f, 0.f}; a1[i] = a0[i]; }
  if (w == 3) cnt_wait<2>(p, C_X1, 20u, lane);
  __syncthreads();
  STAMP(7);
#pragma unroll
  for (int ks = 0; ks < 8; ++ks) X_DMA(ks);
  asm volatile("s_waitcnt vmcnt(4)\n\ts_barrier" ::: "memory");
  STAMP(8);
  {
    v4u A[3][4];
    sfor<8>(LAM { A[ic_ >> 2][ic_ & 3] = ldx(xl[ic_ & 3], xh[ic_ & 3], (ic_ >> 2) * 4096); SB(); });
    sfor<20 * 9>(LAM {
      constexpr int ks = ic_ / 9, m = ic_ % 9;
      if constexpr (m == 0 && (ks & 1) == 0) TS(ks >> 1);
      if constexpr (m == 0 && ks == 2) asm volatile("s_waitcnt vmcnt(6)\n\ts_barrier" ::: "memory");
      if constexpr (m == 0 && (ks == 6 || ks == 10)) asm volatile("s_waitcnt vmcnt(12)\n\ts_barrier" ::: "memory");
      if constexpr (m == 0 && ks == 8) { sfor<4>([&](auto i_) __attribute__((always_inline)) { xl[i_] += 40960; xh[i_] += 40960; PIN(xl[i_]); PIN(xh[i_]); }); }
      if constexpr (m == 0) __builtin_amdgcn_s_waitcnt(0xC87F);
      if constexpr (m == 2) gram = mfma_bf(A[ks % 3][0], A[ks % 3][0], gram);
      else {
        constexpr int mm = m < 2 ? m : m - 1;
        if constexpr (mm & 1) a1[mm >> 1] = mfma_bf(A[ks % 3][mm >> 1], R[2 * ks + 1], a1[mm >> 1]);
        else a0[mm >> 1] = mfma_bf(A[ks % 3][mm >> 1], R[2 * ks], a0[mm >> 1]);
      }
      if constexpr ((m & 1) == 1 && ks < 18) A[(ks + 2) % 3][m >> 1] = ldx(xl[m >> 1], xh[m >> 1], (ks + 2 - (ks >= 8 ? 10 : 0)) * 4096);
      if constexpr (ks >= 18 && m < 8) WR_RD(8 * (ks - 18) + m, 16 + 8 * (ks - 18) + m);
      if constexpr ((m == 2 || m == 4 || m == 8) && ks < 4) X_DMA(8 + 3 * ks + (m >> 2));
      if constexpr ((m == 4 || m == 8) && ks >= 4) { constexpr int n = NR1 + 2 * (ks - 4) + (m == 8); if constexpr (n < 40) R_LD(n, z2s0, z2s1); else R_LD(n - 40, z1s0, z1s1); }
      SB();
    });
    TS(10); TSDUMP(16, 11);
  }
  if (w == 3) {
    if (i16 == 0) {
#pragma unroll
      for (int i = 0; i < 4; ++i) *(f32x4*)(sxL + (i ^ wx) * 16 + 4 * g) = a1[i];
    }
#pragma unroll
    for (int i = 0; i < 4; ++i) a1[i] = f32x4{0.f, 0.f, 0.f, 0.f};
  }
  if ((i16 >> 2) == g) sxL[64 + wx * 16 + i16] = (i16 & 3) == 0 ? gram[0] : (i16 & 3) == 1 ? gram[1] : (i16 & 3) == 2 ? gram[2] : gram[3];
  __syncthreads();
  STAMP(9);
  {
    const float m = sxL[lane] * (1.0f / D);
    const float var = fmaxf(sxL[64 + lane] * (1.0f / D) - m * m, 0.f);
    const float r = __builtin_amdgcn_rsqf(var + 1e-5f);
    st1[lane] = r; st1[64 + lane] = r * m;
  }
  v4u A1[3][4];
  {
    const f32x2* cU2 = (const f32x2*)(smem + L_DUMMY);
    f32x2 u0 = cU2[16 * lb0 + i16], u1 = cU2[16 * lb1 + i16];
    if (!has0) u0 = f32x2{0.f, 0.f};
    if (!has1) u1 = f32x2{0.f, 0.f};
    float s1[16], s2[16];
    f32x4 rr[4], rm[4];
#pragma unroll
    for (int i = 0; i < 4; ++i) { rr[i] = *(const f32x4*)(st1 + (i ^ wx) * 16 + 4 * g); rm[i] = *(const f32x4*)(st1 + 64 + (i ^ wx) * 16 + 4 * g); }
    sfor<8>(LAM {
      constexpr int i = ic_ >> 1, h = ic_ & 1;
      { constexpr int n0 = NR1 - 8, per = (40 - n0) / 8;
#pragma unroll
        for (int q = n0 + per * ic_; q < n0 + per * (ic_ + 1); ++q) R_LD(q, z1s0, z1s1); }
      const f32x2 r2 = {rr[i][2 * h], rr[i][2 * h + 1]}, m2 = {rm[i][2 * h], rm[i][2 * h + 1]};
      const f32x2 v0 = gelu2(f32x2{a0[i][2 * h], a0[i][2 * h + 1]} * r2 + (u0[1] - m2 * u0[0]));
      const f32x2 v1 = gelu2(f32x2{a1[i][2 * h], a1[i][2 * h + 1]} * r2 + (u1[1] - m2 * u1[0]));
      a0[i][2 * h] = v0[0]; a0[i][2 * h + 1] = v0[1]; a1[i][2 * h] = v1[0]; a1[i][2 * h + 1] = v1[1];
      f32x2 q1 = v0 + v1, q2 = v0 * v0 + v1 * v1;
      PIN(q1); PIN(q2);
      s1[4 * i + 2 * h] = q1[0]; s1[4 * i + 2 * h + 1] = q1[1]; s2[4 * i + 2 * h] = q2[0]; s2[4 * i + 2 * h + 1] = q2[1];
      SB();
    });
    sfor<4>(LAM { xl[ic_] -= 40960; xh[ic_] -= 40960; PIN(xl[ic_]); PIN(xh[ic_]); A1[0][ic_] = ldx(xl[ic_], xh[ic_], 0); SB(); A1[1][ic_] = ldx(xl[ic_], xh[ic_], 4096); SB(); });
    const float t1 = bfly16(s1, i16), t2 = bfly16(s2, i16);
    const int tp = ((i16 >> 2) ^ wx) * 16 + 4 * g + (i16 & 3);
    bst1(rs, O_ZST + (((panel * 4 + w) * 8 + j) * 64 + tp) * 16, v4u{f2u(t1), tag, f2u(t2), tag});
  }
  SB();
  STAMP(10);
  f32x4 c0[4], c1[4];
#pragma unroll
  for (int i = 0; i < 4; ++i) { c0[i] = f32x4{0.f, 0.f, 0.f, 0.f}; c1[i] = c0[i]; }
#define Z1M(ks, m) { if constexpr ((m) == 0 && (ks) >= 2 && (ks) < 19 && (ks) != KA && (ks) != KA + 1) __builtin_amdgcn_s_waitcnt(0xC87F); \
    if constexpr ((m) & 1) c1[(m) >> 1] = mfma_bf(R[2 * (ks) + 1], A1[(ks) % 3][(m) >> 1], c1[(m) >> 1]); else c0[(m) >> 1] = mfma_bf(R[2 * (ks)], A1[(ks) % 3][(m) >> 1], c0[(m) >> 1]); \
    if constexpr (((m) & 1) == 1 && (ks) < 18) A1[((ks) + 2) % 3][(m) >> 1] = ldx(xl[(m) >> 1], xh[(m) >> 1], ((ks) + 2 - ((ks) >= KA ? KA : 0)) * 4096); }
  constexpr int KA = KAV;
  v4u zs[8];
  unsigned cfv = 0;
  const int zsoff = O_ZST + ((panel * 4 + w) * 8 * 64) * 16;
  sfor<KA * 8>(LAM {
    constexpr int ks = ic_ / 8, m = ic_ % 8;
    Z1M(ks, m);
    if constexpr ((m == 2 || m == 6) && ks < 8) { constexpr int q = 2 * ks + (m == 6); WSP_DMA(2, q, L_WL); }
    if constexpr (m == 4 && ks == 7) cfv = cnt_load(p, C_F, lane);
    if constexpr ((m & 1) == 0 && ks >= 8 && ks < 10) { constexpr int jj = 4 * (ks - 8) + (m >> 1); zs[jj] = bld1(rs, l16 + (jj & 3) * 1024, zsoff + (jj >> 2) * 4096); }
    SB();
  });
  v2u x1v[5];
#pragma unroll
  for (int db = 0; db < 5; ++db) {
    const int d = 80 * j + 16 * db + 4 * g;
    x1v[db] = *(const v2u*)(smem + (d >> 2) * 512 + ((32 * (w >> 1) + 8 * (i16 >> 2) + 4 * (w & 1) + (i16 & 3)) ^ (4 * ((d >> 3) & 1))) * 8);
  }
  STAMP(11);
  {
#pragma unroll 1
    for (unsigned s = 0; s < SPIN; ++s) {
      unsigned bad = 0;
#pragma unroll
      for (int jj = 0; jj < 8; ++jj) bad |= (zs[jj][1] ^ tag) | (zs[jj][3] ^ tag);
      PIN(bad);
      if (__all(bad == 0u)) break;
      asm volatile("" ::: "memory");
#pragma unroll
      for (int jj = 0; jj < 8; ++jj) zs[jj] = bld1(rs, l16, zsoff + jj * 1024);
    }
    float a = 0.f, b = 0.f;
#pragma unroll
    for (int jj = 0; jj < 8; ++jj) { a += u2f(zs[jj][0]); b += u2f(zs[jj][2]); }
    *(f32x2*)(gsum + (w * 64 + lane) * 2) = f32x2{a, b};
  }
  if (!__all(cfv >= 5u)) cnt_wait<1>(p, C_F, 5u, lane);
  ORDER();
  __syncthreads();
  STAMP(12);
  {
    const f32x2 s = (*(const f32x2*)(gsum + lane * 2) + *(const f32x2*)(gsum + 128 + lane * 2)) + (*(const f32x2*)(gsum + 256 + lane * 2) + *(const f32x2*)(gsum + 384 + lane * 2));
    const float m = s[0] * (1.0f / FH);
    const float var = fmaxf(s[1] * (1.0f / FH) - m * m, 0.f);
    const float r = __builtin_amdgcn_rsqf(var + 1e-5f);
    st2[lane] = r; st2[64 + lane] = r * m;
  }
  {
    unsigned o0[4][2], o1[4][2];
#pragma unroll
    for (int i = 0; i < 4; ++i) {
      const f32x4 rr = *(const f32x4*)(st2 + (i ^ wx) * 16 + 4 * g), rm = *(const f32x4*)(st2 + 64 + (i ^ wx) * 16 + 4 * g);
      const f32x4 n0 = a0[i] * rr - rm, n1 = a1[i] * rr - rm;
      o0[i][0] = pk_bf16(n0[0], n0[1]); o0[i][1] = pk_bf16(n0[2], n0[3]);
      o1[i][0] = pk_bf16(n1[0], n1[1]); o1[i][1] = pk_bf16(n1[2], n1[3]);
    }
    const bool sw = wx & 1;
    const int ph = (wx >> 1) * 1024;
#pragma unroll
    for (int pp = 0; pp < 2; ++pp) {
      const int e = 2 * pp, o = 2 * pp + 1;
      bst1(rs, zu0 + ((pp * 1024) ^ ph) + l16, sw ? v4u{o0[o][0], o0[o][1], o0[e][0], o0[e][1]} : v4u{o0[e][0], o0[e][1], o0[o][0], o0[o][1]});
      bst1(rs, zu1 + ((pp * 1024) ^ ph) + l16, sw ? v4u{o1[o][0], o1[o][1], o1[e][0], o1[e][1]} : v4u{o1[e][0], o1[e][1], o1[o][0], o1[o][1]});
    }
  }
  sfor<4>(LAM { xl[ic_] += KA * 4096; xh[ic_] += KA * 4096; PIN(xl[ic_]); PIN(xh[ic_]); });
  SB();
  STAMP(13);
  sfor<(20 - KA) * 8>(LAM {
    constexpr int ks = KA + ic_ / 8, m = ic_ % 8;
    Z1M(ks, m);
    SB();
  });
  DRAIN();
  __syncthreads();
  GAS unsigned* flags = (GAS unsigned*)(p.ws + O_FLAG);
  if (tid < 4) __hip_atomic_fetch_or(flags + ((j * 4 + (panel >> 3)) * 4 + tid) * 64, 1u << (panel & 7), RLX_AGENT);
  if (w == 2) dma16s(rs, l16, O_MODG + (5 * D + 80 * j) * 8, lds0 + L_DUMMY + 2048);
  STAMP(14);
  int zof[7], rof[7], kof[7];
#pragma unroll
  for (int q = 0; q < 7; ++q) { zof[q] = __builtin_amdgcn_readlane(otab, q); rof[q] = __builtin_amdgcn_readlane(otab, 8 + q); kof[q] = __builtin_amdgcn_readlane(otab, 16 + q); }
  constexpr int ZD = ZDV;
  v4u Z[16][7];
#define Z_LD(i, f) Z[i][f] = bld1(rs, l16 + ((i) & 3) * 1024, zof[f] + ((i) >> 2) * 4096)
  unsigned zmask = 0u, zfv = 0u;
  GAS unsigned* zfp = flags + ((j * 4 + w) * 4 + (panel >> 3)) * 64;
  auto zneed = [&](unsigned bits) __attribute__((always_inline)) {
    if (__builtin_expect((zmask & bits) != bits, 0)) {
#pragma unroll 1
      for (unsigned s = 0; s < SPIN; ++s) {
        zmask = __builtin_amdgcn_readfirstlane(__hip_atomic_load(zfp, RLX_AGENT));
        if ((zmask & bits) == bits) break;
        __builtin_amdgcn_s_sleep(1);
      }
    }
    ORDER();
  };
  unsigned z1p[2][4][2];
  {
    const v4u* cU1 = (const v4u*)(smem + L_DUMMY + 1024);
    const v4u ua0 = cU1[8 * lb0 + 2 * g], ua1 = cU1[8 * lb0 + 2 * g + 1], ub0 = cU1[8 * lb1 + 2 * g], ub1 = cU1[8 * lb1 + 2 * g + 1];
    float sr[4], sm[4];
#pragma unroll
    for (int i = 0; i < 4; ++i) { sr[i] = st1[16 * (i ^ wx) + i16]; sm[i] = st1[64 + 16 * (i ^ wx) + i16]; }
    sfor<8>(LAM {
      constexpr int i = ic_ >> 1, h = ic_ & 1;
      if constexpr (ic_ < 4) { WSP_DMA(3, 4 * ic_, 0); WSP_DMA(3, 4 * ic_ + 1, 0); WSP_DMA(3, 4 * ic_ + 2, 0); WSP_DMA(3, 4 * ic_ + 3, 0); }
      if constexpr (ic_ == ZPOLL - 2) { zfv = __hip_atomic_load(zfp, RLX_AGENT); SB(); }
      if constexpr (ic_ == ZPOLL) { STAMP(15); zmask = __builtin_amdgcn_readfirstlane(zfv); zneed(ZFIRST); STAMP(16); }
      if constexpr (ic_ >= ZPOLL) {
        constexpr int n = 8 - ZPOLL, k = ic_ - ZPOLL;
#pragma unroll
        for (int q = 14 * k / n; q < 14 * (k + 1) / n; ++q) Z_LD(q / 7, q % 7);
        SB();
      }
      const v4u qa = h ? ua1 : ua0, qb = h ? ub1 : ub0;
      const f32x2 v0 = gelu2(f32x2{c0[i][2 * h], c0[i][2 * h + 1]} * sr[i] + (f32x2{u2f(qa[1]), u2f(qa[3])} - f32x2{u2f(qa[0]), u2f(qa[2])} * sm[i]));
      const f32x2 v1 = gelu2(f32x2{c1[i][2 * h], c1[i][2 * h + 1]} * sr[i] + (f32x2{u2f(qb[1]), u2f(qb[3])} - f32x2{u2f(qb[0]), u2f(qb[2])} * sm[i]));
      z1p[0][i][h] = pk_bf16(v0[0], v0[1]); z1p[1][i][h] = pk_bf16(v1[0], v1[1]);
      PIN(z1p[0][i][h]); PIN(z1p[1][i][h]);
    });
  }
  SB();
  f32x4 acc[7][4];
  const int woso = O_WOUTF + (5 * j) * 27 * 1024;
  v4u WO[35];
  v4u WL[4][2];
#define WO_LD(q) WO[q] = bld1(rs, l16, woso + ((q) % 5) * (27 * 1024) + kof[(q) / 5])
#define WL_RD(i, h) WL[(i) & 3][h] = *(const v4u*)(smem + ((h) ? 0 : L_WL) + W_ADDR(i))
  WL_RD(0, 0); WL_RD(0, 1);
  sfor<13 * 28>(LAM {
    constexpr int i = ic_ / 28, f = (ic_ % 28) / 4, t = ic_ % 4;
    if constexpr (f == 0 && t == 0) TS(i);
    constexpr int lo = zs_lo(i, ZD), hi = zs_lo(i + 1, ZD);
    if constexpr (f == 0 && t == 0 && hi > lo && ((lo & 1) == 0 || hi - lo > 1)) zneed((1u << ((hi - 1) >> 1)) | (1u << (lo >> 1)));
    if constexpr (t < 2) mfma_pin<i == 0, true>(acc[f][t], Z[i][f], WR[t * 16 + i]);
    else mfma_pin<i == 0, false>(acc[f][t], Z[i][f], WL[i & 3][t - 2]);
    if constexpr (hi - lo == 2 && (t == 0 || t == 2)) { constexpr int q = 2 * f + (t >> 1); Z_LD(lo + q / 7, q % 7); }
    if constexpr (hi - lo == 1 && t == 0) Z_LD(lo, f);
    if constexpr (t == 1 && f < 2 && i < 12) WL_RD(i + 1, f);
    if constexpr (t == 1 && f < 6 && i == 12) WL_RD(13 + (f >> 1), f & 1);
    SB();
  });
  TS(13);
  asm volatile("s_nop 7\n\ts_waitcnt lgkmcnt(0)\n\ts_barrier" ::: "memory");
  sfor<7 * 12>(LAM {
    constexpr int fo = ic_ / 12, f = (fo + 2) % 7, i = 13 + (ic_ % 12) / 4, t = ic_ % 4, m = ic_ % 12;
    if constexpr (t < 2) acc[f][t] = mfma_bf(Z[i][f], WR[t * 16 + i], acc[f][t]);
    else acc[f][t] = mfma_bf(Z[i][f], WL[i & 3][t - 2], acc[f][t]);
    if constexpr (m % 4 == 0 && 3 * fo + m / 4 < NWO) WO_LD(3 * fo + m / 4);
    if constexpr (fo >= 1 && fo <= 5 && m % 3 == 2) { constexpr int pf = fo + 1, pt = m / 3; *(f32x4*)(smem + ((rof[pf] + l16) ^ (pt << 10))) = acc[pf][pt]; }
    SB();
  });
  TS(14); TSDUMP(32, 15);
  if (w == 3) {
#pragma unroll
    for (int t = 0; t < 4; ++t) *(f32x4*)(smem + ((rof[1] + l16) ^ (t << 10))) = acc[1][t];
  }
  f32x4 o0[4], o1[4];
#pragma unroll
  for (int t = 0; t < 4; ++t) {
    const float bias = bf2f(((const u16*)(smem + L_DUMMY + 3072))[8 * (i16 >> 2) + (i16 & 3) + 32 * ((t ^ wx) >> 1) + 4 * ((t ^ wx) & 1)]);
    o0[t] = acc[0][t] + bias; o1[t] = acc[1][t] + bias; PIN(o0[t]); PIN(o1[t]);
  }
  __syncthreads();
  STAMP(17);
  v4u PO[4];
  {
    f32x4 pa[4][3], pb[4][3];
#pragma unroll
    for (int s = 0; s < 3; ++s) {
      const int s0 = (2 * w * 3 + s) * 4096, s1 = ((w < 3 ? 2 * w + 1 : 6) * 3 + s) * 4096;
      const unsigned char* r0 = smem + (s0 < 65536 ? L_WL + s0 : s0) + l16;
      const unsigned char* r1 = smem + (s1 < 65536 ? L_WL + s1 : s1) + l16;
#pragma unroll
      for (int t = 0; t < 4; ++t) { pa[t][s] = *(const f32x4*)(r0 + t * 1024); pb[t][s] = *(const f32x4*)(r1 + t * 1024); }
    }
    SB();
#pragma unroll
    for (int t = 0; t < 4; ++t) {
      const f32x4 x0 = (o0[t] + pa[t][0]) + (pa[t][1] + pa[t][2]), x1 = (o1[t] + pb[t][0]) + (pb[t][1] + pb[t][2]);
      const f32x2 qa = unp(z1p[0][t][0]) * f32x2{x0[0], x0[1]}, qb = unp(z1p[0][t][1]) * f32x2{x0[2], x0[3]};
      const f32x2 qc = unp(z1p[1][t][0]) * f32x2{x1[0], x1[1]}, qd = unp(z1p[1][t][1]) * f32x2{x1[2], x1[3]};
      PO[t] = v4u{min(pk_bf16(qa[0], qa[1]), SENT - 1u), min(pk_bf16(qb[0], qb[1]), SENT - 1u), min(pk_bf16(qc[0], qc[1]), SENT - 1u), min(pk_bf16(qd[0], qd[1]), SENT - 1u)};
      bst1(rs, pu16 + (t ^ wx) * (27 * 1024) + l16, PO[t]);
      bst1_64(rs, pu8 + (t ^ wx) * (27 * 1024) + l16, v2u{PO[t][0], PO[t][1]});
    }
  }
  SB();
  STAMP(18);
  if (w == 3) {
#pragma unroll
    for (int t = 0; t < 4; ++t) PO[t] = v4u{0u, 0u, 0u, 0u};
  }
  if (j & 1) { const v4u a = PO[0], c = PO[2]; PO[0] = PO[1]; PO[1] = a; PO[2] = PO[3]; PO[3] = c; }
  if (j & 2) { const v4u a = PO[0], c = PO[1]; PO[0] = PO[2]; PO[2] = a; PO[1] = PO[3]; PO[3] = c; }
  f32x4 e0[5][4], e[5][4];
#pragma unroll
  for (int t = 0; t < 4; ++t)
#pragma unroll
    for (int d = 0; d < 5; ++d) e0[d][t] = mfma_bf(WO[d], PO[t], f32x4{0.f, 0.f, 0.f, 0.f});
  const int trow = 64 * panel + 8 * (i16 >> 2) + (i16 & 3) + 32 * (w >> 1) + 4 * (w & 1);
  int pof[4], eof[4];
#pragma unroll
  for (int q = 0; q < 4; ++q) { pof[q] = __builtin_amdgcn_readlane(otab, 24 + q); eof[q] = __builtin_amdgcn_readlane(otab, 28 + q); }
  const int ppo = lane < 48 ? O_P + (4 * panel + ((lane >> 1) & 3)) * (27 * 1024) + __builtin_amdgcn_ds_bpermute((17 + (lane >> 3)) * 4, otab) + 1012 + (lane & 1) * 8 : OOB;
  v4u gb0[5], gb1[5];
#pragma unroll
  for (int d = 0; d < 5; ++d) { gb0[d] = *(const v4u*)(smem + L_DUMMY + 2048 + (16 * d + 4 * g) * 8); gb1[d] = *(const v4u*)(smem + L_DUMMY + 2048 + (16 * d + 4 * g) * 8 + 16); }
  SB();
#pragma unroll
  for (int q = NWO; q < 35; ++q) WO_LD(q);
  SB();
  STAMP(19);
  v4u P[24];
#define P_LD(q) P[q] = bld1(rs, l16, pof[((q) / 6 + 1) & 3] + kof[1 + (q) % 6])
  constexpr int PDN = PDNV;
#pragma unroll 1
  for (unsigned tries = 0; tries < 4u; ++tries) {
#pragma unroll 1
    for (unsigned s = 0; s < SPIN; ++s) {
      asm volatile("" ::: "memory");
      const unsigned m = __builtin_amdgcn_raw_buffer_load_b32(rs, ppo, 0, AUX_SC1);
#pragma unroll
      for (int q = 0; q < PDN; ++q) P_LD(q);
      if (!__any(m == SENT)) break;
    }
    asm volatile("" ::: "memory");
    STAMP(20);
    unsigned pm = 0u;
    sfor<24 * 5>(LAM {
      constexpr int q = ic_ / 5, d = ic_ % 5, lo = q / 6, lt = (lo + 1) & 3, kk = 1 + q % 6;
      e[d][lt] = mfma_bf(WO[5 * kk + d], P[q], kk == 1 ? e0[d][lt] : e[d][lt]);
      if constexpr (d == 0 && q + PDN < 24) P_LD(q + PDN);
      if constexpr (d == 1) { pm = mx4(pm, P[q]); PIN(pm); }
      if constexpr (lo >= 1 && d == 3 && q % 6 < 5) *(f32x4*)(smem + eof[lo] + (q % 6) * 1024 + l16) = e[q % 6][lo];
      SB();
    });
    if (__builtin_expect(!__any(pm == SENT), 1)) break;
  }
  STAMP(21);
  __syncthreads();
  u16* orow = p.out + (size_t)trow * D + 80 * j + 4 * g;
  f32x4 ep[5][3];
#pragma unroll
  for (int d = 0; d < 5; ++d)
#pragma unroll
    for (int s = 0; s < 3; ++s) ep[d][s] = *(const f32x4*)(smem + ((w * 3 + s) * 5 + d) * 1024 + l16);
  SB();
#pragma unroll
  for (int d = 0; d < 5; ++d) {
    const f32x4 eo = (e[d][0] + ep[d][0]) + (ep[d][1] + ep[d][2]);
    const float r0 = bflo(x1v[d][0]) + u2f(gb0[d][0]) * eo[0], r1 = bfhi(x1v[d][0]) + u2f(gb0[d][2]) * eo[1];
    const float r2 = bflo(x1v[d][1]) + u2f(gb1[d][0]) * eo[2], r3 = bfhi(x1v[d][1]) + u2f(gb1[d][2]) * eo[3];
    *(v2u*)(orow + 16 * d) = v2u{pk_bf16(r0, r1), pk_bf16(r2, r3)};
  }
  if (blockIdx.x == 0 && tid == 0) __hip_atomic_store((GAS unsigned*)(p.ws + O_EPOCH), tag, RLX_AGENT);
  STAMP(22);
  asm volatile(".Lkf_end:");
}

extern "C" void kernel_launch(void* const* d_in, const int* in_sizes, int n_in, void* d_out, int out_size, void* d_ws,
                              size_t ws_size, hipStream_t stream) {
  Params p;
  p.u = (const u16*)d_in[0]; p.a_re = (const u16*)d_in[1]; p.a_im = (const u16*)d_in[2];
  p.b_re = (const u16*)d_in[3]; p.b_im = (const u16*)d_in[4]; p.c_re = (const u16*)d_in[5]; p.c_im = (const u16*)d_in[6];
  p.log_dt = (const u16*)d_in[7]; p.dskip = (const u16*)d_in[8];
  p.w_in = (const u16*)d_in[10]; p.w_sp = (const u16*)d_in[11]; p.b_sp = (const u16*)d_in[12]; p.w_out = (const u16*)d_in[13];
  p.cvec = (const u16*)d_in[14]; p.w_mod = (const u16*)d_in[15];
  p.out = (u16*)d_out; p.ws = (unsigned char*)d_ws;
  (void)hipFuncSetAttribute((const void*)k_fused, hipFuncAttributeMaxDynamicSharedMemorySize, L_TOTAL);
  k_fused<<<256, 256, L_TOTAL, stream>>>(p);
}
```

```cpp
#include <hip/hip_runtime.h>
#include <hip/hip_bf16.h>
#include <stdint.h>
#include <utility>

typedef unsigned v4u __attribute__((ext_vector_type(4)));
typedef unsigned v2u __attribute__((ext_vector_type(2)));
typedef float f32x4 __attribute__((ext_vector_type(4)));
typedef float f32x2 __attribute__((ext_vector_type(2)));
typedef short bf16x8 __attribute__((ext_vector_type(8)));
typedef _Float16 f16x8 __attribute__((ext_vector_type(8)));
typedef _Float16 f16x2 __attribute__((ext_vector_type(2)));
typedef __bf16 bf16x2_t __attribute__((ext_vector_type(2)));
typedef unsigned short u16;
typedef unsigned long long u64;
#define GAS __attribute__((address_space(1)))
#define LAS __attribute__((address_space(3)))
#define RLX_AGENT __ATOMIC_RELAXED, __HIP_MEMORY_SCOPE_AGENT
#define DEVI __device__ __forceinline__
#define SB() __builtin_amdgcn_sched_barrier(0)
template <int... I, class F> __device__ __forceinline__ void sfor_impl(std::integer_sequence<int, I...>, F&& f) { (f(std::integral_constant<int, I>{}), ...); }
template <int N, class F> __device__ __forceinline__ void sfor(F&& f) { sfor_impl(std::make_integer_sequence<int, N>{}, f); }
#define LAM [&](auto ic_) __attribute__((always_inline))
#define NTL(p) __builtin_nontemporal_load(p)
#define DRAIN() asm volatile("s_waitcnt vmcnt(0)" ::: "memory")

constexpr int T = 2048, D = 640, NST = 128, FH = 864, F2 = 1728;
constexpr int KL = 2080, KIMG_B = 2 * KL * 2, HL = 2304;
constexpr unsigned SPIN = 1u << 15;
constexpr int AUX_SC1 = 16, AUX_NT = 2;
constexpr int O_EPOCH = 0;
constexpr int O_INIT = 256;
constexpr int O_CNT = 4096;
constexpr int O_FLAG = O_CNT + 3 * 64 * 256;
constexpr int O_MODG = O_FLAG + 256 * 256;
constexpr int O_MODP = O_MODG + 3840 * 8 + 2048;
constexpr int O_STG = O_MODP + 4 * 2560 * 8;
constexpr int O_U = O_STG + 2048 * 16;
constexpr int O_ZST = O_U + 16384;
constexpr int O_X1G = O_ZST + 32 * 32 * 64 * 16;
constexpr int O_WINF = O_X1G + T * D * 2;
constexpr int O_WOUTF = O_WINF + 109 * 20 * 1024;
constexpr int O_Z2N = O_WOUTF + 40 * 27 * 1024;
constexpr int O_P = O_Z2N + 54 * 64 * 1024;
constexpr int O_END = O_P + 128 * 27 * 1024;
constexpr int C_X1 = 0, C_S = 1, C_F = 2;
constexpr unsigned SENT = 0xffffffffu;

constexpr size_t O_STAMP = 200u << 20;
#define TS(k) do {} while (0)
#define TSDUMP(base, n) do {} while (0)
#define STAMP(idx) do {} while (0)

DEVI float u2f(unsigned u) { return __builtin_bit_cast(float, u); }
DEVI unsigned f2u(float f) { return __builtin_bit_cast(unsigned, f); }
DEVI float bf2f(unsigned b) { return u2f(b << 16); }
DEVI float bflo(unsigned u) { return u2f(u << 16); }
DEVI float bfhi(unsigned u) { return u2f(u & 0xffff0000u); }
DEVI unsigned pk_bf16(float a, float b) {
  f32x2 v = {a, b};
  bf16x2_t r = __builtin_convertvector(v, bf16x2_t);
  return __builtin_bit_cast(unsigned, r);
}
DEVI float rbf(float a) { return bflo(pk_bf16(a, 0.f)); }
DEVI unsigned pk_f16(float a, float b) {
  f32x2 v = {a, b};
  f16x2 r = __builtin_convertvector(v, f16x2);
  return __builtin_bit_cast(unsigned, r);
}
DEVI f32x4 mfma_bf(v4u a, v4u b, f32x4 c) {
  return __builtin_amdgcn_mfma_f32_16x16x32_bf16(__builtin_bit_cast(bf16x8, a), __builtin_bit_cast(bf16x8, b), c, 0, 0, 0);
}
DEVI f32x4 mfma_h(v4u a, v4u b, f32x4 c) {
  return __builtin_amdgcn_mfma_f32_16x16x32_f16(__builtin_bit_cast(f16x8, a), __builtin_bit_cast(f16x8, b), c, 0, 0, 0);
}
template <bool FIRST, bool BA> DEVI void mfma_pin(f32x4& acc, const v4u& a, const v4u& bb) {
  if constexpr (FIRST) {
    if constexpr (BA) asm volatile("v_mfma_f32_16x16x32_bf16 %0, %1, %2, 0" : "=a"(acc) : "v"(a), "a"(bb));
    else asm volatile("v_mfma_f32_16x16x32_bf16 %0, %1, %2, 0" : "=a"(acc) : "v"(a), "v"(bb));
  } else {
    if constexpr (BA) asm volatile("v_mfma_f32_16x16x32_bf16 %0, %1, %2, %0" : "+a"(acc) : "v"(a), "a"(bb));
    else asm volatile("v_mfma_f32_16x16x32_bf16 %0, %1, %2, %0" : "+a"(acc) : "v"(a), "v"(bb));
  }
}
typedef __amdgpu_buffer_rsrc_t rsrc_t;
DEVI v4u bld1(rsrc_t rs, int voff, int soff = 0) { return __builtin_amdgcn_raw_buffer_load_b128(rs, voff, soff, AUX_SC1); }
DEVI void bst1(rsrc_t rs, int voff, v4u v) { __builtin_amdgcn_raw_buffer_store_b128(v, rs, voff, 0, AUX_SC1); }
DEVI void bst1_64(rsrc_t rs, int voff, v2u v) { __builtin_amdgcn_raw_buffer_store_b64(v, rs, voff, 0, AUX_SC1); }
template <int CTRL> DEVI float dppf(float x) { return u2f((unsigned)__builtin_amdgcn_update_dpp(0, (int)f2u(x), CTRL, 0xf, 0xf, true)); }
DEVI float dot2(unsigned a, unsigned b, float c) { return __builtin_amdgcn_fdot2_f32_bf16(__builtin_bit_cast(bf16x2_t, a), __builtin_bit_cast(bf16x2_t, b), c, false); }
DEVI unsigned mx4(unsigned m, v4u v) { return max(max(max(m, v[0]), v[1]), max(v[2], v[3])); }
#define PIN(x) asm volatile("" : "+v"(x))
#define ORDER() __builtin_amdgcn_fence(__ATOMIC_ACQUIRE, "wavefront")

struct Params {
  const u16 *u, *a_re, *a_im, *b_re, *b_im, *c_re, *c_im, *log_dt, *dskip, *w_in, *w_sp, *b_sp, *w_out, *cvec, *w_mod;
  u16* out;
  unsigned char* ws;
};

DEVI int blk0(int j) { return j < 6 ? 7 * j : 42 + 6 * (j - 6); }
DEVI int pair_blk(int ks3, int h) {
  if (ks3 < 24) { const int j = ks3 / 3, w = ks3 - 3 * j; return blk0(j) + 2 * w + h; }
  return 7 * (2 * (ks3 - 24) + h) + 6;
}
DEVI void arrive(const Params& p, int c, int tid) {
  if (tid < 8) __hip_atomic_fetch_add((GAS unsigned*)(p.ws + O_CNT) + ((c * 8 + tid) * 8 + (blockIdx.x & 7)) * 64, 1u, RLX_AGENT);
}
DEVI unsigned cnt_load(const Params& p, int c, int lane) {
  return __hip_atomic_load((GAS unsigned*)(p.ws + O_CNT) + ((c * 8 + ((blockIdx.x >> 3) & 7)) * 8 + (lane & 7)) * 64, RLX_AGENT);
}
template <int SLP> DEVI void cnt_wait(const Params& p, int c, unsigned want, int lane) {
#pragma unroll 1
  for (unsigned s = 0; s < SPIN; ++s) {
    if (__all(cnt_load(p, c, lane) >= want)) break;
    __builtin_amdgcn_s_sleep(SLP);
  }
  ORDER();
}
struct C2 { f32x2 re, im; };
DEVI C2 cmul2(C2 a, C2 b) { return C2{a.re * b.re - a.im * b.im, a.re * b.im + a.im * b.re}; }
DEVI C2 cexp2(f32x2 are2, f32x2 arev, float l) {
  const f32x2 e = are2 * l, q = arev * l;
  const f32x2 m = {__builtin_amdgcn_exp2f(e[0]), __builtin_amdgcn_exp2f(e[1])};
  const float p0 = __builtin_amdgcn_fractf(q[0]), p1 = __builtin_amdgcn_fractf(q[1]);
  return C2{m * f32x2{__builtin_amdgcn_cosf(p0), __builtin_amdgcn_cosf(p1)}, m * f32x2{__builtin_amdgcn_sinf(p0), __builtin_amdgcn_sinf(p1)}};
}
constexpr int RS = 132;
DEVI void put_row(unsigned* buf, int row, int lane, C2 v) { *(v2u*)(buf + row * RS + 2 * lane) = v2u{pk_f16(v.re[0], v.im[0]), pk_f16(v.re[1], v.im[1])}; }
DEVI f32x2 unp(unsigned u) { return f32x2{bflo(u), bfhi(u)}; }

template <class HK> DEVI void kgen_wave(unsigned par[6], float dt, float dsk, unsigned* buf0, unsigned char* img, int lane, HK&& hook) {
  unsigned* buf1 = (unsigned*)img;
  u16* tab = (u16*)buf0;
  const int c = lane & 15, g = lane >> 4;
  const f32x2 are2 = unp(par[0]) * (dt * 1.4426950408889634f), arev = unp(par[1]) * (dt * 0.15915494309189535f), nrev = -arev;
  C2 W = cmul2(C2{unp(par[4]), unp(par[5])}, C2{unp(par[2]) * dt, unp(par[3]) * dt});
  float mx = fmaxf(fmaxf(fabsf(W.re[0]), fabsf(W.re[1])), fmaxf(fabsf(W.im[0]), fabsf(W.im[1])));
  unsigned mu = f2u(mx);
  mu = max(mu, f2u(dppf<0x128>(u2f(mu)))); mu = max(mu, f2u(dppf<0x124>(u2f(mu)))); mu = max(mu, f2u(dppf<0x122>(u2f(mu)))); mu = max(mu, f2u(dppf<0x121>(u2f(mu))));
  const unsigned m0 = __builtin_amdgcn_readlane(mu, 0), m1 = __builtin_amdgcn_readlane(mu, 16), m2 = __builtin_amdgcn_readlane(mu, 32), m3 = __builtin_amdgcn_readlane(mu, 48);
  unsigned eb = (max(max(m0, m1), max(m2, m3)) >> 23) & 0xffu;
  eb = eb < 2u ? 2u : (eb > 250u ? 250u : eb);
  const float sc = u2f((eb + 1u) << 23), isc = u2f((253u - eb) << 23);
  W.re *= isc; W.im *= isc;
  {
    const C2 e8 = cexp2(are2, arev, 8.f);
    C2 W8 = cmul2(W, cexp2(are2, arev, 64.f));
#pragma unroll
    for (int b = 0; b < 8; ++b) {
      put_row(buf0, b, lane, W); put_row(buf0, 8 + b, lane, W8);
      if (b < 7) { W = cmul2(W, e8); W8 = cmul2(W8, e8); }
      if (b == 0) hook(2);
      if (b == 4) hook(3);
    }
  }
  v4u A[8];
#pragma unroll
  for (int ks = 0; ks < 8; ++ks) A[ks] = *(const v4u*)(buf0 + c * RS + 16 * ks + 4 * g);
  C2 er[8];
  er[0] = C2{f32x2{1.f, 1.f}, f32x2{0.f, 0.f}};
  er[1] = cexp2(are2, nrev, 1.f);
  er[2] = cmul2(er[1], er[1]); er[3] = cmul2(er[2], er[1]); er[4] = cmul2(er[2], er[2]);
  er[5] = cmul2(er[4], er[1]); er[6] = cmul2(er[3], er[3]); er[7] = cmul2(er[4], er[3]);
  hook(4);
  const C2 e128 = cexp2(are2, nrev, 128.f);
#pragma unroll
  for (int r = 0; r < 8; ++r) { put_row(buf0, r, lane, er[r]); put_row(buf0, 8 + r, lane, r ? cmul2(e128, er[r]) : e128); }
  f32x4 acc0 = {0.f, 0.f, 0.f, 0.f}, acc1 = acc0, acc2 = acc0;
#pragma unroll
  for (int ks = 0; ks < 8; ++ks) acc0 = mfma_h(A[ks], *(const v4u*)(buf0 + c * RS + 16 * ks + 4 * g), acc0);
  hook(5);
  C2 e = cmul2(e128, e128);
  C2 f = cexp2(are2, nrev, 1024.f);
#pragma unroll
  for (int a = 0; a < 2; ++a) {
#pragma unroll
    for (int r = 0; r < 4; ++r) put_row(buf0, 4 * a + r, lane, r ? cmul2(e, er[2 * r]) : e);
    e = cmul2(e, e128);
  }
  hook(6);
#pragma unroll
  for (int a = 0; a < 4; ++a) {
    put_row(buf0, 8 + 2 * a, lane, e); put_row(buf0, 9 + 2 * a, lane, cmul2(e, er[4])); if (a < 3) e = cmul2(e, e128);
    put_row(buf1, 2 * a, lane, f); f = cmul2(f, e128); put_row(buf1, 2 * a + 1, lane, f); f = cmul2(f, e128);
    if (a == 1) hook(7);
  }
  put_row(buf1, 8, lane, f);
  hook(8);
  {
    v4u B1[8], B2[8];
#pragma unroll
    for (int ks = 0; ks < 8; ++ks) { B1[ks] = *(const v4u*)(buf0 + c * RS + 16 * ks + 4 * g); B2[ks] = *(const v4u*)(buf1 + c * RS + 16 * ks + 4 * g); }
    SB();
#pragma unroll
    for (int ks = 0; ks < 8; ++ks) { acc1 = mfma_h(A[ks], B1[ks], acc1); acc2 = mfma_h(A[ks], B2[ks], acc2); }
  }
  hook(9);
  const int cv0 = 128 * (c >> 3) + (c & 7);
  const int cv1 = c < 8 ? 256 + 128 * (c >> 2) + 2 * (c & 3) : 512 + 128 * ((c - 8) >> 1) + 4 * (c & 1);
  const int cv2 = c < 9 ? 1024 + 128 * c : 2048;
#pragma unroll
  for (int r = 0; r < 4; ++r) {
    const int lb = 8 * (4 * g + r);
    const unsigned q01 = pk_bf16(acc0[r] * sc + ((r == 0 && lane == 0) ? dsk : 0.f), acc1[r] * sc);
    tab[lb + cv0] = (u16)q01;
    tab[lb + cv1] = (u16)(q01 >> 16);
    tab[lb + cv2 + (c > 8 ? 1 : 0)] = (u16)pk_bf16(acc2[r] * sc, 0.f);
  }
  {
    const u16* t0 = tab + 2048 - 8 * lane;
    const int o = lane < 32 ? 1 : 0;
    const unsigned H0 = t0[0], L0 = t0[-8], H1 = t0[-512], L1 = t0[-520], H2 = t0[-1024], M2 = t0[-1028], L2 = t0[-1032];
    const u16* t3 = t0 - 1536;
    const unsigned v0 = t3[0], v1 = t3[-1 + o], v2 = t3[-2], v3 = t3[-3 - o], v4 = t3[-4], v5 = t3[-5 + o], v6 = t3[-6], v7 = t3[-7 - o], v8 = t3[-8];
    const unsigned k0 = tab[0];
    SB();
    unsigned char* i0 = img + lane * 16;
    unsigned char* i1 = i0 + KL * 2;
    {
      const unsigned X = (lane & 1) ? L0 : H0, HH = H0 | (H0 << 16), LL = L0 | (L0 << 16);
      *(v4u*)i0 = v4u{HH, HH, X | (L0 << 16), LL};
      *(v4u*)i1 = v4u{HH, H0 | (X << 16), LL, LL};
    }
    {
      const unsigned X = (lane & 1) ? L1 : H1, HH = H1 | (H1 << 16), LL = L1 | (L1 << 16);
      *(v4u*)(i0 + 1024) = v4u{HH, HH, X | (L1 << 16), LL};
      *(v4u*)(i1 + 1024) = v4u{HH, H1 | (X << 16), LL, LL};
    }
    {
      const unsigned HH = H2 | (H2 << 16), MM = M2 | (M2 << 16), LL = L2 | (L2 << 16);
      *(v4u*)(i0 + 2048) = v4u{HH, H2 | (M2 << 16), MM, LL};
      *(v4u*)(i1 + 2048) = v4u{HH, MM, M2 | (L2 << 16), LL};
    }
    *(v4u*)(i0 + 3072) = v4u{v0 | (v1 << 16), v2 | (v3 << 16), v4 | (v5 << 16), v6 | (v7 << 16)};
    *(v4u*)(i1 + 3072) = v4u{v1 | (v2 << 16), v3 | (v4 << 16), v5 | (v6 << 16), v7 | (v8 << 16)};
    if (lane < 8) *(v4u*)(img + (lane >> 2) * (KL * 2) + (256 + (lane & 3)) * 16) = v4u{lane == 0 ? k0 : 0u, 0u, 0u, 0u};
  }
}

constexpr int L_KIMG = 0, L_HIMG = 4 * KIMG_B, L_KSCR = L_HIMG + 4 * HL * 2;
constexpr int L_MISC = 86016, L_WL = L_MISC + 8192, L_DUMMY = L_WL + 65536, L_TOTAL = L_DUMMY + 4096;
static_assert(L_TOTAL <= 163840 && L_KSCR + 4 * 16 * RS * 4 <= L_MISC, "lds");
#define XAUX 0
#define MODHOOK 4
#define ASP 8
#define ROTM 0
#define ROTS 0
#define KAV 11
#define ZFIRST 1u
#define NR1 24
#define ZDV 3
#define NWO 21
#define WNT ""
#define PDNV 8
#define PF0 171
#define A0 0
#define ZPOLL 6
constexpr int OOB = 64 << 20;
constexpr int CA[36] = {3,3,3,3,3,4,4,4,4, 2,2,2,2,2,2,5,5,5, 1,1,1,1,6,6,1,1,1, 0,0,0,0,7,0,0,0,0};
constexpr int CB[36] = {4,3,2,1,0,3,2,1,0, 5,4,3,2,1,0,2,1,0, 6,5,4,3,1,0,2,1,0, 7,6,5,4,0,3,2,1,0};
constexpr int CA7[36] = {3,4,2,5,1,6,0,7, 3,4,2,5,1,6,0, 3,4,2,5,1,0, 3,4,2,1,0, 3,2,1,0, 2,1,0, 1,0, 0};
constexpr int CB7[36] = {4,3,5,2,6,1,7,0, 3,2,4,1,5,0,6, 2,1,3,0,4,5, 1,0,2,3,4, 0,1,2,3, 0,1,2, 0,1, 0};
constexpr int CL[16] = {8+4,3,8+3,8+2,8+1,8+0,4,8+5,2,5,8+6,1,6,8+7,0,7};

constexpr bool cs_dma(int ic) { return ic >= A0 && ((ic - A0) % ASP) == 5 && (ic - A0) / ASP < 16; }
constexpr int zs_lo(int i, int zd) {
  int v = i + zd; if (v > 2 + 2 * i) v = 2 + 2 * i; if (i >= 13 || v > 16) v = 16; return v;
}
DEVI void dma16s(rsrc_t r, int voff, int soff, unsigned ldsaddr) {
  asm volatile("s_mov_b32 m0, %2\n\ts_nop 0\n\tbuffer_load_dwordx4 %0, %1, %3 offen sc1 lds" :: "v"(voff), "s"(r), "s"(ldsaddr), "s"(soff) : "memory");
}
template <int X> DEVI void dma16x(rsrc_t r, int voff, int soff, unsigned ldsaddr) {
  int tmp;
  asm volatile("s_mov_b32 m0, %3\n\tv_xor_b32 %0, %5, %1\n\tbuffer_load_dwordx4 %0, %2, %4 offen lds" : "=&v"(tmp) : "v"(voff), "s"(r), "s"(ldsaddr), "s"(soff), "n"(X) : "memory");
}
DEVI void dma16(rsrc_t r, int voff, int soff, unsigned ldsaddr) {
  asm volatile("s_mov_b32 m0, %2\n\ts_nop 0\n\tbuffer_load_dwordx4 %0, %1, %3 offen " WNT " lds" :: "v"(voff), "s"(r), "s"(ldsaddr), "s"(soff) : "memory");
}

DEVI unsigned plo(unsigned a, unsigned b) { return __builtin_amdgcn_perm(b, a, 0x05040100u); }
DEVI unsigned phi(unsigned a, unsigned b) { return __builtin_amdgcn_perm(b, a, 0x07060302u); }
DEVI unsigned silu_bf(float c) { return pk_bf16(c * __builtin_amdgcn_rcpf(1.0f + __builtin_amdgcn_exp2f(-1.4426950408889634f * c)), 0.f); }
DEVI float col_reduce(float* fl, const float (&acc)[8], int tid) {
  const int rg = tid >> 3, cl = tid & 7;
  *(f32x4*)(fl + rg * 68 + cl * 8) = f32x4{acc[0], acc[1], acc[2], acc[3]};
  *(f32x4*)(fl + rg * 68 + cl * 8 + 4) = f32x4{acc[4], acc[5], acc[6], acc[7]};
  __syncthreads();
  float s = 0.f;
#pragma unroll
  for (int r = 0; r < 8; ++r) s += fl[(8 * (tid & 3) + r) * 68 + (tid >> 2)];
  s += dppf<0xB1>(s); s += dppf<0x4E>(s);
  return s;
}
DEVI void wout_unit(const Params& p, rsrc_t rs, unsigned char* lw, int unit, int lane) {
  const int ks = unit / 5, dc = unit - 5 * ks, d = 128 * dc + 2 * lane;
  const int f0 = 16 * pair_blk(ks, 0), f1 = 16 * pair_blk(ks, 1);
  unsigned wv[4][8];
#pragma unroll
  for (int g = 0; g < 4; ++g)
#pragma unroll
    for (int j = 0; j < 8; ++j) wv[g][j] = NTL((const unsigned*)(p.w_out + (size_t)((j < 4 ? f0 : f1) + 2 * ((4 * g + (j & 3)) & 7) + (g >> 1)) * D + d));
#pragma unroll
  for (int g = 0; g < 4; ++g) {
    unsigned char* dst = lw + (lane >> 3) * 1024 + (g * 16 + 2 * (lane & 7)) * 16;
    *(v4u*)dst = v4u{plo(wv[g][0], wv[g][1]), plo(wv[g][2], wv[g][3]), plo(wv[g][4], wv[g][5]), plo(wv[g][6], wv[g][7])};
    *(v4u*)(dst + 16) = v4u{phi(wv[g][0], wv[g][1]), phi(wv[g][2], wv[g][3]), phi(wv[g][4], wv[g][5]), phi(wv[g][6], wv[g][7])};
  }
#pragma unroll
  for (int i = 0; i < 8; ++i) bst1(rs, O_WOUTF + ((8 * dc + i) * 27 + ks) * 1024 + lane * 16, *(const v4u*)(lw + i * 1024 + lane * 16));
}

DEVI void aux_role(const Params& p, rsrc_t rs, unsigned char* smem, int a, unsigned& tag) {
  const int tid = threadIdx.x, lane = tid & 63, w = __builtin_amdgcn_readfirstlane(tid >> 6);
  float* fl = (float*)smem;
  constexpr unsigned ONES = 0x3f803f80u;
  unsigned wv[5][8];
  const int blk = (a & 7) * 7 + (a >> 3), fp = tid & 15, rgp = tid >> 4;
  const u16* wsrc = p.w_in + (size_t)(8 * rgp) * F2 + 32 * min(blk, 53) + 2 * fp;
#define WIN_LD(i) { _Pragma("unroll") for (int j_ = 0; j_ < 8; ++j_) wv[i][j_] = NTL((const unsigned*)(wsrc + (128 * (i) + j_) * F2)); }
  if (a < 64) {
    const int row = 32 * a + 8 * w + (lane >> 3), c = lane & 7;
    v4u xv[10];
#pragma unroll
    for (int i = 0; i < 10; ++i) xv[i] = *(const v4u*)(p.u + (size_t)row * D + 64 * i + 8 * c);
    tag = __hip_atomic_load((GAS unsigned*)(p.ws + O_EPOCH), RLX_AGENT);
    SB();
    WIN_LD(0); WIN_LD(1);
    SB();
    float s0 = 0.f, s1 = 0.f, q0 = 0.f, q1 = 0.f;
#pragma unroll
    for (int i = 0; i < 10; ++i)
#pragma unroll
      for (int j = 0; j < 4; j += 2) {
        s0 = dot2(xv[i][j], ONES, s0); q0 = dot2(xv[i][j], xv[i][j], q0);
        s1 = dot2(xv[i][j + 1], ONES, s1); q1 = dot2(xv[i][j + 1], xv[i][j + 1], q1);
      }
    float m = s0 + s1, v = q0 + q1;
    m += dppf<0xB1>(m); v += dppf<0xB1>(v); m += dppf<0x4E>(m); v += dppf<0x4E>(v); m += dppf<0x141>(m); v += dppf<0x141>(v);
    m *= (1.0f / D);
    const float r = __builtin_amdgcn_rsqf(fmaxf(v * (1.0f / D) - m * m, 0.f) + 1e-5f);
    tag += 1u;
    if (c == 0) bst1(rs, O_STG + row * 16, v4u{f2u(m), tag, f2u(r), tag});
    SB();
    WIN_LD(2); WIN_LD(3); WIN_LD(4);
  } else if (a < 84) {
    const int rg = tid >> 3, cl = tid & 7, col0 = (a < 74 ? 2 * D + 64 * (a - 64) : 5 * D + 64 * (a - 74));
    unsigned* sL = (unsigned*)(smem + 12288);
    v4u mv[20];
    const v2u cv = *(const v2u*)(p.cvec + 4 * min(tid, 159));
#pragma unroll
    for (int i = 0; i < 20; ++i) mv[i] = __builtin_nontemporal_load((const v4u*)(p.w_mod + (size_t)(20 * rg + i) * 3840 + col0 + 8 * cl));
    tag = __hip_atomic_load((GAS unsigned*)(p.ws + O_EPOCH), RLX_AGENT);
    SB();
    if (tid < 160) *(v4u*)(sL + 4 * tid) = v4u{silu_bf(bflo(cv[0])), silu_bf(bfhi(cv[0])), silu_bf(bflo(cv[1])), silu_bf(bfhi(cv[1]))};
    __syncthreads();
    v4u s4[5];
#pragma unroll
    for (int i = 0; i < 5; ++i) s4[i] = *(const v4u*)(sL + 20 * rg + 4 * i);
    float acc[8];
#pragma unroll
    for (int j = 0; j < 8; ++j) acc[j] = 0.f;
#pragma unroll
    for (int i = 0; i < 20; ++i) {
      const unsigned slo = s4[i >> 2][i & 3], shi = slo << 16;
#pragma unroll
      for (int j = 0; j < 4; ++j) { acc[2 * j] = dot2(mv[i][j], slo, acc[2 * j]); acc[2 * j + 1] = dot2(mv[i][j], shi, acc[2 * j + 1]); }
    }
    const float s = col_reduce(fl, acc, tid);
    tag += 1u;
    if ((tid & 3) == 0) bst1_64(rs, O_MODG + (col0 + (tid >> 2)) * 8, v2u{f2u(rbf(s)), tag});
  } else {
    tag = __hip_atomic_load((GAS unsigned*)(p.ws + O_EPOCH), RLX_AGENT) + 1u;
    if (a == 92) {
      if (tid < 192) __hip_atomic_store((GAS unsigned*)(p.ws + O_CNT) + tid * 64, 0u, RLX_AGENT);
      __hip_atomic_store((GAS unsigned*)(p.ws + O_FLAG) + tid * 64, 0u, RLX_AGENT);
      DRAIN();
      __syncthreads();
      if (tid == 0) bst1_64(rs, O_INIT, v2u{0u, tag});
    }
  }
  STAMP(1);
  if (a < 56) {
    float* c1L = (float*)(smem + 61440);
    u16* sbL = (u16*)(smem + 61440 + 2560);
    float* red = (float*)(smem + 16384);
#pragma unroll 1
    for (unsigned s = 0; s < SPIN; ++s) {
      asm volatile("" ::: "memory");
      v4u v[3][4];
#pragma unroll
      for (int i = 0; i < 3; ++i)
#pragma unroll
        for (int k = 0; k < 4; ++k) v[i][k] = bld1(rs, min(256 * i + tid, 639) * 16, O_MODP + (k * 2560 + 1280) * 8);
      const u64 vi = __hip_atomic_load((GAS u64*)(p.ws + O_INIT), RLX_AGENT);
      unsigned bad = (unsigned)(vi >> 32) ^ tag;
#pragma unroll
      for (int i = 0; i < 3; ++i) {
        const int e = min(256 * i + tid, 639);
#pragma unroll
        for (int k = 0; k < 4; ++k) bad |= (v[i][k][1] ^ tag) | (v[i][k][3] ^ tag);
        const unsigned pr = pk_bf16((u2f(v[i][0][0]) + u2f(v[i][1][0])) + (u2f(v[i][2][0]) + u2f(v[i][3][0])), (u2f(v[i][0][2]) + u2f(v[i][1][2])) + (u2f(v[i][2][2]) + u2f(v[i][3][2])));
        if (e < 320) *(unsigned*)(sbL + 2 * e) = pr; else *(f32x2*)(c1L + 2 * (e - 320)) = f32x2{rbf(1.0f + bflo(pr)), rbf(1.0f + bfhi(pr))};
      }
      PIN(bad);
      if (__all(bad == 0u)) break;
      __builtin_amdgcn_s_sleep(1);
    }
    ORDER();
    __syncthreads();
    f32x4 us[5];
    const int so = blk < 54 ? O_WINF + (2 * blk + (fp >> 3)) * 20480 + (rgp >> 2) * 1024 + ((rgp & 3) * 16 + (fp & 7)) * 16 : OOB;
    auto unit = [&](auto ic_) __attribute__((always_inline)) {
      constexpr int i = ic_;
      const int e = rgp + 16 * i, uo = so + i * 4096;
      const f32x4 ca = *(const f32x4*)(c1L + 8 * e), cb = *(const f32x4*)(c1L + 8 * e + 4);
      const v4u sb = *(const v4u*)(sbL + 8 * e);
      unsigned ta[4], tb[4], ra[4], rb[4];
#pragma unroll
      for (int j = 0; j < 4; ++j) {
        const f32x2 cc = j == 0 ? f32x2{ca[0], ca[1]} : j == 1 ? f32x2{ca[2], ca[3]} : j == 2 ? f32x2{cb[0], cb[1]} : f32x2{cb[2], cb[3]};
        const f32x2 ya = f32x2{bflo(wv[i][2 * j]), bflo(wv[i][2 * j + 1])} * cc, yb = f32x2{bfhi(wv[i][2 * j]), bfhi(wv[i][2 * j + 1])} * cc;
        ra[j] = pk_bf16(ya[0], ya[1]); rb[j] = pk_bf16(yb[0], yb[1]);
      }
      bst1(rs, uo, v4u{ra[0], ra[1], ra[2], ra[3]});
      bst1(rs, uo + 128, v4u{rb[0], rb[1], rb[2], rb[3]});
      float u1a = 0.f, u1b = 0.f, u2a = 0.f, u2b = 0.f;
#pragma unroll
      for (int j = 0; j < 4; ++j) {
        ta[j] = plo(wv[i][2 * j], wv[i][2 * j + 1]); tb[j] = phi(wv[i][2 * j], wv[i][2 * j + 1]);
        u1a = dot2(ra[j], ONES, u1a); u1b = dot2(rb[j], ONES, u1b);
        u2a = dot2(ta[j], sb[j], u2a); u2b = dot2(tb[j], sb[j], u2b);
      }
      us[i] = f32x4{u1a, u2a, u1b, u2b};
    };
    sfor<5>(unit);
    if (blk == 54) {
#pragma unroll
      for (int i = 0; i < 5; ++i) bst1(rs, O_WINF + 108 * 20480 + i * 4096 + tid * 16, v4u{ONES, ONES, ONES, ONES});
    }
    *(f32x4*)(red + tid * 4) = ((us[0] + us[1]) + (us[2] + us[3])) + us[4];
    __syncthreads();
    if (tid < 64) {
      const int c = (tid >> 1) & 15;
      float s = 0.f;
#pragma unroll
      for (int r = 0; r < 16; ++r) s += red[r * 64 + ((tid >> 5) * 8 + (c & 7)) * 4 + (c >> 3) * 2 + (tid & 1)];
      if (blk < 54) __builtin_amdgcn_raw_buffer_store_b32(f2u(s), rs, O_U + 32 * blk * 8 + tid * 4, 0, AUX_SC1);
    }
  } else {
    const int wi = (a >= 84 ? a - 84 : a - 44) * 4 + w;
    if (wi < 135) wout_unit(p, rs, smem + 16384 + w * 8192, wi, lane);
    if (a >= 84) {
#pragma unroll 2
      for (int q = a - 84; q < 864; q += 12) bst1(rs, O_P + q * 4096 + tid * 16, v4u{SENT, SENT, SENT, SENT});
    }
#pragma unroll 1
    for (unsigned s = 0; s < SPIN; ++s) {
      const u64 v = __hip_atomic_load((GAS u64*)(p.ws + O_INIT), RLX_AGENT);
      if (__all((unsigned)(v >> 32) == tag)) break;
      __builtin_amdgcn_s_sleep(1);
    }
    ORDER();
  }
  DRAIN();
  __syncthreads();
  arrive(p, a < 56 ? C_S : C_F, tid);
  STAMP(2);
}

template <int CTRL> DEVI float dpp_add(float keep, float send) { return keep + dppf<CTRL>(send); }
DEVI float bfly16(const float (&v)[16], int i16) {
  float a[8], b[4], c[2];
  const bool h8 = i16 & 8, h4 = i16 & 4, h2 = i16 & 2, h1 = i16 & 1;
#pragma unroll
  for (int k = 0; k < 8; ++k) a[k] = dpp_add<0x128>(h8 ? v[k + 8] : v[k], h8 ? v[k] : v[k + 8]);
#pragma unroll
  for (int k = 0; k < 4; ++k) b[k] = dpp_add<0x141>(h4 ? a[k + 4] : a[k], h4 ? a[k] : a[k + 4]);
#pragma unroll
  for (int k = 0; k < 2; ++k) c[k] = dpp_add<0x4E>(h2 ? b[k + 2] : b[k], h2 ? b[k] : b[k + 2]);
  return dpp_add<0xB1>(h1 ? c[1] : c[0], h1 ? c[0] : c[1]);
}
DEVI f32x2 gelu2(f32x2 v) {
  const float c0 = -2.0f * 0.7978845608028654f * 1.4426950408889634f, c1 = c0 * 0.044715f;
  const f32x2 u = v * (v * v * c1 + c0);
  const f32x2 d = f32x2{__builtin_amdgcn_exp2f(u[0]), __builtin_amdgcn_exp2f(u[1])} + 1.0f;
  return v * f32x2{__builtin_amdgcn_rcpf(d[0]), __builtin_amdgcn_rcpf(d[1])};
}
__global__ void __launch_bounds__(256, 1) k_fused(Params p) {
  extern __shared__ __attribute__((aligned(16))) unsigned char smem[];
  const int tid = threadIdx.x, lane = tid & 63, w = __builtin_amdgcn_readfirstlane(tid >> 6);
  const int i16 = lane & 15, g = lane >> 4, l16 = lane * 16;
  const int bx = blockIdx.x & 7, bi = blockIdx.x >> 3;
  const rsrc_t rs = __builtin_amdgcn_make_buffer_rsrc((void*)p.ws, 0, O_END, 0x00020000);
  const rsrc_t rw = __builtin_amdgcn_make_buffer_rsrc((void*)p.w_sp, 0, T * T * 2, 0x00020000);
  u64 ts_[20];
  STAMP(0);
  unsigned tag;
  const unsigned lds0 = (unsigned)(size_t)(LAS unsigned char*)smem;
  u64 pc_; unsigned csz_;
  asm volatile("s_getpc_b64 %0\n.Lkf_beg:\n\ts_mov_b32 %1, .Lkf_end-.Lkf_beg" : "=s"(pc_), "=s"(csz_));
  auto code_prefetch = [&]() __attribute__((always_inline)) {
    const rsrc_t rc = __builtin_amdgcn_make_buffer_rsrc((void*)(pc_ & ~15ull), 0, (int)csz_, 0x00020000);
    if (w < 3) dma16(rc, l16, (bi * 3 + w) * 1024, lds0 + L_DUMMY + w * 1024);
  };
  const bool is_conv = bi < 20;
  const int q4 = bx * 20 + bi;
  const int panel = (bx >> 1) * 8 + (bi >> 2), j = (bx & 1) * 4 + (bi & 3);
  const int b0 = blk0(j);
  const int lb0 = (w < 3) ? 2 * w : 6, lb1 = 2 * w + 1;
  const bool has0 = (w < 3) || (j < 6), has1 = (w < 3);
  const int z2s0 = has0 ? O_WINF + (54 + b0 + lb0) * 20480 : OOB, z2s1 = O_WINF + (has1 ? 54 + b0 + lb1 : 108) * 20480;
  const int z1s0 = has0 ? O_WINF + (b0 + lb0) * 20480 : OOB, z1s1 = has1 ? O_WINF + (b0 + lb1) * 20480 : OOB;
  const int wx = w ^ (j & 3);
  const int wso = (64 * panel + 32 * (wx >> 1) + 4 * (wx & 1)) * 4096 + w * 1024;
#define WSP_DMA(lt, r, ldsoff) dma16x<(r) * 16>(rw, l16, (wso ^ ((32 * ((lt) >> 1) + 4 * ((lt) & 1)) * 4096)) + (8 * ((r) >> 2) + ((r) & 3)) * 4096, lds0 + (ldsoff) + w * 16384 + (r) * 1024)
  const int wrd = w * 16384 + i16 * 1024 + (g ^ (i16 & 3)) * 16;
#define W_ADDR(s) (wrq[(s) & 3] + ((s) >> 2) * 256)
  const int zu0 = has0 ? O_Z2N + (2 * panel) * 1024 + (b0 + lb0) * 65536 : OOB, zu1 = has1 ? O_Z2N + (2 * panel) * 1024 + (b0 + lb1) * 65536 : OOB;
  const int pu = O_P + ((4 * panel) * 27 + (w < 3 ? 3 * j + w : 24 + (j >> 1))) * 1024;
  const int pu16 = w < 3 ? pu : OOB, pu8 = (w == 3 && j < 6) ? pu + (j & 1) * 8 : OOB;
  v4u WR[32];
  int wrq[4];
#pragma unroll
  for (int q = 0; q < 4; ++q) wrq[q] = wrd + ((q ^ (i16 >> 2)) * 64);
#define WR_RD(q, dst) WR[dst] = *(const v4u*)(smem + L_WL + W_ADDR(q))
  v4u R[40];
#define R_LD(q, s0, s1) R[q] = bld1(rs, l16 + (((q) >> 1) & 3) * 1024, (((q) & 1) ? (s1) : (s0)) + ((q) >> 3) * 4096)
  bool oks = false;
  if (__builtin_expect(is_conv, 1)) {
    const int d = 4 * q4 + w;
    unsigned par[6];
    par[0] = NTL((const unsigned*)(p.a_re + d * NST + 2 * lane)); par[1] = NTL((const unsigned*)(p.a_im + d * NST + 2 * lane));
    par[2] = NTL((const unsigned*)(p.b_re + d * NST + 2 * lane)); par[3] = NTL((const unsigned*)(p.b_im + d * NST + 2 * lane));
    par[4] = NTL((const unsigned*)(p.c_re + d * NST + 2 * lane)); par[5] = NTL((const unsigned*)(p.c_im + d * NST + 2 * lane));
    const float ldt = bf2f(p.log_dt[d]);
    SB();
    v4u mv[5]; unsigned cv[5];
    const int mcg = q4 >> 2, mkq = q4 & 3;
#pragma unroll
    for (int i = 0; i < 5; ++i) {
      const int row = 160 * mkq + 32 * i + (tid >> 3);
      mv[i] = __builtin_nontemporal_load((const v4u*)(p.w_mod + (size_t)row * 3840 + 64 * mcg + (mcg >= 20 ? D : 0) + 8 * (tid & 7)));
      cv[i] = p.cvec[row];
    }
    const unsigned ep = __hip_atomic_load((GAS unsigned*)(p.ws + O_EPOCH), RLX_AGENT);
    const unsigned dku = p.dskip[d];
    v2u xa[8];
    v4u st[4][2]; u64 mg;
    const int mo = lane < 32 ? O_MODP + ((lane & 3) * 2560 + (lane >> 4) * D + 4 * q4 + ((lane >> 2) & 3)) * 8 : (lane < 36 ? O_MODG + (2 * D + 4 * q4 + (lane & 3)) * 8 : O_INIT);
    auto gather = [&]() __attribute__((always_inline)) {
      mg = __hip_atomic_load((GAS u64*)(p.ws + mo), RLX_AGENT);
#pragma unroll
      for (int i = 0; i < 4; ++i) { st[i][0] = bld1(rs, O_STG + (256 * i + tid) * 32); st[i][1] = bld1(rs, O_STG + (256 * i + tid) * 32 + 16); }
    };
    const rsrc_t ru = __builtin_amdgcn_make_buffer_rsrc((void*)p.u, 0, T * D * 2, 0x00020000);
    auto xld = [&](int i) __attribute__((always_inline)) { xa[i] = __builtin_amdgcn_raw_buffer_load_b64(ru, tid * (4 * D), (512 * (i >> 1) + (i & 1)) * (2 * D) + 8 * q4, 0); };
    auto hook = [&](int i) __attribute__((always_inline)) {
      SB();
      if (i == MODHOOK) {
        tag = ep + 1u;
        float acc[8];
#pragma unroll
        for (int k = 0; k < 8; ++k) acc[k] = 0.f;
#pragma unroll
        for (int q = 0; q < 5; ++q) {
          const unsigned slo = silu_bf(bf2f(cv[q])), shi = slo << 16;
#pragma unroll
          for (int k = 0; k < 4; ++k) { acc[2 * k] = dot2(mv[q][k], slo, acc[2 * k]); acc[2 * k + 1] = dot2(mv[q][k], shi, acc[2 * k + 1]); }
        }
        const float s = col_reduce((float*)(smem + L_WL), acc, tid);
        if ((tid & 3) == 0) bst1_64(rs, O_MODP + (mkq * 2560 + 64 * mcg + (tid >> 2)) * 8, v2u{f2u(s), tag});
        SB();
        code_prefetch();
      }
      if (i == 2) xld(2);
      if (i == 4) { xld(3); xld(4); }
      if (i >= 5 && i <= 7) xld(i);
      if (i == 9) gather();
      SB();
    };
    SB();
    xld(0); xld(1);
    SB();
    u16* himg = (u16*)(smem + L_HIMG);
    if (tid < 128) *(v4u*)(himg + (tid >> 5) * HL + (tid & 31) * 8) = v4u{0, 0, 0, 0};
    const float dt = rbf(__builtin_amdgcn_exp2f(ldt * 1.4426950408889634f));
    kgen_wave(par, dt, bf2f(dku), (unsigned*)(smem + L_KSCR) + w * 16 * RS, smem + L_KIMG + w * KIMG_B, lane, hook);
    STAMP(1);
#pragma unroll 1
    for (unsigned s = 0; s < SPIN; ++s) {
      unsigned bad = (lane >> 2) == 8 ? 0u : (unsigned)(mg >> 32) ^ tag;
#pragma unroll
      for (int i = 0; i < 4; ++i) bad |= (st[i][0][1] ^ tag) | (st[i][0][3] ^ tag) | (st[i][1][1] ^ tag) | (st[i][1][3] ^ tag);
      PIN(bad);
      if (__all(bad == 0u)) break;
      asm volatile("" ::: "memory");
      gather();
    }
    ORDER();
    STAMP(2);
    float sa[4], c1[4];
    {
      float ms = u2f((unsigned)mg);
      ms += dppf<0xB1>(ms); ms += dppf<0x4E>(ms);
      const unsigned mr = f2u(rbf(ms));
#pragma unroll
      for (int c = 0; c < 4; ++c) {
        sa[c] = u2f(__builtin_amdgcn_readlane(mr, 4 * c));
        c1[c] = rbf(1.0f + u2f(__builtin_amdgcn_readlane(mr, 16 + 4 * c)));
      }
    }
    if (!__all((unsigned)(mg >> 32) == tag)) mg = __hip_atomic_load((GAS u64*)(p.ws + mo), RLX_AGENT);
    SB();
#pragma unroll
    for (int i = 0; i < 4; ++i) {
      const int pr = 256 * i + tid;
      const f32x2 m = {u2f(st[i][0][0]), u2f(st[i][1][0])}, r = {u2f(st[i][0][2]), u2f(st[i][1][2])};
      const f32x2 x[4] = {f32x2{bflo(xa[2 * i][0]), bflo(xa[2 * i + 1][0])}, f32x2{bfhi(xa[2 * i][0]), bfhi(xa[2 * i + 1][0])},
                          f32x2{bflo(xa[2 * i][1]), bflo(xa[2 * i + 1][1])}, f32x2{bfhi(xa[2 * i][1]), bfhi(xa[2 * i + 1][1])}};
#pragma unroll
      for (int c = 0; c < 4; ++c) {
        const f32x2 h = (x[c] - m) * r * c1[c] + sa[c];
        *(unsigned*)(himg + c * HL + 256 + 2 * pr) = pk_bf16(h[0], h[1]);
      }
    }
    unsigned ab[8];
#pragma unroll
    for (int a = 0; a < 8; ++a) { ab[a] = lds0 + L_KIMG + w * KIMG_B + (i16 & 1) * (KL * 2) + (2032 - (i16 & ~1) - 2 * (i16 & 1) + 8 * g) * 2 - 64 * (8 * a + 7); PIN(ab[a]); }
    unsigned bb = lds0 + L_HIMG + (w * HL + 256 - 16 + 16 * i16 + 8 * g) * 2 - 64 * 7; PIN(bb);
#pragma unroll 1
    for (unsigned s = 0; s < SPIN; ++s) {
      if (__all((unsigned)(mg >> 32) == tag)) break;
      mg = __hip_atomic_load((GAS u64*)(p.ws + mo), RLX_AGENT);
    }
    float ga[4];
#pragma unroll
    for (int c = 0; c < 4; ++c) ga[c] = u2f(__builtin_amdgcn_readlane((unsigned)mg, 32 + c));
    __syncthreads();
    STAMP(3);
    f32x4 acc[8];
#pragma unroll
    for (int c = 0; c < 8; ++c) acc[c] = f32x4{0.f, 0.f, 0.f, 0.f};
    v4u O[2][16];
#define CLOAD1(buf, r, k) { if constexpr (CL[k] < 8) { const LAS unsigned* ap = (const LAS unsigned*)(size_t)ab[CL[k] & 7] + 16 * (7 - (r)); O[buf][CL[k]] = v4u{ap[0], ap[1], ap[2], ap[3]}; } \
                            else O[buf][CL[k]] = *((const LAS v4u*)(size_t)bb + 4 * (8 * (CL[k] - 8) + 7 - (r))); }
    sfor<16>(LAM { constexpr int k = ic_; CLOAD1(0, 0, k); });
    SB();
    unsigned cav = 0;
    u16* ybuf = (u16*)(smem + L_KSCR);
    f32x2 xf[4][4];
#define Y_OUT(c) *(v2u*)(ybuf + w * T + 256 * (c) + 16 * i16 + 4 * g) = v2u{pk_bf16(acc[c][0], acc[c][1]), pk_bf16(acc[c][2], acc[c][3])}
    sfor<8 * 36>(LAM {
      constexpr int r = ic_ / 36, m = ic_ % 36;
      if constexpr (m == 0) TS(r);
      constexpr int ca = r < 7 ? CA[m] : CA7[m], cb = r < 7 ? CB[m] : CB7[m];
      acc[ca + cb] = mfma_bf(O[r & 1][ca], O[r & 1][8 + cb], acc[ca + cb]);
      if constexpr (r < 7 && (m & 1) == 0 && m < 32) CLOAD1((r + 1) & 1, r + 1, m >> 1);
      if constexpr (cs_dma(ic_)) WSP_DMA(0, (ic_ - A0) / ASP, L_WL);
      if constexpr ((r == 5 || r == 6) && (m & 3) == 3 && m < 32) {
        constexpr int q = 8 * (r - 5) + (m >> 2), i = q >> 2, c = q & 3;
        xf[i][c] = (c & 1) ? f32x2{bfhi(xa[2 * i][c >> 1]), bfhi(xa[2 * i + 1][c >> 1])} : f32x2{bflo(xa[2 * i][c >> 1]), bflo(xa[2 * i + 1][c >> 1])};
        PIN(xf[i][c]);
      }
      if constexpr (r == 7) {
        if constexpr (m == 11) Y_OUT(7);
        if constexpr (m == 18) Y_OUT(6);
        if constexpr (m == 24) Y_OUT(5);
        if constexpr (m == 29) Y_OUT(4);
        if constexpr (m == 33) Y_OUT(3);
        if constexpr (m == 34) cav = cnt_load(p, C_S, lane);
      }
      SB();
    });
    TS(8); TSDUMP(0, 9);
    Y_OUT(2); Y_OUT(1); Y_OUT(0);
    __syncthreads();
    STAMP(4);
#pragma unroll
    for (int i = 0; i < 4; ++i) {
      const int pr = 256 * i + tid;
      f32x2 o[4];
#pragma unroll
      for (int c = 0; c < 4; ++c) o[c] = xf[i][c] + unp(*(const unsigned*)(ybuf + c * T + 2 * pr)) * ga[c];
      bst1(rs, O_X1G + (q4 * T + 2 * pr) * 8, v4u{pk_bf16(o[0][0], o[1][0]), pk_bf16(o[2][0], o[3][0]), pk_bf16(o[0][1], o[1][1]), pk_bf16(o[2][1], o[3][1])});
    }
    SB();
    oks = __all(cav >= 7u);
    ORDER();
    STAMP(5);
  } else {
    aux_role(p, rs, smem, (bi - 20) * 8 + bx, tag);
    code_prefetch();
    sfor<16>(LAM { WSP_DMA(0, ic_, L_WL); });
    DRAIN();
  }
#pragma unroll
  for (int q = 0; q < 16; ++q) WR_RD(q, q);
  int otab;
  {
    const int lf = lane & 7;
    int ab = 2 * w + lf; ab = ab >= 7 ? ab - 7 : ab;
    const int zo = (ab == 6 && j >= 6) ? OOB : O_Z2N + (b0 + ab) * 65536 + 16 * w * 1024;
    const int sl = (ab * 3 + ((w - (ab >> 1) - 1) & 3)) * 4096;
    const int ro = (sl < 65536 ? L_WL + sl : sl) + ((w ^ (ab >> 1)) << 10);
    const int n = 6 * w + lf - 1;
    const int ko = (lf == 0 ? 3 * j + min(w, 2) : (n < 3 * j ? n : n + 3)) * 1024;
    const int lt = lane & 3;
    const int po = (lane & 4) ? ((lt ^ w) * 3 + lt - 1) * 5120 : O_P + (4 * panel + (lt ^ w)) * (27 * 1024);
    const int sel = (lane >> 3) & 3;
    otab = sel == 0 ? zo : (sel == 1 ? ro : (sel == 2 ? ko : po));
    PIN(otab);
  }
  asm volatile("s_waitcnt lgkmcnt(0)" ::: "memory");
  if (is_conv) {
    DRAIN();
    __syncthreads();
    arrive(p, C_X1, tid);
  }
  if (!oks) cnt_wait<4>(p, C_S, 7u, lane);
  {
    const rsrc_t rb = __builtin_amdgcn_make_buffer_rsrc((void*)p.b_sp, 0, T * 2, 0x00020000);
    if (w < 3) dma16s(rs, l16, w == 0 ? O_U + (FH + 16 * b0) * 8 : (w == 1 ? O_U + 16 * b0 * 8 : OOB), lds0 + L_DUMMY + w * 1024);
    else dma16(rb, l16, 128 * panel, lds0 + L_DUMMY + 3072);
  }
#pragma unroll
  for (int q = 0; q < NR1; ++q) R_LD(q, z2s0, z2s1);
  SB();
  sfor<16>(LAM { WSP_DMA(1, ic_, L_WL); });
  SB();
  STAMP(6);
  const int xoff = ((lane >> 5) * T + ((2 * (lane & 31)) ^ (4 * (w & 1)))) * 8;
  const int xso = O_X1G + (2 * w * T + 64 * panel) * 8;
#define X_DMA(ks) dma16s(rs, xoff, xso + (ks) * (8 * T * 8), lds0 + ((ks) * 4 + w) * 1024)
  float* sxL = (float*)(smem + L_MISC);
  float* st1 = (float*)(smem + L_MISC + 512) + w * 128;
  float* gsum = (float*)(smem + L_MISC + 2560);
  float* st2 = (float*)(smem + L_MISC + 4608) + w * 128;
  const unsigned xrb = lds0 + g * 1024 + (8 * (i16 >> 2) + (i16 & 3) + 4 * (g & 1)) * 8;
  unsigned xl[4], xh[4];
#pragma unroll
  for (int i = 0; i < 4; ++i) { xl[i] = xrb ^ (((i ^ wx) & 1) * 32) ^ (((i ^ wx) >> 1) * 256); xh[i] = xl[i] + 512; PIN(xl[i]); PIN(xh[i]); }
  auto ldx = [&](unsigned lo, unsigned hi, int off) __attribute__((always_inline)) {
    const v2u a = *(const LAS v2u*)(size_t)(lo + off), c = *(const LAS v2u*)(size_t)(hi + off);
    return v4u{a[0], a[1], c[0], c[1]};
  };
  f32x4 a0[4], a1[4], gram = {0.f, 0.f, 0.f, 0.f};
#pragma unroll
  for (int i = 0; i < 4; ++i) { a0[i] = f32x4{0.f, 0.f, 0.f, 0.f}; a1[i] = a0[i]; }
  if (w == 3) cnt_wait<2>(p, C_X1, 20u, lane);
  __syncthreads();
  STAMP(7);
#pragma unroll
  for (int ks = 0; ks < 8; ++ks) X_DMA(ks);
  asm volatile("s_waitcnt vmcnt(4)\n\ts_barrier" ::: "memory");
  STAMP(8);
  {
    v4u A[3][4];
    sfor<8>(LAM { A[ic_ >> 2][ic_ & 3] = ldx(xl[ic_ & 3], xh[ic_ & 3], (ic_ >> 2) * 4096); SB(); });
    sfor<20 * 9>(LAM {
      constexpr int ks = ic_ / 9, m = ic_ % 9;
      if constexpr (m == 0 && (ks & 1) == 0) TS(ks >> 1);
      if constexpr (m == 0 && ks == 2) asm volatile("s_waitcnt vmcnt(6)\n\ts_barrier" ::: "memory");
      if constexpr (m == 0 && (ks == 6 || ks == 10)) asm volatile("s_waitcnt vmcnt(12)\n\ts_barrier" ::: "memory");
      if constexpr (m == 0 && ks == 8) { sfor<4>([&](auto i_) __attribute__((always_inline)) { xl[i_] += 40960; xh[i_] += 40960; PIN(xl[i_]); PIN(xh[i_]); }); }
      if constexpr (m == 0) __builtin_amdgcn_s_waitcnt(0xC87F);
      if constexpr (m == 2) gram = mfma_bf(A[ks % 3][0], A[ks % 3][0], gram);
      else {
        constexpr int mm = m < 2 ? m : m - 1;
        if constexpr (mm & 1) a1[mm >> 1] = mfma_bf(A[ks % 3][mm >> 1], R[2 * ks + 1], a1[mm >> 1]);
        else a0[mm >> 1] = mfma_bf(A[ks % 3][mm >> 1], R[2 * ks], a0[mm >> 1]);
      }
      if constexpr ((m & 1) == 1 && ks < 18) A[(ks + 2) % 3][m >> 1] = ldx(xl[m >> 1], xh[m >> 1], (ks + 2 - (ks >= 8 ? 10 : 0)) * 4096);
      if constexpr (ks >= 18 && m < 8) WR_RD(8 * (ks - 18) + m, 16 + 8 * (ks - 18) + m);
      if constexpr ((m == 2 || m == 4 || m == 8) && ks < 4) X_DMA(8 + 3 * ks + (m >> 2));
      if constexpr ((m == 4 || m == 8) && ks >= 4) { constexpr int n = NR1 + 2 * (ks - 4) + (m == 8); if constexpr (n < 40) R_LD(n, z2s0, z2s1); else R_LD(n - 40, z1s0, z1s1); }
      SB();
    });
    TS(10); TSDUMP(16, 11);
  }
  if (w == 3) {
    if (i16 == 0) {
#pragma unroll
      for (int i = 0; i < 4; ++i) *(f32x4*)(sxL + (i ^ wx) * 16 + 4 * g) = a1[i];
    }
#pragma unroll
    for (int i = 0; i < 4; ++i) a1[i] = f32x4{0.f, 0.f, 0.f, 0.f};
  }
  if ((i16 >> 2) == g) sxL[64 + wx * 16 + i16] = (i16 & 3) == 0 ? gram[0] : (i16 & 3) == 1 ? gram[1] : (i16 & 3) == 2 ? gram[2] : gram[3];
  __syncthreads();
  STAMP(9);
  {
    const float m = sxL[lane] * (1.0f / D);
    const float var = fmaxf(sxL[64 + lane] * (1.0f / D) - m * m, 0.f);
    const float r = __builtin_amdgcn_rsqf(var + 1e-5f);
    st1[lane] = r; st1[64 + lane] = r * m;
  }
  v4u A1[3][4];
  {
    const f32x2* cU2 = (const f32x2*)(smem + L_DUMMY);
    f32x2 u0 = cU2[16 * lb0 + i16], u1 = cU2[16 * lb1 + i16];
    if (!has0) u0 = f32x2{0.f, 0.f};
    if (!has1) u1 = f32x2{0.f, 0.f};
    float s1[16], s2[16];
    f32x4 rr[4], rm[4];
#pragma unroll
    for (int i = 0; i < 4; ++i) { rr[i] = *(const f32x4*)(st1 + (i ^ wx) * 16 + 4 * g); rm[i] = *(const f32x4*)(st1 + 64 + (i ^ wx) * 16 + 4 * g); }
    sfor<8>(LAM {
      constexpr int i = ic_ >> 1, h = ic_ & 1;
      { constexpr int n0 = NR1 - 8, per = (40 - n0) / 8;
#pragma unroll
        for (int q = n0 + per * ic_; q < n0 + per * (ic_ + 1); ++q) R_LD(q, z1s0, z1s1); }
      const f32x2 r2 = {rr[i][2 * h], rr[i][2 * h + 1]}, m2 = {rm[i][2 * h], rm[i][2 * h + 1]};
      const f32x2 v0 = gelu2(f32x2{a0[i][2 * h], a0[i][2 * h + 1]} * r2 + (u0[1] - m2 * u0[0]));
      const f32x2 v1 = gelu2(f32x2{a1[i][2 * h], a1[i][2 * h + 1]} * r2 + (u1[1] - m2 * u1[0]));
      a0[i][2 * h] = v0[0]; a0[i][2 * h + 1] = v0[1]; a1[i][2 * h] = v1[0]; a1[i][2 * h + 1] = v1[1];
      f32x2 q1 = v0 + v1, q2 = v0 * v0 + v1 * v1;
      PIN(q1); PIN(q2);
      s1[4 * i + 2 * h] = q1[0]; s1[4 * i + 2 * h + 1] = q1[1]; s2[4 * i + 2 * h] = q2[0]; s2[4 * i + 2 * h + 1] = q2[1];
      SB();
    });
    sfor<4>(LAM { xl[ic_] -= 40960; xh[ic_] -= 40960; PIN(xl[ic_]); PIN(xh[ic_]); A1[0][ic_] = ldx(xl[ic_], xh[ic_], 0); SB(); A1[1][ic_] = ldx(xl[ic_], xh[ic_], 4096); SB(); });
    const float t1 = bfly16(s1, i16), t2 = bfly16(s2, i16);
    const int tp = ((i16 >> 2) ^ wx) * 16 + 4 * g + (i16 & 3);
    bst1(rs, O_ZST + (((panel * 4 + w) * 8 + j) * 64 + tp) * 16, v4u{f2u(t1), tag, f2u(t2), tag});
  }
  SB();
  STAMP(10);
  f32x4 c0[4], c1[4];
#pragma unroll
  for (int i = 0; i < 4; ++i) { c0[i] = f32x4{0.f, 0.f, 0.f, 0.f}; c1[i] = c0[i]; }
#define Z1M(ks, m) { if constexpr ((m) == 0 && (ks) >= 2 && (ks) < 19 && (ks) != KA && (ks) != KA + 1) __builtin_amdgcn_s_waitcnt(0xC87F); \
    if constexpr ((m) & 1) c1[(m) >> 1] = mfma_bf(R[2 * (ks) + 1], A1[(ks) % 3][(m) >> 1], c1[(m) >> 1]); else c0[(m) >> 1] = mfma_bf(R[2 * (ks)], A1[(ks) % 3][(m) >> 1], c0[(m) >> 1]); \
    if constexpr (((m) & 1) == 1 && (ks) < 18) A1[((ks) + 2) % 3][(m) >> 1] = ldx(xl[(m) >> 1], xh[(m) >> 1], ((ks) + 2 - ((ks) >= KA ? KA : 0)) * 4096); }
  constexpr int KA = KAV;
  v4u zs[8];
  unsigned cfv = 0;
  const int zsoff = O_ZST + ((panel * 4 + w) * 8 * 64) * 16;
  sfor<KA * 8>(LAM {
    constexpr int ks = ic_ / 8, m = ic_ % 8;
    Z1M(ks, m);
    if constexpr ((m == 2 || m == 6) && ks < 8) { constexpr int q = 2 * ks + (m == 6); WSP_DMA(2, q, L_WL); }
    if constexpr (m == 4 && ks == 7) cfv = cnt_load(p, C_F, lane);
    if constexpr ((m & 1) == 0 && ks >= 8 && ks < 10) { constexpr int jj = 4 * (ks - 8) + (m >> 1); zs[jj] = bld1(rs, l16 + (jj & 3) * 1024, zsoff + (jj >> 2) * 4096); }
    SB();
  });
  v2u x1v[5];
#pragma unroll
  for (int db = 0; db < 5; ++db) {
    const int d = 80 * j + 16 * db + 4 * g;
    x1v[db] = *(const v2u*)(smem + (d >> 2) * 512 + ((32 * (w >> 1) + 8 * (i16 >> 2) + 4 * (w & 1) + (i16 & 3)) ^ (4 * ((d >> 3) & 1))) * 8);
  }
  STAMP(11);
  {
#pragma unroll 1
    for (unsigned s = 0; s < SPIN; ++s) {
      unsigned bad = 0;
#pragma unroll
      for (int jj = 0; jj < 8; ++jj) bad |= (zs[jj][1] ^ tag) | (zs[jj][3] ^ tag);
      PIN(bad);
      if (__all(bad == 0u)) break;
      asm volatile("" ::: "memory");
#pragma unroll
      for (int jj = 0; jj < 8; ++jj) zs[jj] = bld1(rs, l16, zsoff + jj * 1024);
    }
    float a = 0.f, b = 0.f;
#pragma unroll
    for (int jj = 0; jj < 8; ++jj) { a += u2f(zs[jj][0]); b += u2f(zs[jj][2]); }
    *(f32x2*)(gsum + (w * 64 + lane) * 2) = f32x2{a, b};
  }
  if (!__all(cfv >= 5u)) cnt_wait<1>(p, C_F, 5u, lane);
  ORDER();
  __syncthreads();
  STAMP(12);
  {
    const f32x2 s = (*(const f32x2*)(gsum + lane * 2) + *(const f32x2*)(gsum + 128 + lane * 2)) + (*(const f32x2*)(gsum + 256 + lane * 2) + *(const f32x2*)(gsum + 384 + lane * 2));
    const float m = s[0] * (1.0f / FH);
    const float var = fmaxf(s[1] * (1.0f / FH) - m * m, 0.f);
    const float r = __builtin_amdgcn_rsqf(var + 1e-5f);
    st2[lane] = r; st2[64 + lane] = r * m;
  }
  {
    unsigned o0[4][2], o1[4][2];
#pragma unroll
    for (int i = 0; i < 4; ++i) {
      const f32x4 rr = *(const f32x4*)(st2 + (i ^ wx) * 16 + 4 * g), rm = *(const f32x4*)(st2 + 64 + (i ^ wx) * 16 + 4 * g);
      const f32x4 n0 = a0[i] * rr - rm, n1 = a1[i] * rr - rm;
      o0[i][0] = pk_bf16(n0[0], n0[1]); o0[i][1] = pk_bf16(n0[2], n0[3]);
      o1[i][0] = pk_bf16(n1[0], n1[1]); o1[i][1] = pk_bf16(n1[2], n1[3]);
    }
    const bool sw = wx & 1;
    const int ph = (wx >> 1) * 1024;
#pragma unroll
    for (int pp = 0; pp < 2; ++pp) {
      const int e = 2 * pp, o = 2 * pp + 1;
      bst1(rs, zu0 + ((pp * 1024) ^ ph) + l16, sw ? v4u{o0[o][0], o0[o][1], o0[e][0], o0[e][1]} : v4u{o0[e][0], o0[e][1], o0[o][0], o0[o][1]});
      bst1(rs, zu1 + ((pp * 1024) ^ ph) + l16, sw ? v4u{o1[o][0], o1[o][1], o1[e][0], o1[e][1]} : v4u{o1[e][0], o1[e][1], o1[o][0], o1[o][1]});
    }
  }
  sfor<4>(LAM { xl[ic_] += KA * 4096; xh[ic_] += KA * 4096; PIN(xl[ic_]); PIN(xh[ic_]); });
  SB();
  STAMP(13);
  sfor<(20 - KA) * 8>(LAM {
    constexpr int ks = KA + ic_ / 8, m = ic_ % 8;
    Z1M(ks, m);
    SB();
  });
  DRAIN();
  __syncthreads();
  GAS unsigned* flags = (GAS unsigned*)(p.ws + O_FLAG);
  if (tid < 4) __hip_atomic_fetch_or(flags + ((j * 4 + (panel >> 3)) * 4 + tid) * 64, 1u << (panel & 7), RLX_AGENT);
  if (w == 2) dma16s(rs, l16, O_MODG + (5 * D + 80 * j) * 8, lds0 + L_DUMMY + 2048);
  STAMP(14);
  int zof[7], rof[7], kof[7];
#pragma unroll
  for (int q = 0; q < 7; ++q) { zof[q] = __builtin_amdgcn_readlane(otab, q); rof[q] = __builtin_amdgcn_readlane(otab, 8 + q); kof[q] = __builtin_amdgcn_readlane(otab, 16 + q); }
  constexpr int ZD = ZDV;
  v4u Z[16][7];
#define Z_LD(i, f) Z[i][f] = bld1(rs, l16 + ((i) & 3) * 1024, zof[f] + ((i) >> 2) * 4096)
  unsigned zmask = 0u, zfv = 0u;
  GAS unsigned* zfp = flags + ((j * 4 + w) * 4 + (panel >> 3)) * 64;
  auto zneed = [&](unsigned bits) __attribute__((always_inline)) {
    if (__builtin_expect((zmask & bits) != bits, 0)) {
#pragma unroll 1
      for (unsigned s = 0; s < SPIN; ++s) {
        zmask = __builtin_amdgcn_readfirstlane(__hip_atomic_load(zfp, RLX_AGENT));
        if ((zmask & bits) == bits) break;
        __builtin_amdgcn_s_sleep(1);
      }
    }
    ORDER();
  };
  unsigned z1p[2][4][2];
  {
    const v4u* cU1 = (const v4u*)(smem + L_DUMMY + 1024);
    const v4u ua0 = cU1[8 * lb0 + 2 * g], ua1 = cU1[8 * lb0 + 2 * g + 1], ub0 = cU1[8 * lb1 + 2 * g], ub1 = cU1[8 * lb1 + 2 * g + 1];
    float sr[4], sm[4];
#pragma unroll
    for (int i = 0; i < 4; ++i) { sr[i] = st1[16 * (i ^ wx) + i16]; sm[i] = st1[64 + 16 * (i ^ wx) + i16]; }
    sfor<8>(LAM {
      constexpr int i = ic_ >> 1, h = ic_ & 1;
      if constexpr (ic_ < 4) { WSP_DMA(3, 4 * ic_, 0); WSP_DMA(3, 4 * ic_ + 1, 0); WSP_DMA(3, 4 * ic_ + 2, 0); WSP_DMA(3, 4 * ic_ + 3, 0); }
      if constexpr (ic_ == ZPOLL - 2) { zfv = __hip_atomic_load(zfp, RLX_AGENT); SB(); }
      if constexpr (ic_ == ZPOLL) { STAMP(15); zmask = __builtin_amdgcn_readfirstlane(zfv); zneed(ZFIRST); STAMP(16); }
      if constexpr (ic_ >= ZPOLL) {
        constexpr int n = 8 - ZPOLL, k = ic_ - ZPOLL;
#pragma unroll
        for (int q = 14 * k / n; q < 14 * (k + 1) / n; ++q) Z_LD(q / 7, q % 7);
        SB();
      }
      const v4u qa = h ? ua1 : ua0, qb = h ? ub1 : ub0;
      const f32x2 v0 = gelu2(f32x2{c0[i][2 * h], c0[i][2 * h + 1]} * sr[i] + (f32x2{u2f(qa[1]), u2f(qa[3])} - f32x2{u2f(qa[0]), u2f(qa[2])} * sm[i]));
      const f32x2 v1 = gelu2(f32x2{c1[i][2 * h], c1[i][2 * h + 1]} * sr[i] + (f32x2{u2f(qb[1]), u2f(qb[3])} - f32x2{u2f(qb[0]), u2f(qb[2])} * sm[i]));
      z1p[0][i][h] = pk_bf16(v0[0], v0[1]); z1p[1][i][h] = pk_bf16(v1[0], v1[1]);
      PIN(z1p[0][i][h]); PIN(z1p[1][i][h]);
    });
  }
  SB();
  f32x4 acc[7][4];
  const int woso = O_WOUTF + (5 * j) * 27 * 1024;
  v4u WO[35];
  v4u WL[4][2];
#define WO_LD(q) WO[q] = bld1(rs, l16, woso + ((q) % 5) * (27 * 1024) + kof[(q) / 5])
#define WL_RD(i, h) WL[(i) & 3][h] = *(const v4u*)(smem + ((h) ? 0 : L_WL) + W_ADDR(i))
  WL_RD(0, 0); WL_RD(0, 1);
  sfor<13 * 28>(LAM {
    constexpr int i = ic_ / 28, f = (ic_ % 28) / 4, t = ic_ % 4;
    if constexpr (f == 0 && t == 0) TS(i);
    constexpr int lo = zs_lo(i, ZD), hi = zs_lo(i + 1, ZD);
    if constexpr (f == 0 && t == 0 && hi > lo && ((lo & 1) == 0 || hi - lo > 1)) zneed((1u << ((hi - 1) >> 1)) | (1u << (lo >> 1)));
    if constexpr (t < 2) mfma_pin<i == 0, true>(acc[f][t], Z[i][f], WR[t * 16 + i]);
    else mfma_pin<i == 0, false>(acc[f][t], Z[i][f], WL[i & 3][t - 2]);
    if constexpr (hi - lo == 2 && (t == 0 || t == 2)) { constexpr int q = 2 * f + (t >> 1); Z_LD(lo + q / 7, q % 7); }
    if constexpr (hi - lo == 1 && t == 0) Z_LD(lo, f);
    if constexpr (t == 1 && f < 2 && i < 12) WL_RD(i + 1, f);
    if constexpr (t == 1 && f < 6 && i == 12) WL_RD(13 + (f >> 1), f & 1);
    SB();
  });
  TS(13);
  asm volatile("s_nop 7\n\ts_waitcnt lgkmcnt(0)\n\ts_barrier" ::: "memory");
  sfor<7 * 12>(LAM {
    constexpr int fo = ic_ / 12, f = (fo + 2) % 7, i = 13 + (ic_ % 12) / 4, t = ic_ % 4, m = ic_ % 12;
    if constexpr (t < 2) acc[f][t] = mfma_bf(Z[i][f], WR[t * 16 + i], acc[f][t]);
    else acc[f][t] = mfma_bf(Z[i][f], WL[i & 3][t - 2], acc[f][t]);
    if constexpr (m % 4 == 0 && 3 * fo + m / 4 < NWO) WO_LD(3 * fo + m / 4);
    if constexpr (fo >= 1 && fo <= 5 && m % 3 == 2) { constexpr int pf = fo + 1, pt = m / 3; *(f32x4*)(smem + ((rof[pf] + l16) ^ (pt << 10))) = acc[pf][pt]; }
    SB();
  });
  TS(14); TSDUMP(32, 15);
  if (w == 3) {
#pragma unroll
    for (int t = 0; t < 4; ++t) *(f32x4*)(smem + ((rof[1] + l16) ^ (t << 10))) = acc[1][t];
  }
  f32x4 o0[4], o1[4];
#pragma unroll
  for (int t = 0; t < 4; ++t) {
    const float bias = bf2f(((const u16*)(smem + L_DUMMY + 3072))[8 * (i16 >> 2) + (i16 & 3) + 32 * ((t ^ wx) >> 1) + 4 * ((t ^ wx) & 1)]);
    o0[t] = acc[0][t] + bias; o1[t] = acc[1][t] + bias; PIN(o0[t]); PIN(o1[t]);
  }
  __syncthreads();
  STAMP(17);
  v4u PO[4];
  {
    f32x4 pa[4][3], pb[4][3];
#pragma unroll
    for (int s = 0; s < 3; ++s) {
      const int s0 = (2 * w * 3 + s) * 4096, s1 = ((w < 3 ? 2 * w + 1 : 6) * 3 + s) * 4096;
      const unsigned char* r0 = smem + (s0 < 65536 ? L_WL + s0 : s0) + l16;
      const unsigned char* r1 = smem + (s1 < 65536 ? L_WL + s1 : s1) + l16;
#pragma unroll
      for (int t = 0; t < 4; ++t) { pa[t][s] = *(const f32x4*)(r0 + t * 1024); pb[t][s] = *(const f32x4*)(r1 + t * 1024); }
    }
    SB();
#pragma unroll
    for (int t = 0; t < 4; ++t) {
      const f32x4 x0 = (o0[t] + pa[t][0]) + (pa[t][1] + pa[t][2]), x1 = (o1[t] + pb[t][0]) + (pb[t][1] + pb[t][2]);
      const f32x2 qa = unp(z1p[0][t][0]) * f32x2{x0[0], x0[1]}, qb = unp(z1p[0][t][1]) * f32x2{x0[2], x0[3]};
      const f32x2 qc = unp(z1p[1][t][0]) * f32x2{x1[0], x1[1]}, qd = unp(z1p[1][t][1]) * f32x2{x1[2], x1[3]};
      PO[t] = v4u{min(pk_bf16(qa[0], qa[1]), SENT - 1u), min(pk_bf16(qb[0], qb[1]), SENT - 1u), min(pk_bf16(qc[0], qc[1]), SENT - 1u), min(pk_bf16(qd[0], qd[1]), SENT - 1u)};
      bst1(rs, pu16 + (t ^ wx) * (27 * 1024) + l16, PO[t]);
      bst1_64(rs, pu8 + (t ^ wx) * (27 * 1024) + l16, v2u{PO[t][0], PO[t][1]});
    }
  }
  SB();
  STAMP(18);
  if (w == 3) {
#pragma unroll
    for (int t = 0; t < 4; ++t) PO[t] = v4u{0u, 0u, 0u, 0u};
  }
  if (j & 1) { const v4u a = PO[0], c = PO[2]; PO[0] = PO[1]; PO[1] = a; PO[2] = PO[3]; PO[3] = c; }
  if (j & 2) { const v4u a = PO[0], c = PO[1]; PO[0] = PO[2]; PO[2] = a; PO[1] = PO[3]; PO[3] = c; }
  f32x4 e0[5][4], e[5][4];
#pragma unroll
  for (int t = 0; t < 4; ++t)
#pragma unroll
    for (int d = 0; d < 5; ++d) e0[d][t] = mfma_bf(WO[d], PO[t], f32x4{0.f, 0.f, 0.f, 0.f});
  const int trow = 64 * panel + 8 * (i16 >> 2) + (i16 & 3) + 32 * (w >> 1) + 4 * (w & 1);
  int pof[4], eof[4];
#pragma unroll
  for (int q = 0; q < 4; ++q) { pof[q] = __builtin_amdgcn_readlane(otab, 24 + q); eof[q] = __builtin_amdgcn_readlane(otab, 28 + q); }
  const int ppo = lane < 48 ? O_P + (4 * panel + ((lane >> 1) & 3)) * (27 * 1024) + __builtin_amdgcn_ds_bpermute((17 + (lane >> 3)) * 4, otab) + 1012 + (lane & 1) * 8 : OOB;
  v4u gb0[5], gb1[5];
#pragma unroll
  for (int d = 0; d < 5; ++d) { gb0[d] = *(const v4u*)(smem + L_DUMMY + 2048 + (16 * d + 4 * g) * 8); gb1[d] = *(const v4u*)(smem + L_DUMMY + 2048 + (16 * d + 4 * g) * 8 + 16); }
  SB();
#pragma unroll
  for (int q = NWO; q < 35; ++q) WO_LD(q);
  SB();
  STAMP(19);
  v4u P[24];
#define P_LD(q) P[q] = bld1(rs, l16, pof[((q) / 6 + 1) & 3] + kof[1 + (q) % 6])
  constexpr int PDN = PDNV;
#pragma unroll 1
  for (unsigned tries = 0; tries < 4u; ++tries) {
#pragma unroll 1
    for (unsigned s = 0; s < SPIN; ++s) {
      asm volatile("" ::: "memory");
      const unsigned m = __builtin_amdgcn_raw_buffer_load_b32(rs, ppo, 0, AUX_SC1);
#pragma unroll
      for (int q = 0; q < PDN; ++q) P_LD(q);
      if (!__any(m == SENT)) break;
    }
    asm volatile("" ::: "memory");
    STAMP(20);
    unsigned pm = 0u;
    sfor<24 * 5>(LAM {
      constexpr int q = ic_ / 5, d = ic_ % 5, lo = q / 6, lt = (lo + 1) & 3, kk = 1 + q % 6;
      e[d][lt] = mfma_bf(WO[5 * kk + d], P[q], kk == 1 ? e0[d][lt] : e[d][lt]);
      if constexpr (d == 0 && q + PDN < 24) P_LD(q + PDN);
      if constexpr (d == 1) { pm = mx4(pm, P[q]); PIN(pm); }
      if constexpr (lo >= 1 && d == 3 && q % 6 < 5) *(f32x4*)(smem + eof[lo] + (q % 6) * 1024 + l16) = e[q % 6][lo];
      SB();
    });
    if (__builtin_expect(!__any(pm == SENT), 1)) break;
  }
  STAMP(21);
  __syncthreads();
  const rsrc_t ro_ = __builtin_amdgcn_make_buffer_rsrc((void*)p.out, 0, T * D * 2, 0x00020000);
  f32x4 ep[5][3];
#pragma unroll
  for (int d = 0; d < 5; ++d)
#pragma unroll
    for (int s = 0; s < 3; ++s) ep[d][s] = *(const f32x4*)(smem + ((w * 3 + s) * 5 + d) * 1024 + l16);
  SB();
#pragma unroll
  for (int d = 0; d < 5; ++d) {
    const f32x4 eo = (e[d][0] + ep[d][0]) + (ep[d][1] + ep[d][2]);
    const float r0 = bflo(x1v[d][0]) + u2f(gb0[d][0]) * eo[0], r1 = bfhi(x1v[d][0]) + u2f(gb0[d][2]) * eo[1];
    const float r2 = bflo(x1v[d][1]) + u2f(gb1[d][0]) * eo[2], r3 = bfhi(x1v[d][1]) + u2f(gb1[d][2]) * eo[3];
    __builtin_amdgcn_raw_buffer_store_b64(v2u{pk_bf16(r0, r1), pk_bf16(r2, r3)}, ro_, (trow * D + 80 * j + 4 * g + 16 * d) * 2, 0, 16);
  }
  if (blockIdx.x == 0 && tid == 0) __hip_atomic_store((GAS unsigned*)(p.ws + O_EPOCH), tag, RLX_AGENT);
  STAMP(22);
  asm volatile(".Lkf_end:");
}

extern "C" void kernel_launch(void* const* d_in, const int* in_sizes, int n_in, void* d_out, int out_size, void* d_ws,
                              size_t ws_size, hipStream_t stream) {
  Params p;
  p.u = (const u16*)d_in[0]; p.a_re = (const u16*)d_in[1]; p.a_im = (const u16*)d_in[2];
  p.b_re = (const u16*)d_in[3]; p.b_im = (const u16*)d_in[4]; p.c_re = (const u16*)d_in[5]; p.c_im = (const u16*)d_in[6];
  p.log_dt = (const u16*)d_in[7]; p.dskip = (const u16*)d_in[8];
  p.w_in = (const u16*)d_in[10]; p.w_sp = (const u16*)d_in[11]; p.b_sp = (const u16*)d_in[12]; p.w_out = (const u16*)d_in[13];
  p.cvec = (const u16*)d_in[14]; p.w_mod = (const u16*)d_in[15];
  p.out = (u16*)d_out; p.ws = (unsigned char*)d_ws;
  (void)hipFuncSetAttribute((const void*)k_fused, hipFuncAttributeMaxDynamicSharedMemorySize, L_TOTAL);
  k_fused<<<256, 256, L_TOTAL, stream>>>(p);
}
```

```cpp
#include <hip/hip_runtime.h>
#include <hip/hip_bf16.h>
#include <stdint.h>
#include <utility>

typedef unsigned v4u __attribute__((ext_vector_type(4)));
typedef unsigned v2u __attribute__((ext_vector_type(2)));
typedef float f32x4 __attribute__((ext_vector_type(4)));
typedef float f32x2 __attribute__((ext_vector_type(2)));
typedef short bf16x8 __attribute__((ext_vector_type(8)));
typedef _Float16 f16x8 __attribute__((ext_vector_type(8)));
typedef _Float16 f16x2 __attribute__((ext_vector_type(2)));
typedef __bf16 bf16x2_t __attribute__((ext_vector_type(2)));
typedef unsigned short u16;
typedef unsigned long long u64;
#define GAS __attribute__((address_space(1)))
#define LAS __attribute__((address_space(3)))
#define RLX_AGENT __ATOMIC_RELAXED, __HIP_MEMORY_SCOPE_AGENT
#define DEVI __device__ __forceinline__
#define SB() __builtin_amdgcn_sched_barrier(0)
template <int... I, class F> __device__ __forceinline__ void sfor_impl(std::integer_sequence<int, I...>, F&& f) { (f(std::integral_constant<int, I>{}), ...); }
template <int N, class F> __device__ __forceinline__ void sfor(F&& f) { sfor_impl(std::make_integer_sequence<int, N>{}, f); }
#define LAM [&](auto ic_) __attribute__((always_inline))
#define NTL(p) __builtin_nontemporal_load(p)
#define DRAIN() asm volatile("s_waitcnt vmcnt(0)" ::: "memory")

constexpr int T = 2048, D = 640, NST = 128, FH = 864, F2 = 1728;
constexpr int KL = 2080, KIMG_B = 2 * KL * 2, HL = 2304;
constexpr unsigned SPIN = 1u << 15;
constexpr int AUX_SC1 = 16, AUX_NT = 2;
constexpr int O_EPOCH = 0;
constexpr int O_INIT = 256;
constexpr int O_CNT = 4096;
constexpr int O_FLAG = O_CNT + 3 * 64 * 256;
constexpr int O_MODG = O_FLAG + 256 * 256;
constexpr int O_MODP = O_MODG + 3840 * 8 + 2048;
constexpr int O_STG = O_MODP + 4 * 2560 * 8;
constexpr int O_U = O_STG + 2048 * 16;
constexpr int O_ZST = O_U + 16384;
constexpr int O_X1G = O_ZST + 32 * 32 * 64 * 16;
constexpr int O_WINF = O_X1G + T * D * 2;
constexpr int O_WOUTF = O_WINF + 109 * 20 * 1024;
constexpr int O_Z2N = O_WOUTF + 40 * 27 * 1024;
constexpr int O_P = O_Z2N + 54 * 64 * 1024;
constexpr int O_END = O_P + 128 * 27 * 1024;
constexpr int C_X1 = 0, C_S = 1, C_F = 2;
constexpr unsigned SENT = 0xffffffffu;

DEVI float u2f(unsigned u) { return __builtin_bit_cast(float, u); }
DEVI unsigned f2u(float f) { return __builtin_bit_cast(unsigned, f); }
DEVI float bf2f(unsigned b) { return u2f(b << 16); }
DEVI float bflo(unsigned u) { return u2f(u << 16); }
DEVI float bfhi(unsigned u) { return u2f(u & 0xffff0000u); }
DEVI unsigned pk_bf16(float a, float b) {
  f32x2 v = {a, b};
  bf16x2_t r = __builtin_convertvector(v, bf16x2_t);
  return __builtin_bit_cast(unsigned, r);
}
DEVI float rbf(float a) { return bflo(pk_bf16(a, 0.f)); }
DEVI unsigned pk_f16(float a, float b) {
  f32x2 v = {a, b};
  f16x2 r = __builtin_convertvector(v, f16x2);
  return __builtin_bit_cast(unsigned, r);
}
DEVI f32x4 mfma_bf(v4u a, v4u b, f32x4 c) {
  return __builtin_amdgcn_mfma_f32_16x16x32_bf16(__builtin_bit_cast(bf16x8, a), __builtin_bit_cast(bf16x8, b), c, 0, 0, 0);
}
DEVI f32x4 mfma_h(v4u a, v4u b, f32x4 c) {
  return __builtin_amdgcn_mfma_f32_16x16x32_f16(__builtin_bit_cast(f16x8, a), __builtin_bit_cast(f16x8, b), c, 0, 0, 0);
}
template <bool FIRST, bool BA> DEVI void mfma_pin(f32x4& acc, const v4u& a, const v4u& bb) {
  if constexpr (FIRST) {
    if constexpr (BA) asm volatile("v_mfma_f32_16x16x32_bf16 %0, %1, %2, 0" : "=a"(acc) : "v"(a), "a"(bb));
    else asm volatile("v_mfma_f32_16x16x32_bf16 %0, %1, %2, 0" : "=a"(acc) : "v"(a), "v"(bb));
  } else {
    if constexpr (BA) asm volatile("v_mfma_f32_16x16x32_bf16 %0, %1, %2, %0" : "+a"(acc) : "v"(a), "a"(bb));
    else asm volatile("v_mfma_f32_16x16x32_bf16 %0, %1, %2, %0" : "+a"(acc) : "v"(a), "v"(bb));
  }
}
typedef __amdgpu_buffer_rsrc_t rsrc_t;
DEVI v4u bld1(rsrc_t rs, int voff, int soff = 0) { return __builtin_amdgcn_raw_buffer_load_b128(rs, voff, soff, AUX_SC1); }
DEVI void bst1(rsrc_t rs, int voff, v4u v) { __builtin_amdgcn_raw_buffer_store_b128(v, rs, voff, 0, AUX_SC1); }
DEVI void bst1_64(rsrc_t rs, int voff, v2u v) { __builtin_amdgcn_raw_buffer_store_b64(v, rs, voff, 0, AUX_SC1); }
template <int CTRL> DEVI float dppf(float x) { return u2f((unsigned)__builtin_amdgcn_update_dpp(0, (int)f2u(x), CTRL, 0xf, 0xf, true)); }
DEVI float dot2(unsigned a, unsigned b, float c) { return __builtin_amdgcn_fdot2_f32_bf16(__builtin_bit_cast(bf16x2_t, a), __builtin_bit_cast(bf16x2_t, b), c, false); }
DEVI unsigned mx4(unsigned m, v4u v) { return max(max(max(m, v[0]), v[1]), max(v[2], v[3])); }
#define PIN(x) asm volatile("" : "+v"(x))
#define ORDER() __builtin_amdgcn_fence(__ATOMIC_ACQUIRE, "wavefront")

struct Params {
  const u16 *u, *a_re, *a_im, *b_re, *b_im, *c_re, *c_im, *log_dt, *dskip, *w_in, *w_sp, *b_sp, *w_out, *cvec, *w_mod;
  u16* out;
  unsigned char* ws;
};

DEVI int blk0(int j) { return j < 6 ? 7 * j : 42 + 6 * (j - 6); }
DEVI int pair_blk(int ks3, int h) {
  if (ks3 < 24) { const int j = ks3 / 3, w = ks3 - 3 * j; return blk0(j) + 2 * w + h; }
  return 7 * (2 * (ks3 - 24) + h) + 6;
}
DEVI void arrive(const Params& p, int c, int tid) {
  if (tid < 8) __hip_atomic_fetch_add((GAS unsigned*)(p.ws + O_CNT) + ((c * 8 + tid) * 8 + (blockIdx.x & 7)) * 64, 1u, RLX_AGENT);
}
DEVI unsigned cnt_load(const Params& p, int c, int lane) {
  return __hip_atomic_load((GAS unsigned*)(p.ws + O_CNT) + ((c * 8 + ((blockIdx.x >> 3) & 7)) * 8 + (lane & 7)) * 64, RLX_AGENT);
}
template <int SLP> DEVI void cnt_wait(const Params& p, int c, unsigned want, int lane) {
#pragma unroll 1
  for (unsigned s = 0; s < SPIN; ++s) {
    if (__all(cnt_load(p, c, lane) >= want)) break;
    __builtin_amdgcn_s_sleep(SLP);
  }
  ORDER();
}
struct C2 { f32x2 re, im; };
DEVI C2 cmul2(C2 a, C2 b) { return C2{a.re * b.re - a.im * b.im, a.re * b.im + a.im * b.re}; }
DEVI C2 cexp2(f32x2 are2, f32x2 arev, float l) {
  const f32x2 e = are2 * l, q = arev * l;
  const f32x2 m = {__builtin_amdgcn_exp2f(e[0]), __builtin_amdgcn_exp2f(e[1])};
  const float p0 = __builtin_amdgcn_fractf(q[0]), p1 = __builtin_amdgcn_fractf(q[1]);
  return C2{m * f32x2{__builtin_amdgcn_cosf(p0), __builtin_amdgcn_cosf(p1)}, m * f32x2{__builtin_amdgcn_sinf(p0), __builtin_amdgcn_sinf(p1)}};
}
constexpr int RS = 132;
DEVI void put_row(unsigned* buf, int row, int lane, C2 v) { *(v2u*)(buf + row * RS + 2 * lane) = v2u{pk_f16(v.re[0], v.im[0]), pk_f16(v.re[1], v.im[1])}; }
DEVI f32x2 unp(unsigned u) { return f32x2{bflo(u), bfhi(u)}; }

template <class HK> DEVI void kgen_wave(unsigned par[6], float dt, float dsk, unsigned* buf0, unsigned char* img, int lane, HK&& hook) {
  unsigned* buf1 = (unsigned*)img;
  u16* tab = (u16*)buf0;
  const int c = lane & 15, g = lane >> 4;
  const f32x2 are2 = unp(par[0]) * (dt * 1.4426950408889634f), arev = unp(par[1]) * (dt * 0.15915494309189535f), nrev = -arev;
  C2 W = cmul2(C2{unp(par[4]), unp(par[5])}, C2{unp(par[2]) * dt, unp(par[3]) * dt});
  float mx = fmaxf(fmaxf(fabsf(W.re[0]), fabsf(W.re[1])), fmaxf(fabsf(W.im[0]), fabsf(W.im[1])));
  unsigned mu = f2u(mx);
  mu = max(mu, f2u(dppf<0x128>(u2f(mu)))); mu = max(mu, f2u(dppf<0x124>(u2f(mu)))); mu = max(mu, f2u(dppf<0x122>(u2f(mu)))); mu = max(mu, f2u(dppf<0x121>(u2f(mu))));
  const unsigned m0 = __builtin_amdgcn_readlane(mu, 0), m1 = __builtin_amdgcn_readlane(mu, 16), m2 = __builtin_amdgcn_readlane(mu, 32), m3 = __builtin_amdgcn_readlane(mu, 48);
  unsigned eb = (max(max(m0, m1), max(m2, m3)) >> 23) & 0xffu;
  eb = eb < 2u ? 2u : (eb > 250u ? 250u : eb);
  const float sc = u2f((eb + 1u) << 23), isc = u2f((253u - eb) << 23);
  W.re *= isc; W.im *= isc;
  {
    const C2 e8 = cexp2(are2, arev, 8.f);
    C2 W8 = cmul2(W, cexp2(are2, arev, 64.f));
#pragma unroll
    for (int b = 0; b < 8; ++b) {
      put_row(buf0, b, lane, W); put_row(buf0, 8 + b, lane, W8);
      if (b < 7) { W = cmul2(W, e8); W8 = cmul2(W8, e8); }
      if (b == 0) hook(2);
      if (b == 4) hook(3);
    }
  }
  v4u A[8];
#pragma unroll
  for (int ks = 0; ks < 8; ++ks) A[ks] = *(const v4u*)(buf0 + c * RS + 16 * ks + 4 * g);
  C2 er[8];
  er[0] = C2{f32x2{1.f, 1.f}, f32x2{0.f, 0.f}};
  er[1] = cexp2(are2, nrev, 1.f);
  er[2] = cmul2(er[1], er[1]); er[3] = cmul2(er[2], er[1]); er[4] = cmul2(er[2], er[2]);
  er[5] = cmul2(er[4], er[1]); er[6] = cmul2(er[3], er[3]); er[7] = cmul2(er[4], er[3]);
  hook(4);
  const C2 e128 = cexp2(are2, nrev, 128.f);
#pragma unroll
  for (int r = 0; r < 8; ++r) { put_row(buf0, r, lane, er[r]); put_row(buf0, 8 + r, lane, r ? cmul2(e128, er[r]) : e128); }
  f32x4 acc0 = {0.f, 0.f, 0.f, 0.f}, acc1 = acc0, acc2 = acc0;
#pragma unroll
  for (int ks = 0; ks < 8; ++ks) acc0 = mfma_h(A[ks], *(const v4u*)(buf0 + c * RS + 16 * ks + 4 * g), acc0);
  hook(5);
  C2 e = cmul2(e128, e128);
  C2 f = cexp2(are2, nrev, 1024.f);
#pragma unroll
  for (int a = 0; a < 2; ++a) {
#pragma unroll
    for (int r = 0; r < 4; ++r) put_row(buf0, 4 * a + r, lane, r ? cmul2(e, er[2 * r]) : e);
    e = cmul2(e, e128);
  }
  hook(6);
#pragma unroll
  for (int a = 0; a < 4; ++a) {
    put_row(buf0, 8 + 2 * a, lane, e); put_row(buf0, 9 + 2 * a, lane, cmul2(e, er[4])); if (a < 3) e = cmul2(e, e128);
    put_row(buf1, 2 * a, lane, f); f = cmul2(f, e128); put_row(buf1, 2 * a + 1, lane, f); f = cmul2(f, e128);
    if (a == 1) hook(7);
  }
  put_row(buf1, 8, lane, f);
  hook(8);
  {
    v4u B1[8], B2[8];
#pragma unroll
    for (int ks = 0; ks < 8; ++ks) { B1[ks] = *(const v4u*)(buf0 + c * RS + 16 * ks + 4 * g); B2[ks] = *(const v4u*)(buf1 + c * RS + 16 * ks + 4 * g); }
    SB();
#pragma unroll
    for (int ks = 0; ks < 8; ++ks) { acc1 = mfma_h(A[ks], B1[ks], acc1); acc2 = mfma_h(A[ks], B2[ks], acc2); }
  }
  hook(9);
  const int cv0 = 128 * (c >> 3) + (c & 7);
  const int cv1 = c < 8 ? 256 + 128 * (c >> 2) + 2 * (c & 3) : 512 + 128 * ((c - 8) >> 1) + 4 * (c & 1);
  const int cv2 = c < 9 ? 1024 + 128 * c : 2048;
#pragma unroll
  for (int r = 0; r < 4; ++r) {
    const int lb = 8 * (4 * g + r);
    const unsigned q01 = pk_bf16(acc0[r] * sc + ((r == 0 && lane == 0) ? dsk : 0.f), acc1[r] * sc);
    tab[lb + cv0] = (u16)q01;
    tab[lb + cv1] = (u16)(q01 >> 16);
    tab[lb + cv2 + (c > 8 ? 1 : 0)] = (u16)pk_bf16(acc2[r] * sc, 0.f);
  }
  {
    const u16* t0 = tab + 2048 - 8 * lane;
    const int o = lane < 32 ? 1 : 0;
    const unsigned H0 = t0[0], L0 = t0[-8], H1 = t0[-512], L1 = t0[-520], H2 = t0[-1024], M2 = t0[-1028], L2 = t0[-1032];
    const u16* t3 = t0 - 1536;
    const unsigned v0 = t3[0], v1 = t3[-1 + o], v2 = t3[-2], v3 = t3[-3 - o], v4 = t3[-4], v5 = t3[-5 + o], v6 = t3[-6], v7 = t3[-7 - o], v8 = t3[-8];
    const unsigned k0 = tab[0];
    SB();
    unsigned char* i0 = img + lane * 16;
    unsigned char* i1 = i0 + KL * 2;
    {
      const unsigned X = (lane & 1) ? L0 : H0, HH = H0 | (H0 << 16), LL = L0 | (L0 << 16);
      *(v4u*)i0 = v4u{HH, HH, X | (L0 << 16), LL};
      *(v4u*)i1 = v4u{HH, H0 | (X << 16), LL, LL};
    }
    {
      const unsigned X = (lane & 1) ? L1 : H1, HH = H1 | (H1 << 16), LL = L1 | (L1 << 16);
      *(v4u*)(i0 + 1024) = v4u{HH, HH, X | (L1 << 16), LL};
      *(v4u*)(i1 + 1024) = v4u{HH, H1 | (X << 16), LL, LL};
    }
    {
      const unsigned HH = H2 | (H2 << 16), MM = M2 | (M2 << 16), LL = L2 | (L2 << 16);
      *(v4u*)(i0 + 2048) = v4u{HH, H2 | (M2 << 16), MM, LL};
      *(v4u*)(i1 + 2048) = v4u{HH, MM, M2 | (L2 << 16), LL};
    }
    *(v4u*)(i0 + 3072) = v4u{v0 | (v1 << 16), v2 | (v3 << 16), v4 | (v5 << 16), v6 | (v7 << 16)};
    *(v4u*)(i1 + 3072) = v4u{v1 | (v2 << 16), v3 | (v4 << 16), v5 | (v6 << 16), v7 | (v8 << 16)};
    if (lane < 8) *(v4u*)(img + (lane >> 2) * (KL * 2) + (256 + (lane & 3)) * 16) = v4u{lane == 0 ? k0 : 0u, 0u, 0u, 0u};
  }
}

constexpr int L_KIMG = 0, L_HIMG = 4 * KIMG_B, L_KSCR = L_HIMG + 4 * HL * 2;
constexpr int L_MISC = 86016, L_WL = L_MISC + 8192, L_DUMMY = L_WL + 65536, L_TOTAL = L_DUMMY + 4096;
static_assert(L_TOTAL <= 163840 && L_KSCR + 4 * 16 * RS * 4 <= L_MISC, "lds");
constexpr int MODHOOK = 4;
constexpr int ASP = 8;
constexpr int KAV = 11;
constexpr int NR1 = 24;
constexpr int ZDV = 3;
constexpr int NWO = 21;
constexpr int PDNV = 8;
constexpr int ZPOLL = 6;
constexpr int OOB = 64 << 20;
constexpr int CA[36] = {3,3,3,3,3,4,4,4,4, 2,2,2,2,2,2,5,5,5, 1,1,1,1,6,6,1,1,1, 0,0,0,0,7,0,0,0,0};
constexpr int CB[36] = {4,3,2,1,0,3,2,1,0, 5,4,3,2,1,0,2,1,0, 6,5,4,3,1,0,2,1,0, 7,6,5,4,0,3,2,1,0};
constexpr int CA7[36] = {3,4,2,5,1,6,0,7, 3,4,2,5,1,6,0, 3,4,2,5,1,0, 3,4,2,1,0, 3,2,1,0, 2,1,0, 1,0, 0};
constexpr int CB7[36] = {4,3,5,2,6,1,7,0, 3,2,4,1,5,0,6, 2,1,3,0,4,5, 1,0,2,3,4, 0,1,2,3, 0,1,2, 0,1, 0};
constexpr int CL[16] = {8+4,3,8+3,8+2,8+1,8+0,4,8+5,2,5,8+6,1,6,8+7,0,7};

constexpr bool cs_dma(int ic) { return (ic % ASP) == 5 && ic / ASP < 16; }
constexpr int zs_lo(int i, int zd) {
  int v = i + zd; if (v > 2 + 2 * i) v = 2 + 2 * i; if (i >= 13 || v > 16) v = 16; return v;
}
DEVI void dma16s(rsrc_t r, int voff, int soff, unsigned ldsaddr) {
  asm volatile("s_mov_b32 m0, %2\n\ts_nop 0\n\tbuffer_load_dwordx4 %0, %1, %3 offen sc1 lds" :: "v"(voff), "s"(r), "s"(ldsaddr), "s"(soff) : "memory");
}
template <int X> DEVI void dma16x(rsrc_t r, int voff, int soff, unsigned ldsaddr) {
  int tmp;
  asm volatile("s_mov_b32 m0, %3\n\tv_xor_b32 %0, %5, %1\n\tbuffer_load_dwordx4 %0, %2, %4 offen lds" : "=&v"(tmp) : "v"(voff), "s"(r), "s"(ldsaddr), "s"(soff), "n"(X) : "memory");
}
DEVI void dma16(rsrc_t r, int voff, int soff, unsigned ldsaddr) {
  asm volatile("s_mov_b32 m0, %2\n\ts_nop 0\n\tbuffer_load_dwordx4 %0, %1, %3 offen lds" :: "v"(voff), "s"(r), "s"(ldsaddr), "s"(soff) : "memory");
}

DEVI unsigned plo(unsigned a, unsigned b) { return __builtin_amdgcn_perm(b, a, 0x05040100u); }
DEVI unsigned phi(unsigned a, unsigned b) { return __builtin_amdgcn_perm(b, a, 0x07060302u); }
DEVI unsigned silu_bf(float c) { return pk_bf16(c * __builtin_amdgcn_rcpf(1.0f + __builtin_amdgcn_exp2f(-1.4426950408889634f * c)), 0.f); }
DEVI float col_reduce(float* fl, const float (&acc)[8], int tid) {
  const int rg = tid >> 3, cl = tid & 7;
  *(f32x4*)(fl + rg * 68 + cl * 8) = f32x4{acc[0], acc[1], acc[2], acc[3]};
  *(f32x4*)(fl + rg * 68 + cl * 8 + 4) = f32x4{acc[4], acc[5], acc[6], acc[7]};
  __syncthreads();
  float s = 0.f;
#pragma unroll
  for (int r = 0; r < 8; ++r) s += fl[(8 * (tid & 3) + r) * 68 + (tid >> 2)];
  s += dppf<0xB1>(s); s += dppf<0x4E>(s);
  return s;
}
DEVI void wout_unit(const Params& p, rsrc_t rs, unsigned char* lw, int unit, int lane) {
  const int ks = unit / 5, dc = unit - 5 * ks, d = 128 * dc + 2 * lane;
  const int f0 = 16 * pair_blk(ks, 0), f1 = 16 * pair_blk(ks, 1);
  unsigned wv[4][8];
#pragma unroll
  for (int g = 0; g < 4; ++g)
#pragma unroll
    for (int j = 0; j < 8; ++j) wv[g][j] = NTL((const unsigned*)(p.w_out + (size_t)((j < 4 ? f0 : f1) + 2 * ((4 * g + (j & 3)) & 7) + (g >> 1)) * D + d));
#pragma unroll
  for (int g = 0; g < 4; ++g) {
    unsigned char* dst = lw + (lane >> 3) * 1024 + (g * 16 + 2 * (lane & 7)) * 16;
    *(v4u*)dst = v4u{plo(wv[g][0], wv[g][1]), plo(wv[g][2], wv[g][3]), plo(wv[g][4], wv[g][5]), plo(wv[g][6], wv[g][7])};
    *(v4u*)(dst + 16) = v4u{phi(wv[g][0], wv[g][1]), phi(wv[g][2], wv[g][3]), phi(wv[g][4], wv[g][5]), phi(wv[g][6], wv[g][7])};
  }
#pragma unroll
  for (int i = 0; i < 8; ++i) bst1(rs, O_WOUTF + ((8 * dc + i) * 27 + ks) * 1024 + lane * 16, *(const v4u*)(lw + i * 1024 + lane * 16));
}

DEVI void aux_role(const Params& p, rsrc_t rs, unsigned char* smem, int a, unsigned& tag) {
  const int tid = threadIdx.x, lane = tid & 63, w = __builtin_amdgcn_readfirstlane(tid >> 6);
  float* fl = (float*)smem;
  constexpr unsigned ONES = 0x3f803f80u;
  unsigned wv[5][8];
  const int blk = (a & 7) * 7 + (a >> 3), fp = tid & 15, rgp = tid >> 4;
  const u16* wsrc = p.w_in + (size_t)(8 * rgp) * F2 + 32 * min(blk, 53) + 2 * fp;
#define WIN_LD(i) { _Pragma("unroll") for (int j_ = 0; j_ < 8; ++j_) wv[i][j_] = NTL((const unsigned*)(wsrc + (128 * (i) + j_) * F2)); }
  if (a < 64) {
    const int row = 32 * a + 8 * w + (lane >> 3), c = lane & 7;
    v4u xv[10];
#pragma unroll
    for (int i = 0; i < 10; ++i) xv[i] = *(const v4u*)(p.u + (size_t)row * D + 64 * i + 8 * c);
    tag = __hip_atomic_load((GAS unsigned*)(p.ws + O_EPOCH), RLX_AGENT);
    SB();
    WIN_LD(0); WIN_LD(1);
    SB();
    float s0 = 0.f, s1 = 0.f, q0 = 0.f, q1 = 0.f;
#pragma unroll
    for (int i = 0; i < 10; ++i)
#pragma unroll
      for (int j = 0; j < 4; j += 2) {
        s0 = dot2(xv[i][j], ONES, s0); q0 = dot2(xv[i][j], xv[i][j], q0);
        s1 = dot2(xv[i][j + 1], ONES, s1); q1 = dot2(xv[i][j + 1], xv[i][j + 1], q1);
      }
    float m = s0 + s1, v = q0 + q1;
    m += dppf<0xB1>(m); v += dppf<0xB1>(v); m += dppf<0x4E>(m); v += dppf<0x4E>(v); m += dppf<0x141>(m); v += dppf<0x141>(v);
    m *= (1.0f / D);
    const float r = __builtin_amdgcn_rsqf(fmaxf(v * (1.0f / D) - m * m, 0.f) + 1e-5f);
    tag += 1u;
    if (c == 0) bst1(rs, O_STG + row * 16, v4u{f2u(m), tag, f2u(r), tag});
    SB();
    WIN_LD(2); WIN_LD(3); WIN_LD(4);
  } else if (a < 84) {
    const int rg = tid >> 3, cl = tid & 7, col0 = (a < 74 ? 2 * D + 64 * (a - 64) : 5 * D + 64 * (a - 74));
    unsigned* sL = (unsigned*)(smem + 12288);
    v4u mv[20];
    const v2u cv = *(const v2u*)(p.cvec + 4 * min(tid, 159));
#pragma unroll
    for (int i = 0; i < 20; ++i) mv[i] = __builtin_nontemporal_load((const v4u*)(p.w_mod + (size_t)(20 * rg + i) * 3840 + col0 + 8 * cl));
    tag = __hip_atomic_load((GAS unsigned*)(p.ws + O_EPOCH), RLX_AGENT);
    SB();
    if (tid < 160) *(v4u*)(sL + 4 * tid) = v4u{silu_bf(bflo(cv[0])), silu_bf(bfhi(cv[0])), silu_bf(bflo(cv[1])), silu_bf(bfhi(cv[1]))};
    __syncthreads();
    v4u s4[5];
#pragma unroll
    for (int i = 0; i < 5; ++i) s4[i] = *(const v4u*)(sL + 20 * rg + 4 * i);
    float acc[8];
#pragma unroll
    for (int j = 0; j < 8; ++j) acc[j] = 0.f;
#pragma unroll
    for (int i = 0; i < 20; ++i) {
      const unsigned slo = s4[i >> 2][i & 3], shi = slo << 16;
#pragma unroll
      for (int j = 0; j < 4; ++j) { acc[2 * j] = dot2(mv[i][j], slo, acc[2 * j]); acc[2 * j + 1] = dot2(mv[i][j], shi, acc[2 * j + 1]); }
    }
    const float s = col_reduce(fl, acc, tid);
    tag += 1u;
    if ((tid & 3) == 0) bst1_64(rs, O_MODG + (col0 + (tid >> 2)) * 8, v2u{f2u(rbf(s)), tag});
  } else {
    tag = __hip_atomic_load((GAS unsigned*)(p.ws + O_EPOCH), RLX_AGENT) + 1u;
    if (a == 92) {
      if (tid < 192) __hip_atomic_store((GAS unsigned*)(p.ws + O_CNT) + tid * 64, 0u, RLX_AGENT);
      __hip_atomic_store((GAS unsigned*)(p.ws + O_FLAG) + tid * 64, 0u, RLX_AGENT);
      DRAIN();
      __syncthreads();
      if (tid == 0) bst1_64(rs, O_INIT, v2u{0u, tag});
    }
  }
  if (a < 56) {
    float* c1L = (float*)(smem + 61440);
    u16* sbL = (u16*)(smem + 61440 + 2560);
    float* red = (float*)(smem + 16384);
#pragma unroll 1
    for (unsigned s = 0; s < SPIN; ++s) {
      asm volatile("" ::: "memory");
      v4u v[3][4];
#pragma unroll
      for (int i = 0; i < 3; ++i)
#pragma unroll
        for (int k = 0; k < 4; ++k) v[i][k] = bld1(rs, min(256 * i + tid, 639) * 16, O_MODP + (k * 2560 + 1280) * 8);
      const u64 vi = __hip_atomic_load((GAS u64*)(p.ws + O_INIT), RLX_AGENT);
      unsigned bad = (unsigned)(vi >> 32) ^ tag;
#pragma unroll
      for (int i = 0; i < 3; ++i) {
        const int e = min(256 * i + tid, 639);
#pragma unroll
        for (int k = 0; k < 4; ++k) bad |= (v[i][k][1] ^ tag) | (v[i][k][3] ^ tag);
        const unsigned pr = pk_bf16((u2f(v[i][0][0]) + u2f(v[i][1][0])) + (u2f(v[i][2][0]) + u2f(v[i][3][0])), (u2f(v[i][0][2]) + u2f(v[i][1][2])) + (u2f(v[i][2][2]) + u2f(v[i][3][2])));
        if (e < 320) *(unsigned*)(sbL + 2 * e) = pr; else *(f32x2*)(c1L + 2 * (e - 320)) = f32x2{rbf(1.0f + bflo(pr)), rbf(1.0f + bfhi(pr))};
      }
      PIN(bad);
      if (__all(bad == 0u)) break;
      __builtin_amdgcn_s_sleep(1);
    }
    ORDER();
    __syncthreads();
    f32x4 us[5];
    const int so = blk < 54 ? O_WINF + (2 * blk + (fp >> 3)) * 20480 + (rgp >> 2) * 1024 + ((rgp & 3) * 16 + (fp & 7)) * 16 : OOB;
    auto unit = [&](auto ic_) __attribute__((always_inline)) {
      constexpr int i = ic_;
      const int e = rgp + 16 * i, uo = so + i * 4096;
      const f32x4 ca = *(const f32x4*)(c1L + 8 * e), cb = *(const f32x4*)(c1L + 8 * e + 4);
      const v4u sb = *(const v4u*)(sbL + 8 * e);
      unsigned ta[4], tb[4], ra[4], rb[4];
#pragma unroll
      for (int j = 0; j < 4; ++j) {
        const f32x2 cc = j == 0 ? f32x2{ca[0], ca[1]} : j == 1 ? f32x2{ca[2], ca[3]} : j == 2 ? f32x2{cb[0], cb[1]} : f32x2{cb[2], cb[3]};
        const f32x2 ya = f32x2{bflo(wv[i][2 * j]), bflo(wv[i][2 * j + 1])} * cc, yb = f32x2{bfhi(wv[i][2 * j]), bfhi(wv[i][2 * j + 1])} * cc;
        ra[j] = pk_bf16(ya[0], ya[1]); rb[j] = pk_bf16(yb[0], yb[1]);
      }
      bst1(rs, uo, v4u{ra[0], ra[1], ra[2], ra[3]});
      bst1(rs, uo + 128, v4u{rb[0], rb[1], rb[2], rb[3]});
      float u1a = 0.f, u1b = 0.f, u2a = 0.f, u2b = 0.f;
#pragma unroll
      for (int j = 0; j < 4; ++j) {
        ta[j] = plo(wv[i][2 * j], wv[i][2 * j + 1]); tb[j] = phi(wv[i][2 * j], wv[i][2 * j + 1]);
        u1a = dot2(ra[j], ONES, u1a); u1b = dot2(rb[j], ONES, u1b);
        u2a = dot2(ta[j], sb[j], u2a); u2b = dot2(tb[j], sb[j], u2b);
      }
      us[i] = f32x4{u1a, u2a, u1b, u2b};
    };
    sfor<5>(unit);
    if (blk == 54) {
#pragma unroll
      for (int i = 0; i < 5; ++i) bst1(rs, O_WINF + 108 * 20480 + i * 4096 + tid * 16, v4u{ONES, ONES, ONES, ONES});
    }
    *(f32x4*)(red + tid * 4) = ((us[0] + us[1]) + (us[2] + us[3])) + us[4];
    __syncthreads();
    if (tid < 64) {
      const int c = (tid >> 1) & 15;
      float s = 0.f;
#pragma unroll
      for (int r = 0; r < 16; ++r) s += red[r * 64 + ((tid >> 5) * 8 + (c & 7)) * 4 + (c >> 3) * 2 + (tid & 1)];
      if (blk < 54) __builtin_amdgcn_raw_buffer_store_b32(f2u(s), rs, O_U + 32 * blk * 8 + tid * 4, 0, AUX_SC1);
    }
  } else {
    const int wi = (a >= 84 ? a - 84 : a - 44) * 4 + w;
    if (wi < 135) wout_unit(p, rs, smem + 16384 + w * 8192, wi, lane);
    if (a >= 84) {
#pragma unroll 2
      for (int q = a - 84; q < 864; q += 12) bst1(rs, O_P + q * 4096 + tid * 16, v4u{SENT, SENT, SENT, SENT});
    }
#pragma unroll 1
    for (unsigned s = 0; s < SPIN; ++s) {
      const u64 v = __hip_atomic_load((GAS u64*)(p.ws + O_INIT), RLX_AGENT);
      if (__all((unsigned)(v >> 32) == tag)) break;
      __builtin_amdgcn_s_sleep(1);
    }
    ORDER();
  }
  DRAIN();
  __syncthreads();
  arrive(p, a < 56 ? C_S : C_F, tid);
}

template <int CTRL> DEVI float dpp_add(float keep, float send) { return keep + dppf<CTRL>(send); }
DEVI float bfly16(const float (&v)[16], int i16) {
  float a[8], b[4], c[2];
  const bool h8 = i16 & 8, h4 = i16 & 4, h2 = i16 & 2, h1 = i16 & 1;
#pragma unroll
  for (int k = 0; k < 8; ++k) a[k] = dpp_add<0x128>(h8 ? v[k + 8] : v[k], h8 ? v[k] : v[k + 8]);
#pragma unroll
  for (int k = 0; k < 4; ++k) b[k] = dpp_add<0x141>(h4 ? a[k + 4] : a[k], h4 ? a[k] : a[k + 4]);
#pragma unroll
  for (int k = 0; k < 2; ++k) c[k] = dpp_add<0x4E>(h2 ? b[k + 2] : b[k], h2 ? b[k] : b[k + 2]);
  return dpp_add<0xB1>(h1 ? c[1] : c[0], h1 ? c[0] : c[1]);
}
DEVI f32x2 gelu2(f32x2 v) {
  const float c0 = -2.0f * 0.7978845608028654f * 1.4426950408889634f, c1 = c0 * 0.044715f;
  const f32x2 u = v * (v * v * c1 + c0);
  const f32x2 d = f32x2{__builtin_amdgcn_exp2f(u[0]), __builtin_amdgcn_exp2f(u[1])} + 1.0f;
  return v * f32x2{__builtin_amdgcn_rcpf(d[0]), __builtin_amdgcn_rcpf(d[1])};
}
__global__ void __launch_bounds__(256, 1) k_fused(Params p) {
  extern __shared__ __attribute__((aligned(16))) unsigned char smem[];
  const int tid = threadIdx.x, lane = tid & 63, w = __builtin_amdgcn_readfirstlane(tid >> 6);
  const int i16 = lane & 15, g = lane >> 4, l16 = lane * 16;
  const int bx = blockIdx.x & 7, bi = blockIdx.x >> 3;
  const rsrc_t rs = __builtin_amdgcn_make_buffer_rsrc((void*)p.ws, 0, O_END, 0x00020000);
  const rsrc_t rw = __builtin_amdgcn_make_buffer_rsrc((void*)p.w_sp, 0, T * T * 2, 0x00020000);
  unsigned tag;
  const unsigned lds0 = (unsigned)(size_t)(LAS unsigned char*)smem;
  u64 pc_; unsigned csz_;
  asm volatile("s_getpc_b64 %0\n.Lkf_beg:\n\ts_mov_b32 %1, .Lkf_end-.Lkf_beg" : "=s"(pc_), "=s"(csz_));
  auto code_prefetch = [&]() __attribute__((always_inline)) {
    const rsrc_t rc = __builtin_amdgcn_make_buffer_rsrc((void*)(pc_ & ~15ull), 0, (int)csz_, 0x00020000);
    if (w < 3) dma16(rc, l16, (bi * 3 + w) * 1024, lds0 + L_DUMMY + w * 1024);
  };
  const bool is_conv = bi < 20;
  const int q4 = bx * 20 + bi;
  const int panel = (bx >> 1) * 8 + (bi >> 2), j = (bx & 1) * 4 + (bi & 3);
  const int b0 = blk0(j);
  const int lb0 = (w < 3) ? 2 * w : 6, lb1 = 2 * w + 1;
  const bool has0 = (w < 3) || (j < 6), has1 = (w < 3);
  const int z2s0 = has0 ? O_WINF + (54 + b0 + lb0) * 20480 : OOB, z2s1 = O_WINF + (has1 ? 54 + b0 + lb1 : 108) * 20480;
  const int z1s0 = has0 ? O_WINF + (b0 + lb0) * 20480 : OOB, z1s1 = has1 ? O_WINF + (b0 + lb1) * 20480 : OOB;
  const int wx = w ^ (j & 3);
  const int wso = (64 * panel + 32 * (wx >> 1) + 4 * (wx & 1)) * 4096 + w * 1024;
#define WSP_DMA(lt, r, ldsoff) dma16x<(r) * 16>(rw, l16, (wso ^ ((32 * ((lt) >> 1) + 4 * ((lt) & 1)) * 4096)) + (8 * ((r) >> 2) + ((r) & 3)) * 4096, lds0 + (ldsoff) + w * 16384 + (r) * 1024)
  const int wrd = w * 16384 + i16 * 1024 + (g ^ (i16 & 3)) * 16;
#define W_ADDR(s) (wrq[(s) & 3] + ((s) >> 2) * 256)
  const int zu0 = has0 ? O_Z2N + (2 * panel) * 1024 + (b0 + lb0) * 65536 : OOB, zu1 = has1 ? O_Z2N + (2 * panel) * 1024 + (b0 + lb1) * 65536 : OOB;
  const int pu = O_P + ((4 * panel) * 27 + (w < 3 ? 3 * j + w : 24 + (j >> 1))) * 1024;
  const int pu16 = w < 3 ? pu : OOB, pu8 = (w == 3 && j < 6) ? pu + (j & 1) * 8 : OOB;
  v4u WR[32];
  int wrq[4];
#pragma unroll
  for (int q = 0; q < 4; ++q) wrq[q] = wrd + ((q ^ (i16 >> 2)) * 64);
#define WR_RD(q, dst) WR[dst] = *(const v4u*)(smem + L_WL + W_ADDR(q))
  v4u R[40];
#define R_LD(q, s0, s1) R[q] = bld1(rs, l16 + (((q) >> 1) & 3) * 1024, (((q) & 1) ? (s1) : (s0)) + ((q) >> 3) * 4096)
  bool oks = false;
  if (__builtin_expect(is_conv, 1)) {
    const int d = 4 * q4 + w;
    unsigned par[6];
    par[0] = NTL((const unsigned*)(p.a_re + d * NST + 2 * lane)); par[1] = NTL((const unsigned*)(p.a_im + d * NST + 2 * lane));
    par[2] = NTL((const unsigned*)(p.b_re + d * NST + 2 * lane)); par[3] = NTL((const unsigned*)(p.b_im + d * NST + 2 * lane));
    par[4] = NTL((const unsigned*)(p.c_re + d * NST + 2 * lane)); par[5] = NTL((const unsigned*)(p.c_im + d * NST + 2 * lane));
    const float ldt = bf2f(p.log_dt[d]);
    SB();
    v4u mv[5]; unsigned cv[5];
    const int mcg = q4 >> 2, mkq = q4 & 3;
#pragma unroll
    for (int i = 0; i < 5; ++i) {
      const int row = 160 * mkq + 32 * i + (tid >> 3);
      mv[i] = __builtin_nontemporal_load((const v4u*)(p.w_mod + (size_t)row * 3840 + 64 * mcg + (mcg >= 20 ? D : 0) + 8 * (tid & 7)));
      cv[i] = p.cvec[row];
    }
    const unsigned ep = __hip_atomic_load((GAS unsigned*)(p.ws + O_EPOCH), RLX_AGENT);
    const unsigned dku = p.dskip[d];
    v2u xa[8];
    const int ro = 64 * (tid >> 5) + 32 * ((tid >> 4) & 1) + 8 * ((tid >> 1) & 3) + 4 * ((tid >> 3) & 1) + 2 * (tid & 1);
    v4u st[4][2]; u64 mg;
    const int mo = lane < 32 ? O_MODP + ((lane & 3) * 2560 + (lane >> 4) * D + 4 * q4 + ((lane >> 2) & 3)) * 8 : (lane < 36 ? O_MODG + (2 * D + 4 * q4 + (lane & 3)) * 8 : O_INIT);
    auto gather = [&]() __attribute__((always_inline)) {
      mg = __hip_atomic_load((GAS u64*)(p.ws + mo), RLX_AGENT);
#pragma unroll
      for (int i = 0; i < 4; ++i) { st[i][0] = bld1(rs, ro * 16, O_STG + i * 8192); st[i][1] = bld1(rs, ro * 16 + 16, O_STG + i * 8192); }
    };
    const rsrc_t ru = __builtin_amdgcn_make_buffer_rsrc((void*)p.u, 0, T * D * 2, 0x00020000);
    auto xld = [&](int i) __attribute__((always_inline)) { xa[i] = __builtin_amdgcn_raw_buffer_load_b64(ru, ro * (2 * D), (512 * (i >> 1) + (i & 1)) * (2 * D) + 8 * q4, 0); };
    auto hook = [&](int i) __attribute__((always_inline)) {
      SB();
      if (i == MODHOOK) {
        tag = ep + 1u;
        float acc[8];
#pragma unroll
        for (int k = 0; k < 8; ++k) acc[k] = 0.f;
#pragma unroll
        for (int q = 0; q < 5; ++q) {
          const unsigned slo = silu_bf(bf2f(cv[q])), shi = slo << 16;
#pragma unroll
          for (int k = 0; k < 4; ++k) { acc[2 * k] = dot2(mv[q][k], slo, acc[2 * k]); acc[2 * k + 1] = dot2(mv[q][k], shi, acc[2 * k + 1]); }
        }
        const float s = col_reduce((float*)(smem + L_WL), acc, tid);
        if ((tid & 3) == 0) bst1_64(rs, O_MODP + (mkq * 2560 + 64 * mcg + (tid >> 2)) * 8, v2u{f2u(s), tag});
        SB();
        code_prefetch();
      }
      if (i == 2) xld(2);
      if (i == 4) { xld(3); xld(4); }
      if (i >= 5 && i <= 7) xld(i);
      if (i == 9) gather();
      SB();
    };
    SB();
    xld(0); xld(1);
    SB();
    u16* himg = (u16*)(smem + L_HIMG);
    if (tid < 128) *(v4u*)(himg + (tid >> 5) * HL + (tid & 31) * 8) = v4u{0, 0, 0, 0};
    const float dt = rbf(__builtin_amdgcn_exp2f(ldt * 1.4426950408889634f));
    kgen_wave(par, dt, bf2f(dku), (unsigned*)(smem + L_KSCR) + w * 16 * RS, smem + L_KIMG + w * KIMG_B, lane, hook);
#pragma unroll 1
    for (unsigned s = 0; s < SPIN; ++s) {
      unsigned bad = (lane >> 2) == 8 ? 0u : (unsigned)(mg >> 32) ^ tag;
#pragma unroll
      for (int i = 0; i < 4; ++i) bad |= (st[i][0][1] ^ tag) | (st[i][0][3] ^ tag) | (st[i][1][1] ^ tag) | (st[i][1][3] ^ tag);
      PIN(bad);
      if (__all(bad == 0u)) break;
      asm volatile("" ::: "memory");
      gather();
    }
    ORDER();
    float sa[4], c1[4];
    {
      float ms = u2f((unsigned)mg);
      ms += dppf<0xB1>(ms); ms += dppf<0x4E>(ms);
      const unsigned mr = f2u(rbf(ms));
#pragma unroll
      for (int c = 0; c < 4; ++c) {
        sa[c] = u2f(__builtin_amdgcn_readlane(mr, 4 * c));
        c1[c] = rbf(1.0f + u2f(__builtin_amdgcn_readlane(mr, 16 + 4 * c)));
      }
    }
    if (!__all((unsigned)(mg >> 32) == tag)) mg = __hip_atomic_load((GAS u64*)(p.ws + mo), RLX_AGENT);
    SB();
#pragma unroll
    for (int i = 0; i < 4; ++i) {
      const int pr = 256 * i + tid;
      const f32x2 m = {u2f(st[i][0][0]), u2f(st[i][1][0])}, r = {u2f(st[i][0][2]), u2f(st[i][1][2])};
      const f32x2 x[4] = {f32x2{bflo(xa[2 * i][0]), bflo(xa[2 * i + 1][0])}, f32x2{bfhi(xa[2 * i][0]), bfhi(xa[2 * i + 1][0])},
                          f32x2{bflo(xa[2 * i][1]), bflo(xa[2 * i + 1][1])}, f32x2{bfhi(xa[2 * i][1]), bfhi(xa[2 * i + 1][1])}};
#pragma unroll
      for (int c = 0; c < 4; ++c) {
        const f32x2 h = (x[c] - m) * r * c1[c] + sa[c];
        *(unsigned*)(himg + c * HL + 256 + 512 * i + ro) = pk_bf16(h[0], h[1]);
      }
    }
    unsigned ab[8];
#pragma unroll
    for (int a = 0; a < 8; ++a) { ab[a] = lds0 + L_KIMG + w * KIMG_B + (i16 & 1) * (KL * 2) + (2032 - (i16 & ~1) - 2 * (i16 & 1) + 8 * g) * 2 - 64 * (8 * a + 7); PIN(ab[a]); }
    unsigned bb = lds0 + L_HIMG + (w * HL + 256 - 16 + 16 * i16 + 8 * g) * 2 - 64 * 7; PIN(bb);
#pragma unroll 1
    for (unsigned s = 0; s < SPIN; ++s) {
      if (__all((unsigned)(mg >> 32) == tag)) break;
      mg = __hip_atomic_load((GAS u64*)(p.ws + mo), RLX_AGENT);
    }
    float ga[4];
#pragma unroll
    for (int c = 0; c < 4; ++c) ga[c] = u2f(__builtin_amdgcn_readlane((unsigned)mg, 32 + c));
    __syncthreads();
    f32x4 acc[8];
#pragma unroll
    for (int c = 0; c < 8; ++c) acc[c] = f32x4{0.f, 0.f, 0.f, 0.f};
    v4u O[2][16];
#define CLOAD1(buf, r, k) { if constexpr (CL[k] < 8) { const LAS unsigned* ap = (const LAS unsigned*)(size_t)ab[CL[k] & 7] + 16 * (7 - (r)); O[buf][CL[k]] = v4u{ap[0], ap[1], ap[2], ap[3]}; } \
                            else O[buf][CL[k]] = *((const LAS v4u*)(size_t)bb + 4 * (8 * (CL[k] - 8) + 7 - (r))); }
    sfor<16>(LAM { constexpr int k = ic_; CLOAD1(0, 0, k); });
    SB();
    unsigned cav = 0;
    u16* ybuf = (u16*)(smem + L_KSCR);
    f32x2 xf[4][4];
#define Y_OUT(c) *(v2u*)(ybuf + w * T + 256 * (c) + 16 * i16 + 4 * g) = v2u{pk_bf16(acc[c][0], acc[c][1]), pk_bf16(acc[c][2], acc[c][3])}
    sfor<8 * 36>(LAM {
      constexpr int r = ic_ / 36, m = ic_ % 36;
      constexpr int ca = r < 7 ? CA[m] : CA7[m], cb = r < 7 ? CB[m] : CB7[m];
      acc[ca + cb] = mfma_bf(O[r & 1][ca], O[r & 1][8 + cb], acc[ca + cb]);
      if constexpr (r < 7 && (m & 1) == 0 && m < 32) CLOAD1((r + 1) & 1, r + 1, m >> 1);
      if constexpr (cs_dma(ic_)) WSP_DMA(0, ic_ / ASP, L_WL);
      if constexpr ((r == 5 || r == 6) && (m & 3) == 3 && m < 32) {
        constexpr int q = 8 * (r - 5) + (m >> 2), i = q >> 2, c = q & 3;
        xf[i][c] = (c & 1) ? f32x2{bfhi(xa[2 * i][c >> 1]), bfhi(xa[2 * i + 1][c >> 1])} : f32x2{bflo(xa[2 * i][c >> 1]), bflo(xa[2 * i + 1][c >> 1])};
        PIN(xf[i][c]);
      }
      if constexpr (r == 7) {
        if constexpr (m == 11) Y_OUT(7);
        if constexpr (m == 18) Y_OUT(6);
        if constexpr (m == 24) Y_OUT(5);
        if constexpr (m == 29) Y_OUT(4);
        if constexpr (m == 33) Y_OUT(3);
        if constexpr (m == 34) cav = cnt_load(p, C_S, lane);
      }
      SB();
    });
    Y_OUT(2); Y_OUT(1); Y_OUT(0);
    __syncthreads();
#pragma unroll
    for (int i = 0; i < 4; ++i) {
      const int pr = 256 * i + tid;
      f32x2 o[4];
#pragma unroll
      for (int c = 0; c < 4; ++c) o[c] = xf[i][c] + unp(*(const unsigned*)(ybuf + c * T + 512 * i + ro)) * ga[c];
      bst1(rs, O_X1G + (q4 * T + 2 * pr) * 8, v4u{pk_bf16(o[0][0], o[1][0]), pk_bf16(o[2][0], o[3][0]), pk_bf16(o[0][1], o[1][1]), pk_bf16(o[2][1], o[3][1])});
    }
    SB();
    oks = __all(cav >= 7u);
    ORDER();
  } else {
    aux_role(p, rs, smem, (bi - 20) * 8 + bx, tag);
    code_prefetch();
    sfor<16>(LAM { WSP_DMA(0, ic_, L_WL); });
    DRAIN();
  }
#pragma unroll
  for (int q = 0; q < 16; ++q) WR_RD(q, q);
  int otab;
  {
    const int lf = lane & 7;
    int ab = 2 * w + lf; ab = ab >= 7 ? ab - 7 : ab;
    const int zo = (ab == 6 && j >= 6) ? OOB : O_Z2N + (b0 + ab) * 65536 + 16 * w * 1024;
    const int sl = (ab * 3 + ((w - (ab >> 1) - 1) & 3)) * 4096;
    const int ro = (sl < 65536 ? L_WL + sl : sl) + ((w ^ (ab >> 1)) << 10);
    const int n = 6 * w + lf - 1;
    const int ko = (lf == 0 ? 3 * j + min(w, 2) : (n < 3 * j ? n : n + 3)) * 1024;
    const int lt = lane & 3;
    const int po = (lane & 4) ? ((lt ^ w) * 3 + lt - 1) * 5120 : O_P + (4 * panel + (lt ^ w)) * (27 * 1024);
    const int sel = (lane >> 3) & 3;
    otab = sel == 0 ? zo : (sel == 1 ? ro : (sel == 2 ? ko : po));
    PIN(otab);
  }
  asm volatile("s_waitcnt lgkmcnt(0)" ::: "memory");
  if (is_conv) {
    DRAIN();
    __syncthreads();
    arrive(p, C_X1, tid);
  }
  if (!oks) cnt_wait<4>(p, C_S, 7u, lane);
  {
    const rsrc_t rb = __builtin_amdgcn_make_buffer_rsrc((void*)p.b_sp, 0, T * 2, 0x00020000);
    if (w < 3) dma16s(rs, l16, w == 0 ? O_U + (FH + 16 * b0) * 8 : (w == 1 ? O_U + 16 * b0 * 8 : OOB), lds0 + L_DUMMY + w * 1024);
    else dma16(rb, l16, 128 * panel, lds0 + L_DUMMY + 3072);
  }
#pragma unroll
  for (int q = 0; q < NR1; ++q) R_LD(q, z2s0, z2s1);
  SB();
  sfor<16>(LAM { WSP_DMA(1, ic_, L_WL); });
  SB();
  const int xoff = ((lane >> 5) * T + 2 * (lane & 31)) * 8;
  const int xso = O_X1G + (2 * w * T + 64 * panel) * 8;
#define X_DMA(ks) dma16s(rs, xoff, xso + (ks) * (8 * T * 8), lds0 + ((ks) * 4 + w) * 1024)
  float* sxL = (float*)(smem + L_MISC);
  float* st1 = (float*)(smem + L_MISC + 512) + w * 128;
  float* gsum = (float*)(smem + L_MISC + 2560);
  float* st2 = (float*)(smem + L_MISC + 4608) + w * 128;
  unsigned xl[4];
#pragma unroll
  for (int i = 0; i < 4; ++i) { xl[i] = lds0 + g * 1024 + (i ^ wx) * 128 + i16 * 8; PIN(xl[i]); }
  auto ldx = [&](unsigned lo, int off) __attribute__((always_inline)) {
    const v2u a = *(const LAS v2u*)(size_t)(lo + off), c = *(const LAS v2u*)(size_t)(lo + off + 512);
    return v4u{a[0], a[1], c[0], c[1]};
  };
  f32x4 a0[4], a1[4], gram = {0.f, 0.f, 0.f, 0.f};
#pragma unroll
  for (int i = 0; i < 4; ++i) { a0[i] = f32x4{0.f, 0.f, 0.f, 0.f}; a1[i] = a0[i]; }
  if (w == 3) cnt_wait<2>(p, C_X1, 20u, lane);
  __syncthreads();
#pragma unroll
  for (int ks = 0; ks < 8; ++ks) X_DMA(ks);
  asm volatile("s_waitcnt vmcnt(4)\n\ts_barrier" ::: "memory");
  {
    v4u A[3][4];
    sfor<8>(LAM { A[ic_ >> 2][ic_ & 3] = ldx(xl[ic_ & 3], (ic_ >> 2) * 4096); SB(); });
    sfor<20 * 9>(LAM {
      constexpr int ks = ic_ / 9, m = ic_ % 9;
      if constexpr (m == 0 && ks == 2) asm volatile("s_waitcnt vmcnt(6)\n\ts_barrier" ::: "memory");
      if constexpr (m == 0 && (ks == 6 || ks == 10)) asm volatile("s_waitcnt vmcnt(12)\n\ts_barrier" ::: "memory");
      if constexpr (m == 0 && ks == 8) { sfor<4>([&](auto i_) __attribute__((always_inline)) { xl[i_] += 40960; PIN(xl[i_]); }); }
      if constexpr (m == 0 && ks < 19) __builtin_amdgcn_s_waitcnt(0xC47F);
      if constexpr (m == 2) gram = mfma_bf(A[ks % 3][0], A[ks % 3][0], gram);
      else {
        constexpr int mm = m < 2 ? m : m - 1;
        if constexpr (mm & 1) a1[mm >> 1] = mfma_bf(A[ks % 3][mm >> 1], R[2 * ks + 1], a1[mm >> 1]);
        else a0[mm >> 1] = mfma_bf(A[ks % 3][mm >> 1], R[2 * ks], a0[mm >> 1]);
      }
      if constexpr ((m & 1) == 1 && ks < 18) A[(ks + 2) % 3][m >> 1] = ldx(xl[m >> 1], (ks + 2 - (ks >= 8 ? 10 : 0)) * 4096);
      if constexpr (ks >= 18 && m < 8) WR_RD(8 * (ks - 18) + m, 16 + 8 * (ks - 18) + m);
      if constexpr ((m == 2 || m == 4 || m == 8) && ks < 4) X_DMA(8 + 3 * ks + (m >> 2));
      if constexpr ((m == 4 || m == 8) && ks >= 4) { constexpr int n = NR1 + 2 * (ks - 4) + (m == 8); if constexpr (n < 40) R_LD(n, z2s0, z2s1); else R_LD(n - 40, z1s0, z1s1); }
      SB();
    });
  }
  if (w == 3) {
    if (i16 == 0) {
#pragma unroll
      for (int i = 0; i < 4; ++i) *(f32x4*)(sxL + (i ^ wx) * 16 + 4 * g) = a1[i];
    }
#pragma unroll
    for (int i = 0; i < 4; ++i) a1[i] = f32x4{0.f, 0.f, 0.f, 0.f};
  }
  if ((i16 >> 2) == g) sxL[64 + wx * 16 + i16] = (i16 & 3) == 0 ? gram[0] : (i16 & 3) == 1 ? gram[1] : (i16 & 3) == 2 ? gram[2] : gram[3];
  __syncthreads();
  {
    const float m = sxL[lane] * (1.0f / D);
    const float var = fmaxf(sxL[64 + lane] * (1.0f / D) - m * m, 0.f);
    const float r = __builtin_amdgcn_rsqf(var + 1e-5f);
    st1[lane] = r; st1[64 + lane] = r * m;
  }
  v4u A1[3][4];
  {
    const f32x2* cU2 = (const f32x2*)(smem + L_DUMMY);
    f32x2 u0 = cU2[16 * lb0 + i16], u1 = cU2[16 * lb1 + i16];
    if (!has0) u0 = f32x2{0.f, 0.f};
    if (!has1) u1 = f32x2{0.f, 0.f};
    float s1[16], s2[16];
    f32x4 rr[4], rm[4];
#pragma unroll
    for (int i = 0; i < 4; ++i) { rr[i] = *(const f32x4*)(st1 + (i ^ wx) * 16 + 4 * g); rm[i] = *(const f32x4*)(st1 + 64 + (i ^ wx) * 16 + 4 * g); }
    sfor<8>(LAM {
      constexpr int i = ic_ >> 1, h = ic_ & 1;
      { constexpr int n0 = NR1 - 8, per = (40 - n0) / 8;
#pragma unroll
        for (int q = n0 + per * ic_; q < n0 + per * (ic_ + 1); ++q) R_LD(q, z1s0, z1s1); }
      const f32x2 r2 = {rr[i][2 * h], rr[i][2 * h + 1]}, m2 = {rm[i][2 * h], rm[i][2 * h + 1]};
      const f32x2 v0 = gelu2(f32x2{a0[i][2 * h], a0[i][2 * h + 1]} * r2 + (u0[1] - m2 * u0[0]));
      const f32x2 v1 = gelu2(f32x2{a1[i][2 * h], a1[i][2 * h + 1]} * r2 + (u1[1] - m2 * u1[0]));
      a0[i][2 * h] = v0[0]; a0[i][2 * h + 1] = v0[1]; a1[i][2 * h] = v1[0]; a1[i][2 * h + 1] = v1[1];
      f32x2 q1 = v0 + v1, q2 = v0 * v0 + v1 * v1;
      PIN(q1); PIN(q2);
      s1[4 * i + 2 * h] = q1[0]; s1[4 * i + 2 * h + 1] = q1[1]; s2[4 * i + 2 * h] = q2[0]; s2[4 * i + 2 * h + 1] = q2[1];
      SB();
    });
    sfor<4>(LAM { xl[ic_] -= 40960; PIN(xl[ic_]); A1[0][ic_] = ldx(xl[ic_], 0); SB(); A1[1][ic_] = ldx(xl[ic_], 4096); SB(); });
    const float t1 = bfly16(s1, i16), t2 = bfly16(s2, i16);
    const int tp = ((i16 >> 2) ^ wx) * 16 + 4 * g + (i16 & 3);
    bst1(rs, O_ZST + (((panel * 4 + w) * 8 + j) * 64 + tp) * 16, v4u{f2u(t1), tag, f2u(t2), tag});
  }
  SB();
  f32x4 c0[4], c1[4];
#pragma unroll
  for (int i = 0; i < 4; ++i) { c0[i] = f32x4{0.f, 0.f, 0.f, 0.f}; c1[i] = c0[i]; }
#define Z1M(ks, m) { if constexpr ((m) == 0 && (ks) >= 2 && (ks) < 19 && (ks) != KA && (ks) != KA + 1) __builtin_amdgcn_s_waitcnt(0xC47F); \
    if constexpr ((m) & 1) c1[(m) >> 1] = mfma_bf(R[2 * (ks) + 1], A1[(ks) % 3][(m) >> 1], c1[(m) >> 1]); else c0[(m) >> 1] = mfma_bf(R[2 * (ks)], A1[(ks) % 3][(m) >> 1], c0[(m) >> 1]); \
    if constexpr (((m) & 1) == 1 && (ks) < 18) A1[((ks) + 2) % 3][(m) >> 1] = ldx(xl[(m) >> 1], ((ks) + 2 - ((ks) >= KA ? KA : 0)) * 4096); }
  constexpr int KA = KAV;
  v4u zs[8];
  unsigned cfv = 0;
  const int zsoff = O_ZST + ((panel * 4 + w) * 8 * 64) * 16;
  sfor<KA * 8>(LAM {
    constexpr int ks = ic_ / 8, m = ic_ % 8;
    Z1M(ks, m);
    if constexpr ((m == 2 || m == 6) && ks < 8) { constexpr int q = 2 * ks + (m == 6); WSP_DMA(2, q, L_WL); }
    if constexpr (m == 4 && ks == 7) cfv = cnt_load(p, C_F, lane);
    if constexpr ((m & 1) == 0 && ks >= 8 && ks < 10) { constexpr int jj = 4 * (ks - 8) + (m >> 1); zs[jj] = bld1(rs, l16 + (jj & 3) * 1024, zsoff + (jj >> 2) * 4096); }
    SB();
  });
  v2u x1v[5];
#pragma unroll
  for (int db = 0; db < 5; ++db) {
    const int d = 80 * j + 16 * db + 4 * g;
    x1v[db] = *(const v2u*)(smem + (d >> 2) * 512 + (16 * w + i16) * 8);
  }
  {
#pragma unroll 1
    for (unsigned s = 0; s < SPIN; ++s) {
      unsigned bad = 0;
#pragma unroll
      for (int jj = 0; jj < 8; ++jj) bad |= (zs[jj][1] ^ tag) | (zs[jj][3] ^ tag);
      PIN(bad);
      if (__all(bad == 0u)) break;
      asm volatile("" ::: "memory");
#pragma unroll
      for (int jj = 0; jj < 8; ++jj) zs[jj] = bld1(rs, l16, zsoff + jj * 1024);
    }
    float a = 0.f, b = 0.f;
#pragma unroll
    for (int jj = 0; jj < 8; ++jj) { a += u2f(zs[jj][0]); b += u2f(zs[jj][2]); }
    *(f32x2*)(gsum + (w * 64 + lane) * 2) = f32x2{a, b};
  }
  if (!__all(cfv >= 5u)) cnt_wait<1>(p, C_F, 5u, lane);
  ORDER();
  __syncthreads();
  {
    const f32x2 s = (*(const f32x2*)(gsum + lane * 2) + *(const f32x2*)(gsum + 128 + lane * 2)) + (*(const f32x2*)(gsum + 256 + lane * 2) + *(const f32x2*)(gsum + 384 + lane * 2));
    const float m = s[0] * (1.0f / FH);
    const float var = fmaxf(s[1] * (1.0f / FH) - m * m, 0.f);
    const float r = __builtin_amdgcn_rsqf(var + 1e-5f);
    st2[lane] = r; st2[64 + lane] = r * m;
  }
  {
    unsigned o0[4][2], o1[4][2];
#pragma unroll
    for (int i = 0; i < 4; ++i) {
      const f32x4 rr = *(const f32x4*)(st2 + (i ^ wx) * 16 + 4 * g), rm = *(const f32x4*)(st2 + 64 + (i ^ wx) * 16 + 4 * g);
      const f32x4 n0 = a0[i] * rr - rm, n1 = a1[i] * rr - rm;
      o0[i][0] = pk_bf16(n0[0], n0[1]); o0[i][1] = pk_bf16(n0[2], n0[3]);
      o1[i][0] = pk_bf16(n1[0], n1[1]); o1[i][1] = pk_bf16(n1[2], n1[3]);
    }
    const bool sw = wx & 1;
    const int ph = (wx >> 1) * 1024;
#pragma unroll
    for (int pp = 0; pp < 2; ++pp) {
      const int e = 2 * pp, o = 2 * pp + 1;
      bst1(rs, zu0 + ((pp * 1024) ^ ph) + l16, sw ? v4u{o0[o][0], o0[o][1], o0[e][0], o0[e][1]} : v4u{o0[e][0], o0[e][1], o0[o][0], o0[o][1]});
      bst1(rs, zu1 + ((pp * 1024) ^ ph) + l16, sw ? v4u{o1[o][0], o1[o][1], o1[e][0], o1[e][1]} : v4u{o1[e][0], o1[e][1], o1[o][0], o1[o][1]});
    }
  }
  sfor<4>(LAM { xl[ic_] += KA * 4096; PIN(xl[ic_]); });
  SB();
  sfor<(20 - KA) * 8>(LAM {
    constexpr int ks = KA + ic_ / 8, m = ic_ % 8;
    Z1M(ks, m);
    SB();
  });
  DRAIN();
  __syncthreads();
  GAS unsigned* flags = (GAS unsigned*)(p.ws + O_FLAG);
  if (tid < 4) __hip_atomic_fetch_or(flags + ((j * 4 + (panel >> 3)) * 4 + tid) * 64, 1u << (panel & 7), RLX_AGENT);
  if (w == 2) dma16s(rs, l16, O_MODG + (5 * D + 80 * j) * 8, lds0 + L_DUMMY + 2048);
  int zof[7], rof[7], kof[7];
#pragma unroll
  for (int q = 0; q < 7; ++q) { zof[q] = __builtin_amdgcn_readlane(otab, q); rof[q] = __builtin_amdgcn_readlane(otab, 8 + q); kof[q] = __builtin_amdgcn_readlane(otab, 16 + q); }
  constexpr int ZD = ZDV;
  v4u Z[16][7];
#define Z_LD(i, f) Z[i][f] = bld1(rs, l16 + ((i) & 3) * 1024, zof[f] + ((i) >> 2) * 4096)
  unsigned zmask = 0u, zfv = 0u;
  GAS unsigned* zfp = flags + ((j * 4 + w) * 4 + (panel >> 3)) * 64;
  auto zneed = [&](unsigned bits) __attribute__((always_inline)) {
    if (__builtin_expect((zmask & bits) != bits, 0)) {
#pragma unroll 1
      for (unsigned s = 0; s < SPIN; ++s) {
        zmask = __builtin_amdgcn_readfirstlane(__hip_atomic_load(zfp, RLX_AGENT));
        if ((zmask & bits) == bits) break;
        __builtin_amdgcn_s_sleep(1);
      }
    }
    ORDER();
  };
  unsigned z1p[2][4][2];
  {
    const v4u* cU1 = (const v4u*)(smem + L_DUMMY + 1024);
    const v4u ua0 = cU1[8 * lb0 + 2 * g], ua1 = cU1[8 * lb0 + 2 * g + 1], ub0 = cU1[8 * lb1 + 2 * g], ub1 = cU1[8 * lb1 + 2 * g + 1];
    float sr[4], sm[4];
#pragma unroll
    for (int i = 0; i < 4; ++i) { sr[i] = st1[16 * (i ^ wx) + i16]; sm[i] = st1[64 + 16 * (i ^ wx) + i16]; }
    sfor<8>(LAM {
      constexpr int i = ic_ >> 1, h = ic_ & 1;
      if constexpr (ic_ < 4) { WSP_DMA(3, 4 * ic_, 0); WSP_DMA(3, 4 * ic_ + 1, 0); WSP_DMA(3, 4 * ic_ + 2, 0); WSP_DMA(3, 4 * ic_ + 3, 0); }
      if constexpr (ic_ == ZPOLL - 2) { zfv = __hip_atomic_load(zfp, RLX_AGENT); SB(); }
      if constexpr (ic_ == ZPOLL) { zmask = __builtin_amdgcn_readfirstlane(zfv); zneed(1u); }
      if constexpr (ic_ >= ZPOLL) {
        constexpr int n = 8 - ZPOLL, k = ic_ - ZPOLL;
#pragma unroll
        for (int q = 14 * k / n; q < 14 * (k + 1) / n; ++q) Z_LD(q / 7, q % 7);
        SB();
      }
      const v4u qa = h ? ua1 : ua0, qb = h ? ub1 : ub0;
      const f32x2 v0 = gelu2(f32x2{c0[i][2 * h], c0[i][2 * h + 1]} * sr[i] + (f32x2{u2f(qa[1]), u2f(qa[3])} - f32x2{u2f(qa[0]), u2f(qa[2])} * sm[i]));
      const f32x2 v1 = gelu2(f32x2{c1[i][2 * h], c1[i][2 * h + 1]} * sr[i] + (f32x2{u2f(qb[1]), u2f(qb[3])} - f32x2{u2f(qb[0]), u2f(qb[2])} * sm[i]));
      z1p[0][i][h] = pk_bf16(v0[0], v0[1]); z1p[1][i][h] = pk_bf16(v1[0], v1[1]);
      PIN(z1p[0][i][h]); PIN(z1p[1][i][h]);
    });
  }
  SB();
  f32x4 acc[7][4];
  const int woso = O_WOUTF + (5 * j) * 27 * 1024;
  v4u WO[35];
  v4u WL[4][2];
#define WO_LD(q) WO[q] = bld1(rs, l16, woso + ((q) % 5) * (27 * 1024) + kof[(q) / 5])
#define WL_RD(i, h) WL[(i) & 3][h] = *(const v4u*)(smem + ((h) ? 0 : L_WL) + W_ADDR(i))
  WL_RD(0, 0); WL_RD(0, 1);
  sfor<13 * 28>(LAM {
    constexpr int i = ic_ / 28, f = (ic_ % 28) / 4, t = ic_ % 4;
    constexpr int lo = zs_lo(i, ZD), hi = zs_lo(i + 1, ZD);
    if constexpr (f == 0 && t == 0 && hi > lo && ((lo & 1) == 0 || hi - lo > 1)) zneed((1u << ((hi - 1) >> 1)) | (1u << (lo >> 1)));
    if constexpr (t < 2) mfma_pin<i == 0, true>(acc[f][t], Z[i][f], WR[t * 16 + i]);
    else mfma_pin<i == 0, false>(acc[f][t], Z[i][f], WL[i & 3][t - 2]);
    if constexpr (hi - lo == 2 && (t == 0 || t == 2)) { constexpr int q = 2 * f + (t >> 1); Z_LD(lo + q / 7, q % 7); }
    if constexpr (hi - lo == 1 && t == 0) Z_LD(lo, f);
    if constexpr (t == 1 && f < 2 && i < 12) WL_RD(i + 1, f);
    if constexpr (t == 1 && f < 6 && i == 12) WL_RD(13 + (f >> 1), f & 1);
    SB();
  });
  asm volatile("s_nop 7\n\ts_waitcnt lgkmcnt(0)\n\ts_barrier" ::: "memory");
  sfor<7 * 12>(LAM {
    constexpr int fo = ic_ / 12, f = (fo + 2) % 7, i = 13 + (ic_ % 12) / 4, t = ic_ % 4, m = ic_ % 12;
    if constexpr (t < 2) acc[f][t] = mfma_bf(Z[i][f], WR[t * 16 + i], acc[f][t]);
    else acc[f][t] = mfma_bf(Z[i][f], WL[i & 3][t - 2], acc[f][t]);
    if constexpr (m % 4 == 0 && 3 * fo + m / 4 < NWO) WO_LD(3 * fo + m / 4);
    if constexpr (fo >= 1 && fo <= 5 && m % 3 == 2) { constexpr int pf = fo + 1, pt = m / 3; *(f32x4*)(smem + ((rof[pf] + l16) ^ (pt << 10))) = acc[pf][pt]; }
    SB();
  });
  if (w == 3) {
#pragma unroll
    for (int t = 0; t < 4; ++t) *(f32x4*)(smem + ((rof[1] + l16) ^ (t << 10))) = acc[1][t];
  }
  f32x4 o0[4], o1[4];
#pragma unroll
  for (int t = 0; t < 4; ++t) {
    const float bias = bf2f(((const u16*)(smem + L_DUMMY + 3072))[8 * (i16 >> 2) + (i16 & 3) + 32 * ((t ^ wx) >> 1) + 4 * ((t ^ wx) & 1)]);
    o0[t] = acc[0][t] + bias; o1[t] = acc[1][t] + bias; PIN(o0[t]); PIN(o1[t]);
  }
  __syncthreads();
  v4u PO[4];
  {
    f32x4 pa[4][3], pb[4][3];
#pragma unroll
    for (int s = 0; s < 3; ++s) {
      const int s0 = (2 * w * 3 + s) * 4096, s1 = ((w < 3 ? 2 * w + 1 : 6) * 3 + s) * 4096;
      const unsigned char* r0 = smem + (s0 < 65536 ? L_WL + s0 : s0) + l16;
      const unsigned char* r1 = smem + (s1 < 65536 ? L_WL + s1 : s1) + l16;
#pragma unroll
      for (int t = 0; t < 4; ++t) { pa[t][s] = *(const f32x4*)(r0 + t * 1024); pb[t][s] = *(const f32x4*)(r1 + t * 1024); }
    }
    SB();
#pragma unroll
    for (int t = 0; t < 4; ++t) {
      const f32x4 x0 = (o0[t] + pa[t][0]) + (pa[t][1] + pa[t][2]), x1 = (o1[t] + pb[t][0]) + (pb[t][1] + pb[t][2]);
      const f32x2 qa = unp(z1p[0][t][0]) * f32x2{x0[0], x0[1]}, qb = unp(z1p[0][t][1]) * f32x2{x0[2], x0[3]};
      const f32x2 qc = unp(z1p[1][t][0]) * f32x2{x1[0], x1[1]}, qd = unp(z1p[1][t][1]) * f32x2{x1[2], x1[3]};
      PO[t] = v4u{min(pk_bf16(qa[0], qa[1]), SENT - 1u), min(pk_bf16(qb[0], qb[1]), SENT - 1u), min(pk_bf16(qc[0], qc[1]), SENT - 1u), min(pk_bf16(qd[0], qd[1]), SENT - 1u)};
      bst1(rs, pu16 + (t ^ wx) * (27 * 1024) + l16, PO[t]);
      bst1_64(rs, pu8 + (t ^ wx) * (27 * 1024) + l16, v2u{PO[t][0], PO[t][1]});
    }
  }
  SB();
  if (w == 3) {
#pragma unroll
    for (int t = 0; t < 4; ++t) PO[t] = v4u{0u, 0u, 0u, 0u};
  }
  if (j & 1) { const v4u a = PO[0], c = PO[2]; PO[0] = PO[1]; PO[1] = a; PO[2] = PO[3]; PO[3] = c; }
  if (j & 2) { const v4u a = PO[0], c = PO[1]; PO[0] = PO[2]; PO[2] = a; PO[1] = PO[3]; PO[3] = c; }
  f32x4 e0[5][4], e[5][4];
#pragma unroll
  for (int t = 0; t < 4; ++t)
#pragma unroll
    for (int d = 0; d < 5; ++d) e0[d][t] = mfma_bf(WO[d], PO[t], f32x4{0.f, 0.f, 0.f, 0.f});
  const int trow = 64 * panel + 8 * (i16 >> 2) + (i16 & 3) + 32 * (w >> 1) + 4 * (w & 1);
  int pof[4], eof[4];
#pragma unroll
  for (int q = 0; q < 4; ++q) { pof[q] = __builtin_amdgcn_readlane(otab, 24 + q); eof[q] = __builtin_amdgcn_readlane(otab, 28 + q); }
  const int ppo = lane < 48 ? O_P + (4 * panel + ((lane >> 1) & 3)) * (27 * 1024) + __builtin_amdgcn_ds_bpermute((17 + (lane >> 3)) * 4, otab) + 1012 + (lane & 1) * 8 : OOB;
  v4u gb0[5], gb1[5];
#pragma unroll
  for (int d = 0; d < 5; ++d) { gb0[d] = *(const v4u*)(smem + L_DUMMY + 2048 + (16 * d + 4 * g) * 8); gb1[d] = *(const v4u*)(smem + L_DUMMY + 2048 + (16 * d + 4 * g) * 8 + 16); }
  SB();
#pragma unroll
  for (int q = NWO; q < 35; ++q) WO_LD(q);
  SB();
  v4u P[24];
#define P_LD(q) P[q] = bld1(rs, l16, pof[((q) / 6 + 1) & 3] + kof[1 + (q) % 6])
  constexpr int PDN = PDNV;
#pragma unroll 1
  for (unsigned tries = 0; tries < 4u; ++tries) {
#pragma unroll 1
    for (unsigned s = 0; s < SPIN; ++s) {
      asm volatile("" ::: "memory");
      const unsigned m = __builtin_amdgcn_raw_buffer_load_b32(rs, ppo, 0, AUX_SC1);
#pragma unroll
      for (int q = 0; q < PDN; ++q) P_LD(q);
      if (!__any(m == SENT)) break;
    }
    asm volatile("" ::: "memory");
    unsigned pm = 0u;
    sfor<24 * 5>(LAM {
      constexpr int q = ic_ / 5, d = ic_ % 5, lo = q / 6, lt = (lo + 1) & 3, kk = 1 + q % 6;
      e[d][lt] = mfma_bf(WO[5 * kk + d], P[q], kk == 1 ? e0[d][lt] : e[d][lt]);
      if constexpr (d == 0 && q + PDN < 24) P_LD(q + PDN);
      if constexpr (d == 1) { pm = mx4(pm, P[q]); PIN(pm); }
      if constexpr (lo >= 1 && d == 3 && q % 6 < 5) *(f32x4*)(smem + eof[lo] + (q % 6) * 1024 + l16) = e[q % 6][lo];
      SB();
    });
    if (__builtin_expect(!__any(pm == SENT), 1)) break;
  }
  __syncthreads();
  u16* orow = p.out + (size_t)trow * D + 80 * j + 4 * g;
  f32x4 ep[5][3];
#pragma unroll
  for (int d = 0; d < 5; ++d)
#pragma unroll
    for (int s = 0; s < 3; ++s) ep[d][s] = *(const f32x4*)(smem + ((w * 3 + s) * 5 + d) * 1024 + l16);
  SB();
#pragma unroll
  for (int d = 0; d < 5; ++d) {
    const f32x4 eo = (e[d][0] + ep[d][0]) + (ep[d][1] + ep[d][2]);
    const float r0 = bflo(x1v[d][0]) + u2f(gb0[d][0]) * eo[0], r1 = bfhi(x1v[d][0]) + u2f(gb0[d][2]) * eo[1];
    const float r2 = bflo(x1v[d][1]) + u2f(gb1[d][0]) * eo[2], r3 = bfhi(x1v[d][1]) + u2f(gb1[d][2]) * eo[3];
    *(v2u*)(orow + 16 * d) = v2u{pk_bf16(r0, r1), pk_bf16(r2, r3)};
  }
  if (blockIdx.x == 0 && tid == 0) __hip_atomic_store((GAS unsigned*)(p.ws + O_EPOCH), tag, RLX_AGENT);
  asm volatile(".Lkf_end:");
}

extern "C" void kernel_launch(void* const* d_in, const int* in_sizes, int n_in, void* d_out, int out_size, void* d_ws,
                              size_t ws_size, hipStream_t stream) {
  Params p;
  p.u = (const u16*)d_in[0]; p.a_re = (const u16*)d_in[1]; p.a_im = (const u16*)d_in[2];
  p.b_re = (const u16*)d_in[3]; p.b_im = (const u16*)d_in[4]; p.c_re = (const u16*)d_in[5]; p.c_im = (const u16*)d_in[6];
  p.log_dt = (const u16*)d_in[7]; p.dskip = (const u16*)d_in[8];
  p.w_in = (const u16*)d_in[10]; p.w_sp = (const u16*)d_in[11]; p.b_sp = (const u16*)d_in[12]; p.w_out = (const u16*)d_in[13];
  p.cvec = (const u16*)d_in[14]; p.w_mod = (const u16*)d_in[15];
  p.out = (u16*)d_out; p.ws = (unsigned char*)d_ws;
  (void)hipFuncSetAttribute((const void*)k_fused, hipFuncAttributeMaxDynamicSharedMemorySize, L_TOTAL);
  k_fused<<<256, 256, L_TOTAL, stream>>>(p);
}
```

```cpp
#include <hip/hip_runtime.h>
#include <hip/hip_bf16.h>
#include <stdint.h>
#include <utility>

typedef unsigned v4u __attribute__((ext_vector_type(4)));
typedef unsigned v2u __attribute__((ext_vector_type(2)));
typedef float f32x4 __attribute__((ext_vector_type(4)));
typedef float f32x2 __attribute__((ext_vector_type(2)));
typedef short bf16x8 __attribute__((ext_vector_type(8)));
typedef _Float16 f16x8 __attribute__((ext_vector_type(8)));
typedef _Float16 f16x2 __attribute__((ext_vector_type(2)));
typedef __bf16 bf16x2_t __attribute__((ext_vector_type(2)));
typedef unsigned short u16;
typedef unsigned long long u64;
#define GAS __attribute__((address_space(1)))
#define LAS __attribute__((address_space(3)))
#define RLX_AGENT __ATOMIC_RELAXED, __HIP_MEMORY_SCOPE_AGENT
#define DEVI __device__ __forceinline__
#define SB() __builtin_amdgcn_sched_barrier(0)
template <int... I, class F> __device__ __forceinline__ void sfor_impl(std::integer_sequence<int, I...>, F&& f) { (f(std::integral_constant<int, I>{}), ...); }
template <int N, class F> __device__ __forceinline__ void sfor(F&& f) { sfor_impl(std::make_integer_sequence<int, N>{}, f); }
#define LAM [&](auto ic_) __attribute__((always_inline))
#define NTL(p) __builtin_nontemporal_load(p)
#define DRAIN() asm volatile("s_waitcnt vmcnt(0)" ::: "memory")

constexpr int T = 2048, D = 640, NST = 128, FH = 864, F2 = 1728;
constexpr int KL = 2080, KIMG_B = 2 * KL * 2, HL = 2304;
constexpr unsigned SPIN = 1u << 15;
constexpr int AUX_SC1 = 16, AUX_NT = 2;
constexpr int O_EPOCH = 0;
constexpr int O_INIT = 256;
constexpr int O_CNT = 4096;
constexpr int O_FLAG = O_CNT + 3 * 64 * 256;
constexpr int O_MODG = O_FLAG + 256 * 256;
constexpr int O_MODP = O_MODG + 3840 * 8 + 2048;
constexpr int O_STG = O_MODP + 4 * 2560 * 8;
constexpr int O_U = O_STG + 2048 * 16;
constexpr int O_ZST = O_U + 16384;
constexpr int O_X1G = O_ZST + 32 * 32 * 64 * 16;
constexpr int O_WINF = O_X1G + T * D * 2;
constexpr int O_WOUTF = O_WINF + 109 * 20 * 1024;
constexpr int O_Z2N = O_WOUTF + 40 * 27 * 1024;
constexpr int O_P = O_Z2N + 54 * 64 * 1024;
constexpr int O_END = O_P + 128 * 27 * 1024;
constexpr int C_X1 = 0, C_S = 1, C_F = 2;
constexpr unsigned SENT = 0xffffffffu;

DEVI float u2f(unsigned u) { return __builtin_bit_cast(float, u); }
DEVI unsigned f2u(float f) { return __builtin_bit_cast(unsigned, f); }
DEVI float bf2f(unsigned b) { return u2f(b << 16); }
DEVI float bflo(unsigned u) { return u2f(u << 16); }
DEVI float bfhi(unsigned u) { return u2f(u & 0xffff0000u); }
DEVI unsigned pk_bf16(float a, float b) {
  f32x2 v = {a, b};
  bf16x2_t r = __builtin_convertvector(v, bf16x2_t);
  return __builtin_bit_cast(unsigned, r);
}
DEVI float rbf(float a) { return bflo(pk_bf16(a, 0.f)); }
DEVI unsigned pk_f16(float a, float b) {
  f32x2 v = {a, b};
  f16x2 r = __builtin_convertvector(v, f16x2);
  return __builtin_bit_cast(unsigned, r);
}
DEVI f32x4 mfma_bf(v4u a, v4u b, f32x4 c) {
  return __builtin_amdgcn_mfma_f32_16x16x32_bf16(__builtin_bit_cast(bf16x8, a), __builtin_bit_cast(bf16x8, b), c, 0, 0, 0);
}
DEVI f32x4 mfma_h(v4u a, v4u b, f32x4 c) {
  return __builtin_amdgcn_mfma_f32_16x16x32_f16(__builtin_bit_cast(f16x8, a), __builtin_bit_cast(f16x8, b), c, 0, 0, 0);
}
template <bool FIRST, bool BA> DEVI void mfma_pin(f32x4& acc, const v4u& a, const v4u& bb) {
  if constexpr (FIRST) {
    if constexpr (BA) asm volatile("v_mfma_f32_16x16x32_bf16 %0, %1, %2, 0" : "=a"(acc) : "v"(a), "a"(bb));
    else asm volatile("v_mfma_f32_16x16x32_bf16 %0, %1, %2, 0" : "=a"(acc) : "v"(a), "v"(bb));
  } else {
    if constexpr (BA) asm volatile("v_mfma_f32_16x16x32_bf16 %0, %1, %2, %0" : "+a"(acc) : "v"(a), "a"(bb));
    else asm volatile("v_mfma_f32_16x16x32_bf16 %0, %1, %2, %0" : "+a"(acc) : "v"(a), "v"(bb));
  }
}
typedef __amdgpu_buffer_rsrc_t rsrc_t;
DEVI v4u bld1(rsrc_t rs, int voff, int soff = 0) { return __builtin_amdgcn_raw_buffer_load_b128(rs, voff, soff, AUX_SC1); }
DEVI void bst1(rsrc_t rs, int voff, v4u v) { __builtin_amdgcn_raw_buffer_store_b128(v, rs, voff, 0, AUX_SC1); }
DEVI void bst1_64(rsrc_t rs, int voff, v2u v) { __builtin_amdgcn_raw_buffer_store_b64(v, rs, voff, 0, AUX_SC1); }
template <int CTRL> DEVI float dppf(float x) { return u2f((unsigned)__builtin_amdgcn_update_dpp(0, (int)f2u(x), CTRL, 0xf, 0xf, true)); }
DEVI float dot2(unsigned a, unsigned b, float c) { return __builtin_amdgcn_fdot2_f32_bf16(__builtin_bit_cast(bf16x2_t, a), __builtin_bit_cast(bf16x2_t, b), c, false); }
DEVI unsigned mx4(unsigned m, v4u v) { return max(max(max(m, v[0]), v[1]), max(v[2], v[3])); }
#define PIN(x) asm volatile("" : "+v"(x))
#define ORDER() __builtin_amdgcn_fence(__ATOMIC_ACQUIRE, "wavefront")

struct Params {
  const u16 *u, *a_re, *a_im, *b_re, *b_im, *c_re, *c_im, *log_dt, *dskip, *w_in, *w_sp, *b_sp, *w_out, *cvec, *w_mod;
  u16* out;
  unsigned char* ws;
};

DEVI int blk0(int j) { return j < 6 ? 7 * j : 42 + 6 * (j - 6); }
DEVI int pair_blk(int ks3, int h) {
  if (ks3 < 24) { const int j = ks3 / 3, w = ks3 - 3 * j; return blk0(j) + 2 * w + h; }
  return 7 * (2 * (ks3 - 24) + h) + 6;
}
DEVI void arrive(const Params& p, int c, int tid) {
  if (tid < 8) __hip_atomic_fetch_add((GAS unsigned*)(p.ws + O_CNT) + ((c * 8 + tid) * 8 + (blockIdx.x & 7)) * 64, 1u, RLX_AGENT);
}
DEVI unsigned cnt_load(const Params& p, int c, int lane) {
  return __hip_atomic_load((GAS unsigned*)(p.ws + O_CNT) + ((c * 8 + ((blockIdx.x >> 3) & 7)) * 8 + (lane & 7)) * 64, RLX_AGENT);
}
template <int SLP> DEVI void cnt_wait(const Params& p, int c, unsigned want, int lane) {
#pragma unroll 1
  for (unsigned s = 0; s < SPIN; ++s) {
    if (__all(cnt_load(p, c, lane) >= want)) break;
    __builtin_amdgcn_s_sleep(SLP);
  }
  ORDER();
}
struct C2 { f32x2 re, im; };
DEVI C2 cmul2(C2 a, C2 b) { return C2{a.re * b.re - a.im * b.im, a.re * b.im + a.im * b.re}; }
DEVI C2 cexp2(f32x2 are2, f32x2 arev, float l) {
  const f32x2 e = are2 * l, q = arev * l;
  const f32x2 m = {__builtin_amdgcn_exp2f(e[0]), __builtin_amdgcn_exp2f(e[1])};
  const float p0 = __builtin_amdgcn_fractf(q[0]), p1 = __builtin_amdgcn_fractf(q[1]);
  return C2{m * f32x2{__builtin_amdgcn_cosf(p0), __builtin_amdgcn_cosf(p1)}, m * f32x2{__builtin_amdgcn_sinf(p0), __builtin_amdgcn_sinf(p1)}};
}
constexpr int RS = 132;
DEVI void put_row(unsigned* buf, int row, int lane, C2 v) { *(v2u*)(buf + row * RS + 2 * lane) = v2u{pk_f16(v.re[0], v.im[0]), pk_f16(v.re[1], v.im[1])}; }
DEVI f32x2 unp(unsigned u) { return f32x2{bflo(u), bfhi(u)}; }

template <class HK> DEVI void kgen_wave(unsigned par[6], float dt, float dsk, unsigned* buf0, unsigned char* img, int lane, HK&& hook) {
  unsigned* buf1 = (unsigned*)img;
  u16* tab = (u16*)buf0;
  const int c = lane & 15, g = lane >> 4;
  const f32x2 are2 = unp(par[0]) * (dt * 1.4426950408889634f), arev = unp(par[1]) * (dt * 0.15915494309189535f), nrev = -arev;
  C2 W = cmul2(C2{unp(par[4]), unp(par[5])}, C2{unp(par[2]) * dt, unp(par[3]) * dt});
  float mx = fmaxf(fmaxf(fabsf(W.re[0]), fabsf(W.re[1])), fmaxf(fabsf(W.im[0]), fabsf(W.im[1])));
  unsigned mu = f2u(mx);
  mu = max(mu, f2u(dppf<0x128>(u2f(mu)))); mu = max(mu, f2u(dppf<0x124>(u2f(mu)))); mu = max(mu, f2u(dppf<0x122>(u2f(mu)))); mu = max(mu, f2u(dppf<0x121>(u2f(mu))));
  const unsigned m0 = __builtin_amdgcn_readlane(mu, 0), m1 = __builtin_amdgcn_readlane(mu, 16), m2 = __builtin_amdgcn_readlane(mu, 32), m3 = __builtin_amdgcn_readlane(mu, 48);
  unsigned eb = (max(max(m0, m1), max(m2, m3)) >> 23) & 0xffu;
  eb = eb < 2u ? 2u : (eb > 250u ? 250u : eb);
  const float sc = u2f((eb + 1u) << 23), isc = u2f((253u - eb) << 23);
  W.re *= isc; W.im *= isc;
  {
    const C2 e8 = cexp2(are2, arev, 8.f);
    C2 W8 = cmul2(W, cexp2(are2, arev, 64.f));
#pragma unroll
    for (int b = 0; b < 8; ++b) {
      put_row(buf0, b, lane, W); put_row(buf0, 8 + b, lane, W8);
      if (b < 7) { W = cmul2(W, e8); W8 = cmul2(W8, e8); }
      if (b == 0) hook(2);
      if (b == 4) hook(3);
    }
  }
  v4u A[8];
#pragma unroll
  for (int ks = 0; ks < 8; ++ks) A[ks] = *(const v4u*)(buf0 + c * RS + 16 * ks + 4 * g);
  C2 er[8];
  er[0] = C2{f32x2{1.f, 1.f}, f32x2{0.f, 0.f}};
  er[1] = cexp2(are2, nrev, 1.f);
  er[2] = cmul2(er[1], er[1]); er[3] = cmul2(er[2], er[1]); er[4] = cmul2(er[2], er[2]);
  er[5] = cmul2(er[4], er[1]); er[6] = cmul2(er[3], er[3]); er[7] = cmul2(er[4], er[3]);
  hook(4);
  const C2 e128 = cexp2(are2, nrev, 128.f);
#pragma unroll
  for (int r = 0; r < 8; ++r) { put_row(buf0, r, lane, er[r]); put_row(buf0, 8 + r, lane, r ? cmul2(e128, er[r]) : e128); }
  f32x4 acc0 = {0.f, 0.f, 0.f, 0.f}, acc1 = acc0, acc2 = acc0;
#pragma unroll
  for (int ks = 0; ks < 8; ++ks) acc0 = mfma_h(A[ks], *(const v4u*)(buf0 + c * RS + 16 * ks + 4 * g), acc0);
  hook(5);
  C2 e = cmul2(e128, e128);
  C2 f = cexp2(are2, nrev, 1024.f);
#pragma unroll
  for (int a = 0; a < 2; ++a) {
#pragma unroll
    for (int r = 0; r < 4; ++r) put_row(buf0, 4 * a + r, lane, r ? cmul2(e, er[2 * r]) : e);
    e = cmul2(e, e128);
  }
  hook(6);
#pragma unroll
  for (int a = 0; a < 4; ++a) {
    put_row(buf0, 8 + 2 * a, lane, e); put_row(buf0, 9 + 2 * a, lane, cmul2(e, er[4])); if (a < 3) e = cmul2(e, e128);
    put_row(buf1, 2 * a, lane, f); f = cmul2(f, e128); put_row(buf1, 2 * a + 1, lane, f); f = cmul2(f, e128);
    if (a == 1) hook(7);
  }
  put_row(buf1, 8, lane, f);
  hook(8);
  {
    v4u B1[8], B2[8];
#pragma unroll
    for (int ks = 0; ks < 8; ++ks) { B1[ks] = *(const v4u*)(buf0 + c * RS + 16 * ks + 4 * g); B2[ks] = *(const v4u*)(buf1 + c * RS + 16 * ks + 4 * g); }
    SB();
#pragma unroll
    for (int ks = 0; ks < 8; ++ks) { acc1 = mfma_h(A[ks], B1[ks], acc1); acc2 = mfma_h(A[ks], B2[ks], acc2); }
  }
  hook(9);
  const int cv0 = 128 * (c >> 3) + (c & 7);
  const int cv1 = c < 8 ? 256 + 128 * (c >> 2) + 2 * (c & 3) : 512 + 128 * ((c - 8) >> 1) + 4 * (c & 1);
  const int cv2 = c < 9 ? 1024 + 128 * c : 2048;
#pragma unroll
  for (int r = 0; r < 4; ++r) {
    const int lb = 8 * (4 * g + r);
    const unsigned q01 = pk_bf16(acc0[r] * sc + ((r == 0 && lane == 0) ? dsk : 0.f), acc1[r] * sc);
    tab[lb + cv0] = (u16)q01;
    tab[lb + cv1] = (u16)(q01 >> 16);
    tab[lb + cv2 + (c > 8 ? 1 : 0)] = (u16)pk_bf16(acc2[r] * sc, 0.f);
  }
  {
    const u16* t0 = tab + 2048 - 8 * lane;
    const int o = lane < 32 ? 1 : 0;
    const unsigned H0 = t0[0], L0 = t0[-8], H1 = t0[-512], L1 = t0[-520], H2 = t0[-1024], M2 = t0[-1028], L2 = t0[-1032];
    const u16* t3 = t0 - 1536;
    const unsigned v0 = t3[0], v1 = t3[-1 + o], v2 = t3[-2], v3 = t3[-3 - o], v4 = t3[-4], v5 = t3[-5 + o], v6 = t3[-6], v7 = t3[-7 - o], v8 = t3[-8];
    const unsigned k0 = tab[0];
    SB();
    unsigned char* i0 = img + lane * 16;
    unsigned char* i1 = i0 + KL * 2;
    {
      const unsigned X = (lane & 1) ? L0 : H0, HH = H0 | (H0 << 16), LL = L0 | (L0 << 16);
      *(v4u*)i0 = v4u{HH, HH, X | (L0 << 16), LL};
      *(v4u*)i1 = v4u{HH, H0 | (X << 16), LL, LL};
    }
    {
      const unsigned X = (lane & 1) ? L1 : H1, HH = H1 | (H1 << 16), LL = L1 | (L1 << 16);
      *(v4u*)(i0 + 1024) = v4u{HH, HH, X | (L1 << 16), LL};
      *(v4u*)(i1 + 1024) = v4u{HH, H1 | (X << 16), LL, LL};
    }
    {
      const unsigned HH = H2 | (H2 << 16), MM = M2 | (M2 << 16), LL = L2 | (L2 << 16);
      *(v4u*)(i0 + 2048) = v4u{HH, H2 | (M2 << 16), MM, LL};
      *(v4u*)(i1 + 2048) = v4u{HH, MM, M2 | (L2 << 16), LL};
    }
    *(v4u*)(i0 + 3072) = v4u{v0 | (v1 << 16), v2 | (v3 << 16), v4 | (v5 << 16), v6 | (v7 << 16)};
    *(v4u*)(i1 + 3072) = v4u{v1 | (v2 << 16), v3 | (v4 << 16), v5 | (v6 << 16), v7 | (v8 << 16)};
    if (lane < 8) *(v4u*)(img + (lane >> 2) * (KL * 2) + (256 + (lane & 3)) * 16) = v4u{lane == 0 ? k0 : 0u, 0u, 0u, 0u};
  }
}

constexpr int L_KIMG = 0, L_HIMG = 4 * KIMG_B, L_KSCR = L_HIMG + 4 * HL * 2;
constexpr int L_MISC = 86016, L_WL = L_MISC + 8192, L_DUMMY = L_WL + 65536, L_TOTAL = L_DUMMY + 4096;
static_assert(L_TOTAL <= 163840 && L_KSCR + 4 * 16 * RS * 4 <= L_MISC, "lds");
constexpr int MODHOOK = 4;
constexpr int ASP = 8;
constexpr int KAV = 11;
constexpr int NR1 = 24;
constexpr int FK = 12;
constexpr int ZDV = 3;
constexpr int NWO = 21;
constexpr int PDNV = 8;
constexpr int ZPOLL = 6;
constexpr int OOB = 64 << 20;
constexpr int CA[36] = {3,3,3,3,3,4,4,4,4, 2,2,2,2,2,2,5,5,5, 1,1,1,1,6,6,1,1,1, 0,0,0,0,7,0,0,0,0};
constexpr int CB[36] = {4,3,2,1,0,3,2,1,0, 5,4,3,2,1,0,2,1,0, 6,5,4,3,1,0,2,1,0, 7,6,5,4,0,3,2,1,0};
constexpr int CA7[36] = {3,4,2,5,1,6,0,7, 3,4,2,5,1,6,0, 3,4,2,5,1,0, 3,4,2,1,0, 3,2,1,0, 2,1,0, 1,0, 0};
constexpr int CB7[36] = {4,3,5,2,6,1,7,0, 3,2,4,1,5,0,6, 2,1,3,0,4,5, 1,0,2,3,4, 0,1,2,3, 0,1,2, 0,1, 0};
constexpr int CL[16] = {8+4,3,8+3,8+2,8+1,8+0,4,8+5,2,5,8+6,1,6,8+7,0,7};

constexpr bool cs_dma(int ic) { return (ic % ASP) == 5 && ic / ASP < 16; }
constexpr int zs_lo(int i, int zd) {
  int v = i + zd; if (v > 2 + 2 * i) v = 2 + 2 * i; if (i >= 13 || v > 16) v = 16; return v;
}
DEVI void dma16s(rsrc_t r, int voff, int soff, unsigned ldsaddr) {
  asm volatile("s_mov_b32 m0, %2\n\ts_nop 0\n\tbuffer_load_dwordx4 %0, %1, %3 offen sc1 lds" :: "v"(voff), "s"(r), "s"(ldsaddr), "s"(soff) : "memory");
}
template <int X> DEVI void dma16x(rsrc_t r, int voff, int soff, unsigned ldsaddr) {
  int tmp;
  asm volatile("s_mov_b32 m0, %3\n\tv_xor_b32 %0, %5, %1\n\tbuffer_load_dwordx4 %0, %2, %4 offen lds" : "=&v"(tmp) : "v"(voff), "s"(r), "s"(ldsaddr), "s"(soff), "n"(X) : "memory");
}
DEVI void dma16(rsrc_t r, int voff, int soff, unsigned ldsaddr) {
  asm volatile("s_mov_b32 m0, %2\n\ts_nop 0\n\tbuffer_load_dwordx4 %0, %1, %3 offen lds" :: "v"(voff), "s"(r), "s"(ldsaddr), "s"(soff) : "memory");
}

DEVI unsigned plo(unsigned a, unsigned b) { return __builtin_amdgcn_perm(b, a, 0x05040100u); }
DEVI unsigned phi(unsigned a, unsigned b) { return __builtin_amdgcn_perm(b, a, 0x07060302u); }
DEVI unsigned silu_bf(float c) { return pk_bf16(c * __builtin_amdgcn_rcpf(1.0f + __builtin_amdgcn_exp2f(-1.4426950408889634f * c)), 0.f); }
DEVI float col_reduce(float* fl, const float (&acc)[8], int tid) {
  const int rg = tid >> 3, cl = tid & 7;
  *(f32x4*)(fl + rg * 68 + cl * 8) = f32x4{acc[0], acc[1], acc[2], acc[3]};
  *(f32x4*)(fl + rg * 68 + cl * 8 + 4) = f32x4{acc[4], acc[5], acc[6], acc[7]};
  __syncthreads();
  float s = 0.f;
#pragma unroll
  for (int r = 0; r < 8; ++r) s += fl[(8 * (tid & 3) + r) * 68 + (tid >> 2)];
  s += dppf<0xB1>(s); s += dppf<0x4E>(s);
  return s;
}
DEVI void wout_unit(const Params& p, rsrc_t rs, unsigned char* lw, int unit, int lane) {
  const int ks = unit / 5, dc = unit - 5 * ks, d = 128 * dc + 2 * lane;
  const int f0 = 16 * pair_blk(ks, 0), f1 = 16 * pair_blk(ks, 1);
  unsigned wv[4][8];
#pragma unroll
  for (int g = 0; g < 4; ++g)
#pragma unroll
    for (int j = 0; j < 8; ++j) wv[g][j] = NTL((const unsigned*)(p.w_out + (size_t)((j < 4 ? f0 : f1) + 2 * ((4 * g + (j & 3)) & 7) + (g >> 1)) * D + d));
#pragma unroll
  for (int g = 0; g < 4; ++g) {
    unsigned char* dst = lw + (lane >> 3) * 1024 + (g * 16 + 2 * (lane & 7)) * 16;
    *(v4u*)dst = v4u{plo(wv[g][0], wv[g][1]), plo(wv[g][2], wv[g][3]), plo(wv[g][4], wv[g][5]), plo(wv[g][6], wv[g][7])};
    *(v4u*)(dst + 16) = v4u{phi(wv[g][0], wv[g][1]), phi(wv[g][2], wv[g][3]), phi(wv[g][4], wv[g][5]), phi(wv[g][6], wv[g][7])};
  }
#pragma unroll
  for (int i = 0; i < 8; ++i) bst1(rs, O_WOUTF + ((8 * dc + i) * 27 + ks) * 1024 + lane * 16, *(const v4u*)(lw + i * 1024 + lane * 16));
}

DEVI void aux_role(const Params& p, rsrc_t rs, unsigned char* smem, int a, unsigned& tag) {
  const int tid = threadIdx.x, lane = tid & 63, w = __builtin_amdgcn_readfirstlane(tid >> 6);
  float* fl = (float*)smem;
  constexpr unsigned ONES = 0x3f803f80u;
  unsigned wv[5][8];
  const int blk = (a & 7) * 7 + (a >> 3), fp = tid & 15, rgp = tid >> 4;
  const u16* wsrc = p.w_in + (size_t)(8 * rgp) * F2 + 32 * min(blk, 53) + 2 * fp;
#define WIN_LD(i) { _Pragma("unroll") for (int j_ = 0; j_ < 8; ++j_) wv[i][j_] = NTL((const unsigned*)(wsrc + (128 * (i) + j_) * F2)); }
  if (a < 64) {
    const int row = 32 * a + 8 * w + (lane >> 3), c = lane & 7;
    v4u xv[10];
#pragma unroll
    for (int i = 0; i < 10; ++i) xv[i] = *(const v4u*)(p.u + (size_t)row * D + 64 * i + 8 * c);
    tag = __hip_atomic_load((GAS unsigned*)(p.ws + O_EPOCH), RLX_AGENT);
    SB();
    WIN_LD(0); WIN_LD(1);
    SB();
    float s0 = 0.f, s1 = 0.f, q0 = 0.f, q1 = 0.f;
#pragma unroll
    for (int i = 0; i < 10; ++i)
#pragma unroll
      for (int j = 0; j < 4; j += 2) {
        s0 = dot2(xv[i][j], ONES, s0); q0 = dot2(xv[i][j], xv[i][j], q0);
        s1 = dot2(xv[i][j + 1], ONES, s1); q1 = dot2(xv[i][j + 1], xv[i][j + 1], q1);
      }
    float m = s0 + s1, v = q0 + q1;
    m += dppf<0xB1>(m); v += dppf<0xB1>(v); m += dppf<0x4E>(m); v += dppf<0x4E>(v); m += dppf<0x141>(m); v += dppf<0x141>(v);
    m *= (1.0f / D);
    const float r = __builtin_amdgcn_rsqf(fmaxf(v * (1.0f / D) - m * m, 0.f) + 1e-5f);
    tag += 1u;
    if (c == 0) bst1(rs, O_STG + row * 16, v4u{f2u(m), tag, f2u(r), tag});
    SB();
    WIN_LD(2); WIN_LD(3); WIN_LD(4);
  } else if (a < 84) {
    const int rg = tid >> 3, cl = tid & 7, col0 = (a < 74 ? 2 * D + 64 * (a - 64) : 5 * D + 64 * (a - 74));
    unsigned* sL = (unsigned*)(smem + 12288);
    v4u mv[20];
    const v2u cv = *(const v2u*)(p.cvec + 4 * min(tid, 159));
#pragma unroll
    for (int i = 0; i < 20; ++i) mv[i] = __builtin_nontemporal_load((const v4u*)(p.w_mod + (size_t)(20 * rg + i) * 3840 + col0 + 8 * cl));
    tag = __hip_atomic_load((GAS unsigned*)(p.ws + O_EPOCH), RLX_AGENT);
    SB();
    if (tid < 160) *(v4u*)(sL + 4 * tid) = v4u{silu_bf(bflo(cv[0])), silu_bf(bfhi(cv[0])), silu_bf(bflo(cv[1])), silu_bf(bfhi(cv[1]))};
    __syncthreads();
    v4u s4[5];
#pragma unroll
    for (int i = 0; i < 5; ++i) s4[i] = *(const v4u*)(sL + 20 * rg + 4 * i);
    float acc[8];
#pragma unroll
    for (int j = 0; j < 8; ++j) acc[j] = 0.f;
#pragma unroll
    for (int i = 0; i < 20; ++i) {
      const unsigned slo = s4[i >> 2][i & 3], shi = slo << 16;
#pragma unroll
      for (int j = 0; j < 4; ++j) { acc[2 * j] = dot2(mv[i][j], slo, acc[2 * j]); acc[2 * j + 1] = dot2(mv[i][j], shi, acc[2 * j + 1]); }
    }
    const float s = col_reduce(fl, acc, tid);
    tag += 1u;
    if ((tid & 3) == 0) bst1_64(rs, O_MODG + (col0 + (tid >> 2)) * 8, v2u{f2u(rbf(s)), tag});
  } else {
    tag = __hip_atomic_load((GAS unsigned*)(p.ws + O_EPOCH), RLX_AGENT) + 1u;
    if (a == 92) {
      if (tid < 192) __hip_atomic_store((GAS unsigned*)(p.ws + O_CNT) + tid * 64, 0u, RLX_AGENT);
      __hip_atomic_store((GAS unsigned*)(p.ws + O_FLAG) + tid * 64, 0u, RLX_AGENT);
      DRAIN();
      __syncthreads();
      if (tid == 0) bst1_64(rs, O_INIT, v2u{0u, tag});
    }
  }
  if (a < 56) {
    float* c1L = (float*)(smem + 61440);
    u16* sbL = (u16*)(smem + 61440 + 2560);
    float* red = (float*)(smem + 16384);
#pragma unroll 1
    for (unsigned s = 0; s < SPIN; ++s) {
      asm volatile("" ::: "memory");
      v4u v[3][4];
#pragma unroll
      for (int i = 0; i < 3; ++i)
#pragma unroll
        for (int k = 0; k < 4; ++k) v[i][k] = bld1(rs, min(256 * i + tid, 639) * 16, O_MODP + (k * 2560 + 1280) * 8);
      const u64 vi = __hip_atomic_load((GAS u64*)(p.ws + O_INIT), RLX_AGENT);
      unsigned bad = (unsigned)(vi >> 32) ^ tag;
#pragma unroll
      for (int i = 0; i < 3; ++i) {
        const int e = min(256 * i + tid, 639);
#pragma unroll
        for (int k = 0; k < 4; ++k) bad |= (v[i][k][1] ^ tag) | (v[i][k][3] ^ tag);
        const unsigned pr = pk_bf16((u2f(v[i][0][0]) + u2f(v[i][1][0])) + (u2f(v[i][2][0]) + u2f(v[i][3][0])), (u2f(v[i][0][2]) + u2f(v[i][1][2])) + (u2f(v[i][2][2]) + u2f(v[i][3][2])));
        if (e < 320) *(unsigned*)(sbL + 2 * e) = pr; else *(f32x2*)(c1L + 2 * (e - 320)) = f32x2{rbf(1.0f + bflo(pr)), rbf(1.0f + bfhi(pr))};
      }
      PIN(bad);
      if (__all(bad == 0u)) break;
      __builtin_amdgcn_s_sleep(1);
    }
    ORDER();
    __syncthreads();
    f32x4 us[5];
    const int so = blk < 54 ? O_WINF + (2 * blk + (fp >> 3)) * 20480 + (rgp >> 2) * 1024 + ((rgp & 3) * 16 + (fp & 7)) * 16 : OOB;
    auto unit = [&](auto ic_) __attribute__((always_inline)) {
      constexpr int i = ic_;
      const int e = rgp + 16 * i, uo = so + i * 4096;
      const f32x4 ca = *(const f32x4*)(c1L + 8 * e), cb = *(const f32x4*)(c1L + 8 * e + 4);
      const v4u sb = *(const v4u*)(sbL + 8 * e);
      unsigned ta[4], tb[4], ra[4], rb[4];
#pragma unroll
      for (int j = 0; j < 4; ++j) {
        const f32x2 cc = j == 0 ? f32x2{ca[0], ca[1]} : j == 1 ? f32x2{ca[2], ca[3]} : j == 2 ? f32x2{cb[0], cb[1]} : f32x2{cb[2], cb[3]};
        const f32x2 ya = f32x2{bflo(wv[i][2 * j]), bflo(wv[i][2 * j + 1])} * cc, yb = f32x2{bfhi(wv[i][2 * j]), bfhi(wv[i][2 * j + 1])} * cc;
        ra[j] = pk_bf16(ya[0], ya[1]); rb[j] = pk_bf16(yb[0], yb[1]);
      }
      bst1(rs, uo, v4u{ra[0], ra[1], ra[2], ra[3]});
      bst1(rs, uo + 128, v4u{rb[0], rb[1], rb[2], rb[3]});
      float u1a = 0.f, u1b = 0.f, u2a = 0.f, u2b = 0.f;
#pragma unroll
      for (int j = 0; j < 4; ++j) {
        ta[j] = plo(wv[i][2 * j], wv[i][2 * j + 1]); tb[j] = phi(wv[i][2 * j], wv[i][2 * j + 1]);
        u1a = dot2(ra[j], ONES, u1a); u1b = dot2(rb[j], ONES, u1b);
        u2a = dot2(ta[j], sb[j], u2a); u2b = dot2(tb[j], sb[j], u2b);
      }
      us[i] = f32x4{u1a, u2a, u1b, u2b};
    };
    sfor<5>(unit);
    if (blk == 54) {
#pragma unroll
      for (int i = 0; i < 5; ++i) bst1(rs, O_WINF + 108 * 20480 + i * 4096 + tid * 16, v4u{ONES, ONES, ONES, ONES});
    }
    *(f32x4*)(red + tid * 4) = ((us[0] + us[1]) + (us[2] + us[3])) + us[4];
    __syncthreads();
    if (tid < 64) {
      const int c = (tid >> 1) & 15;
      float s = 0.f;
#pragma unroll
      for (int r = 0; r < 16; ++r) s += red[r * 64 + ((tid >> 5) * 8 + (c & 7)) * 4 + (c >> 3) * 2 + (tid & 1)];
      if (blk < 54) __builtin_amdgcn_raw_buffer_store_b32(f2u(s), rs, O_U + 32 * blk * 8 + tid * 4, 0, AUX_SC1);
    }
  } else {
    const int wi = (a >= 84 ? a - 84 : a - 44) * 4 + w;
    if (wi < 135) wout_unit(p, rs, smem + 16384 + w * 8192, wi, lane);
#pragma unroll 1
    for (unsigned s = 0; s < SPIN; ++s) {
      const u64 v = __hip_atomic_load((GAS u64*)(p.ws + O_INIT), RLX_AGENT);
      if (__all((unsigned)(v >> 32) == tag)) break;
      __builtin_amdgcn_s_sleep(1);
    }
    ORDER();
  }
  DRAIN();
  __syncthreads();
  arrive(p, a < 56 ? C_S : C_F, tid);
}

template <int CTRL> DEVI float dpp_add(float keep, float send) { return keep + dppf<CTRL>(send); }
DEVI float bfly16(const float (&v)[16], int i16) {
  float a[8], b[4], c[2];
  const bool h8 = i16 & 8, h4 = i16 & 4, h2 = i16 & 2, h1 = i16 & 1;
#pragma unroll
  for (int k = 0; k < 8; ++k) a[k] = dpp_add<0x128>(h8 ? v[k + 8] : v[k], h8 ? v[k] : v[k + 8]);
#pragma unroll
  for (int k = 0; k < 4; ++k) b[k] = dpp_add<0x141>(h4 ? a[k + 4] : a[k], h4 ? a[k] : a[k + 4]);
#pragma unroll
  for (int k = 0; k < 2; ++k) c[k] = dpp_add<0x4E>(h2 ? b[k + 2] : b[k], h2 ? b[k] : b[k + 2]);
  return dpp_add<0xB1>(h1 ? c[1] : c[0], h1 ? c[0] : c[1]);
}
DEVI f32x2 gelu2(f32x2 v) {
  const float c0 = -2.0f * 0.7978845608028654f * 1.4426950408889634f, c1 = c0 * 0.044715f;
  const f32x2 u = v * (v * v * c1 + c0);
  const f32x2 d = f32x2{__builtin_amdgcn_exp2f(u[0]), __builtin_amdgcn_exp2f(u[1])} + 1.0f;
  return v * f32x2{__builtin_amdgcn_rcpf(d[0]), __builtin_amdgcn_rcpf(d[1])};
}
__global__ void __launch_bounds__(256, 1) k_fused(Params p) {
  extern __shared__ __attribute__((aligned(16))) unsigned char smem[];
  const int tid = threadIdx.x, lane = tid & 63, w = __builtin_amdgcn_readfirstlane(tid >> 6);
  const int i16 = lane & 15, g = lane >> 4, l16 = lane * 16;
  const int bx = blockIdx.x & 7, bi = blockIdx.x >> 3;
  const rsrc_t rs = __builtin_amdgcn_make_buffer_rsrc((void*)p.ws, 0, O_END, 0x00020000);
  const rsrc_t rw = __builtin_amdgcn_make_buffer_rsrc((void*)p.w_sp, 0, T * T * 2, 0x00020000);
  unsigned tag;
  const unsigned lds0 = (unsigned)(size_t)(LAS unsigned char*)smem;
  u64 pc_; unsigned csz_;
  asm volatile("s_getpc_b64 %0\n.Lkf_beg:\n\ts_mov_b32 %1, .Lkf_end-.Lkf_beg" : "=s"(pc_), "=s"(csz_));
  auto code_prefetch = [&]() __attribute__((always_inline)) {
    const rsrc_t rc = __builtin_amdgcn_make_buffer_rsrc((void*)(pc_ & ~15ull), 0, (int)csz_, 0x00020000);
    if (w < 3) dma16(rc, l16, (bi * 3 + w) * 1024, lds0 + L_DUMMY + w * 1024);
  };
  const bool is_conv = bi < 20;
  const int q4 = bx * 20 + bi;
  const int panel = (bx >> 1) * 8 + (bi >> 2), j = (bx & 1) * 4 + (bi & 3);
  const int b0 = blk0(j);
  const int lb0 = (w < 3) ? 2 * w : 6, lb1 = 2 * w + 1;
  const bool has0 = (w < 3) || (j < 6), has1 = (w < 3);
  const int z2s0 = has0 ? O_WINF + (54 + b0 + lb0) * 20480 : OOB, z2s1 = O_WINF + (has1 ? 54 + b0 + lb1 : 108) * 20480;
  const int z1s0 = has0 ? O_WINF + (b0 + lb0) * 20480 : OOB, z1s1 = has1 ? O_WINF + (b0 + lb1) * 20480 : OOB;
  const int wx = w ^ (j & 3);
  const int wso = (64 * panel + 32 * (wx >> 1) + 4 * (wx & 1)) * 4096 + w * 1024;
#define WSP_DMA(lt, r, ldsoff) dma16x<(r) * 16>(rw, l16, (wso ^ ((32 * ((lt) >> 1) + 4 * ((lt) & 1)) * 4096)) + (8 * ((r) >> 2) + ((r) & 3)) * 4096, lds0 + (ldsoff) + w * 16384 + (r) * 1024)
  const int wrd = w * 16384 + i16 * 1024 + (g ^ (i16 & 3)) * 16;
#define W_ADDR(s) (wrq[(s) & 3] + ((s) >> 2) * 256)
  const int zu0 = has0 ? O_Z2N + (2 * panel) * 1024 + (b0 + lb0) * 65536 : OOB, zu1 = has1 ? O_Z2N + (2 * panel) * 1024 + (b0 + lb1) * 65536 : OOB;
  const int pu = O_P + ((4 * panel) * 27 + (w < 3 ? 3 * j + w : 24 + (j >> 1))) * 1024;
  const int pu16 = w < 3 ? pu : OOB, pu8 = (w == 3 && j < 6) ? pu + (j & 1) * 8 : OOB;
  v4u WR[32];
  int wrq[4];
#pragma unroll
  for (int q = 0; q < 4; ++q) wrq[q] = wrd + ((q ^ (i16 >> 2)) * 64);
#define WR_RD(q, dst) WR[dst] = *(const v4u*)(smem + L_WL + W_ADDR(q))
  v4u R[40];
#define R_LD(q, s0, s1) R[q] = bld1(rs, l16 + (((q) >> 1) & 3) * 1024, (((q) & 1) ? (s1) : (s0)) + ((q) >> 3) * 4096)
  bool oks = false;
  if (__builtin_expect(is_conv, 1)) {
    const int d = 4 * q4 + w;
    unsigned par[6];
    par[0] = NTL((const unsigned*)(p.a_re + d * NST + 2 * lane)); par[1] = NTL((const unsigned*)(p.a_im + d * NST + 2 * lane));
    par[2] = NTL((const unsigned*)(p.b_re + d * NST + 2 * lane)); par[3] = NTL((const unsigned*)(p.b_im + d * NST + 2 * lane));
    par[4] = NTL((const unsigned*)(p.c_re + d * NST + 2 * lane)); par[5] = NTL((const unsigned*)(p.c_im + d * NST + 2 * lane));
    const float ldt = bf2f(p.log_dt[d]);
    SB();
    v4u mv[5]; unsigned cv[5];
    const int mcg = q4 >> 2, mkq = q4 & 3;
#pragma unroll
    for (int i = 0; i < 5; ++i) {
      const int row = 160 * mkq + 32 * i + (tid >> 3);
      mv[i] = __builtin_nontemporal_load((const v4u*)(p.w_mod + (size_t)row * 3840 + 64 * mcg + (mcg >= 20 ? D : 0) + 8 * (tid & 7)));
      cv[i] = p.cvec[row];
    }
    const unsigned ep = __hip_atomic_load((GAS unsigned*)(p.ws + O_EPOCH), RLX_AGENT);
    const unsigned dku = p.dskip[d];
    v2u xa[8];
    const int ro = 64 * (tid >> 5) + 32 * ((tid >> 4) & 1) + 8 * ((tid >> 1) & 3) + 4 * ((tid >> 3) & 1) + 2 * (tid & 1);
    v4u st[4][2]; u64 mg;
    const int mo = lane < 32 ? O_MODP + ((lane & 3) * 2560 + (lane >> 4) * D + 4 * q4 + ((lane >> 2) & 3)) * 8 : (lane < 36 ? O_MODG + (2 * D + 4 * q4 + (lane & 3)) * 8 : O_INIT);
    auto gather = [&]() __attribute__((always_inline)) {
      mg = __hip_atomic_load((GAS u64*)(p.ws + mo), RLX_AGENT);
#pragma unroll
      for (int i = 0; i < 4; ++i) { st[i][0] = bld1(rs, ro * 16, O_STG + i * 8192); st[i][1] = bld1(rs, ro * 16 + 16, O_STG + i * 8192); }
    };
    const rsrc_t ru = __builtin_amdgcn_make_buffer_rsrc((void*)p.u, 0, T * D * 2, 0x00020000);
    auto xld = [&](int i) __attribute__((always_inline)) { xa[i] = __builtin_amdgcn_raw_buffer_load_b64(ru, ro * (2 * D), (512 * (i >> 1) + (i & 1)) * (2 * D) + 8 * q4, 0); };
    auto hook = [&](int i) __attribute__((always_inline)) {
      SB();
      if (i == MODHOOK) {
        tag = ep + 1u;
        float acc[8];
#pragma unroll
        for (int k = 0; k < 8; ++k) acc[k] = 0.f;
#pragma unroll
        for (int q = 0; q < 5; ++q) {
          const unsigned slo = silu_bf(bf2f(cv[q])), shi = slo << 16;
#pragma unroll
          for (int k = 0; k < 4; ++k) { acc[2 * k] = dot2(mv[q][k], slo, acc[2 * k]); acc[2 * k + 1] = dot2(mv[q][k], shi, acc[2 * k + 1]); }
        }
        const float s = col_reduce((float*)(smem + L_WL), acc, tid);
        if ((tid & 3) == 0) bst1_64(rs, O_MODP + (mkq * 2560 + 64 * mcg + (tid >> 2)) * 8, v2u{f2u(s), tag});
        SB();
        code_prefetch();
      }
      if (i == 2) xld(2);
      if (i == 4) { xld(3); xld(4); }
      if (i >= 5 && i <= 7) xld(i);
      if (i == 9) gather();
      SB();
    };
    SB();
    asm volatile("s_barrier" ::: "memory");
    xld(0); xld(1);
    SB();
    u16* himg = (u16*)(smem + L_HIMG);
    if (tid < 128) *(v4u*)(himg + (tid >> 5) * HL + (tid & 31) * 8) = v4u{0, 0, 0, 0};
    const float dt = rbf(__builtin_amdgcn_exp2f(ldt * 1.4426950408889634f));
    kgen_wave(par, dt, bf2f(dku), (unsigned*)(smem + L_KSCR) + w * 16 * RS, smem + L_KIMG + w * KIMG_B, lane, hook);
#pragma unroll 1
    for (unsigned s = 0; s < SPIN; ++s) {
      unsigned bad = (lane >> 2) == 8 ? 0u : (unsigned)(mg >> 32) ^ tag;
#pragma unroll
      for (int i = 0; i < 4; ++i) bad |= (st[i][0][1] ^ tag) | (st[i][0][3] ^ tag) | (st[i][1][1] ^ tag) | (st[i][1][3] ^ tag);
      PIN(bad);
      if (__all(bad == 0u)) break;
      asm volatile("" ::: "memory");
      gather();
    }
    ORDER();
    float sa[4], c1[4];
    {
      float ms = u2f((unsigned)mg);
      ms += dppf<0xB1>(ms); ms += dppf<0x4E>(ms);
      const unsigned mr = f2u(rbf(ms));
#pragma unroll
      for (int c = 0; c < 4; ++c) {
        sa[c] = u2f(__builtin_amdgcn_readlane(mr, 4 * c));
        c1[c] = rbf(1.0f + u2f(__builtin_amdgcn_readlane(mr, 16 + 4 * c)));
      }
    }
    if (!__all((unsigned)(mg >> 32) == tag)) mg = __hip_atomic_load((GAS u64*)(p.ws + mo), RLX_AGENT);
    SB();
#pragma unroll
    for (int i = 0; i < 4; ++i) {
      const int pr = 256 * i + tid;
      const f32x2 m = {u2f(st[i][0][0]), u2f(st[i][1][0])}, r = {u2f(st[i][0][2]), u2f(st[i][1][2])};
      const f32x2 x[4] = {f32x2{bflo(xa[2 * i][0]), bflo(xa[2 * i + 1][0])}, f32x2{bfhi(xa[2 * i][0]), bfhi(xa[2 * i + 1][0])},
                          f32x2{bflo(xa[2 * i][1]), bflo(xa[2 * i + 1][1])}, f32x2{bfhi(xa[2 * i][1]), bfhi(xa[2 * i + 1][1])}};
#pragma unroll
      for (int c = 0; c < 4; ++c) {
        const f32x2 h = (x[c] - m) * r * c1[c] + sa[c];
        *(unsigned*)(himg + c * HL + 256 + 512 * i + ro) = pk_bf16(h[0], h[1]);
      }
    }
    unsigned ab[8];
#pragma unroll
    for (int a = 0; a < 8; ++a) { ab[a] = lds0 + L_KIMG + w * KIMG_B + (i16 & 1) * (KL * 2) + (2032 - (i16 & ~1) - 2 * (i16 & 1) + 8 * g) * 2 - 64 * (8 * a + 7); PIN(ab[a]); }
    unsigned bb = lds0 + L_HIMG + (w * HL + 256 - 16 + 16 * i16 + 8 * g) * 2 - 64 * 7; PIN(bb);
#pragma unroll 1
    for (unsigned s = 0; s < SPIN; ++s) {
      if (__all((unsigned)(mg >> 32) == tag)) break;
      mg = __hip_atomic_load((GAS u64*)(p.ws + mo), RLX_AGENT);
    }
    float ga[4];
#pragma unroll
    for (int c = 0; c < 4; ++c) ga[c] = u2f(__builtin_amdgcn_readlane((unsigned)mg, 32 + c));
    __syncthreads();
    f32x4 acc[8];
#pragma unroll
    for (int c = 0; c < 8; ++c) acc[c] = f32x4{0.f, 0.f, 0.f, 0.f};
    v4u O[2][16];
#define CLOAD1(buf, r, k) { if constexpr (CL[k] < 8) { const LAS unsigned* ap = (const LAS unsigned*)(size_t)ab[CL[k] & 7] + 16 * (7 - (r)); O[buf][CL[k]] = v4u{ap[0], ap[1], ap[2], ap[3]}; } \
                            else O[buf][CL[k]] = *((const LAS v4u*)(size_t)bb + 4 * (8 * (CL[k] - 8) + 7 - (r))); }
    sfor<16>(LAM { constexpr int k = ic_; CLOAD1(0, 0, k); });
    SB();
    unsigned cav = 0;
    u16* ybuf = (u16*)(smem + L_KSCR);
    f32x2 xf[4][4];
#define Y_OUT(c) *(v2u*)(ybuf + w * T + 256 * (c) + 16 * i16 + 4 * g) = v2u{pk_bf16(acc[c][0], acc[c][1]), pk_bf16(acc[c][2], acc[c][3])}
    sfor<8 * 36>(LAM {
      constexpr int r = ic_ / 36, m = ic_ % 36;
      constexpr int ca = r < 7 ? CA[m] : CA7[m], cb = r < 7 ? CB[m] : CB7[m];
      acc[ca + cb] = mfma_bf(O[r & 1][ca], O[r & 1][8 + cb], acc[ca + cb]);
      if constexpr (r < 7 && (m & 1) == 0 && m < 32) CLOAD1((r + 1) & 1, r + 1, m >> 1);
      if constexpr (cs_dma(ic_)) WSP_DMA(0, ic_ / ASP, L_WL);
      if constexpr ((r == 5 || r == 6) && (m & 3) == 3 && m < 32) {
        constexpr int q = 8 * (r - 5) + (m >> 2), i = q >> 2, c = q & 3;
        xf[i][c] = (c & 1) ? f32x2{bfhi(xa[2 * i][c >> 1]), bfhi(xa[2 * i + 1][c >> 1])} : f32x2{bflo(xa[2 * i][c >> 1]), bflo(xa[2 * i + 1][c >> 1])};
        PIN(xf[i][c]);
      }
      if constexpr (r == 7) {
        if constexpr (m == 11) Y_OUT(7);
        if constexpr (m == 18) Y_OUT(6);
        if constexpr (m == 24) Y_OUT(5);
        if constexpr (m == 29) Y_OUT(4);
        if constexpr (m == 33) Y_OUT(3);
        if constexpr (m == 34) cav = cnt_load(p, C_S, lane);
      }
      SB();
    });
    Y_OUT(2); Y_OUT(1); Y_OUT(0);
    __syncthreads();
#pragma unroll
    for (int i = 0; i < 4; ++i) {
      const int pr = 256 * i + tid;
      f32x2 o[4];
#pragma unroll
      for (int c = 0; c < 4; ++c) o[c] = xf[i][c] + unp(*(const unsigned*)(ybuf + c * T + 512 * i + ro)) * ga[c];
      bst1(rs, O_X1G + (q4 * T + 2 * pr) * 8, v4u{pk_bf16(o[0][0], o[1][0]), pk_bf16(o[2][0], o[3][0]), pk_bf16(o[0][1], o[1][1]), pk_bf16(o[2][1], o[3][1])});
    }
    SB();
    oks = __all(cav >= 7u);
    ORDER();
  } else {
    aux_role(p, rs, smem, (bi - 20) * 8 + bx, tag);
    code_prefetch();
    sfor<16>(LAM { WSP_DMA(0, ic_, L_WL); });
    DRAIN();
  }
#pragma unroll
  for (int q = 0; q < 16; ++q) WR_RD(q, q);
  int otab;
  {
    const int lf = lane & 7;
    int ab = 2 * w + lf; ab = ab >= 7 ? ab - 7 : ab;
    const int zo = (ab == 6 && j >= 6) ? OOB : O_Z2N + (b0 + ab) * 65536 + 16 * w * 1024;
    const int sl = (ab * 3 + ((w - (ab >> 1) - 1) & 3)) * 4096;
    const int ro = (sl < 65536 ? L_WL + sl : sl) + ((w ^ (ab >> 1)) << 10);
    const int n = 6 * w + lf - 1;
    const int ko = (lf == 0 ? 3 * j + min(w, 2) : (n < 3 * j ? n : n + 3)) * 1024;
    const int lt = lane & 3;
    const int po = (lane & 4) ? ((lt ^ w) * 3 + lt - 1) * 5120 : O_P + (4 * panel + (lt ^ w)) * (27 * 1024);
    const int sel = (lane >> 3) & 3;
    otab = sel == 0 ? zo : (sel == 1 ? ro : (sel == 2 ? ko : po));
    PIN(otab);
  }
  asm volatile("s_waitcnt lgkmcnt(0)" ::: "memory");
  if (is_conv) {
    DRAIN();
    __syncthreads();
    arrive(p, C_X1, tid);
  }
  if (!oks) cnt_wait<4>(p, C_S, 7u, lane);
  {
    const rsrc_t rb = __builtin_amdgcn_make_buffer_rsrc((void*)p.b_sp, 0, T * 2, 0x00020000);
    if (w < 3) dma16s(rs, l16, w == 0 ? O_U + (FH + 16 * b0) * 8 : (w == 1 ? O_U + 16 * b0 * 8 : OOB), lds0 + L_DUMMY + w * 1024);
    else dma16(rb, l16, 128 * panel, lds0 + L_DUMMY + 3072);
  }
#pragma unroll
  for (int q = 0; q < NR1; ++q) R_LD(q, z2s0, z2s1);
  SB();
  sfor<16>(LAM { WSP_DMA(1, ic_, L_WL); });
  SB();
  const int xoff = ((lane >> 5) * T + 2 * (lane & 31)) * 8;
  const int xso = O_X1G + (2 * w * T + 64 * panel) * 8;
#define X_DMA(ks) dma16s(rs, xoff, xso + (ks) * (8 * T * 8), lds0 + ((ks) * 4 + w) * 1024)
  float* sxL = (float*)(smem + L_MISC);
  float* st1 = (float*)(smem + L_MISC + 512) + w * 128;
  float* gsum = (float*)(smem + L_MISC + 2560);
  float* st2 = (float*)(smem + L_MISC + 4608) + w * 128;
  unsigned xl[4];
#pragma unroll
  for (int i = 0; i < 4; ++i) { xl[i] = lds0 + g * 1024 + (i ^ wx) * 128 + i16 * 8; PIN(xl[i]); }
  auto ldx = [&](unsigned lo, int off) __attribute__((always_inline)) {
    const v2u a = *(const LAS v2u*)(size_t)(lo + off), c = *(const LAS v2u*)(size_t)(lo + off + 512);
    return v4u{a[0], a[1], c[0], c[1]};
  };
  const int pfo = ((w < 3 || j < 6) ? pu : OOB) + l16;
  f32x4 a0[4], a1[4], gram = {0.f, 0.f, 0.f, 0.f};
#pragma unroll
  for (int i = 0; i < 4; ++i) { a0[i] = f32x4{0.f, 0.f, 0.f, 0.f}; a1[i] = a0[i]; }
  if (w == 3) cnt_wait<2>(p, C_X1, 20u, lane);
  __syncthreads();
#pragma unroll
  for (int ks = 0; ks < 8; ++ks) X_DMA(ks);
  asm volatile("s_waitcnt vmcnt(4)\n\ts_barrier" ::: "memory");
  {
    v4u A[3][4];
    sfor<8>(LAM { A[ic_ >> 2][ic_ & 3] = ldx(xl[ic_ & 3], (ic_ >> 2) * 4096); SB(); });
    sfor<20 * 9>(LAM {
      constexpr int ks = ic_ / 9, m = ic_ % 9;
      if constexpr (m == 0 && ks == 2) asm volatile("s_waitcnt vmcnt(6)\n\ts_barrier" ::: "memory");
      if constexpr (m == 0 && (ks == 6 || ks == 10)) asm volatile("s_waitcnt vmcnt(12)\n\ts_barrier" ::: "memory");
      if constexpr (m == 0 && ks == 8) { sfor<4>([&](auto i_) __attribute__((always_inline)) { xl[i_] += 40960; PIN(xl[i_]); }); }
      if constexpr (m == 0 && ks < 19) __builtin_amdgcn_s_waitcnt(0xC47F);
      if constexpr (m == 2) gram = mfma_bf(A[ks % 3][0], A[ks % 3][0], gram);
      else {
        constexpr int mm = m < 2 ? m : m - 1;
        if constexpr (mm & 1) a1[mm >> 1] = mfma_bf(A[ks % 3][mm >> 1], R[2 * ks + 1], a1[mm >> 1]);
        else a0[mm >> 1] = mfma_bf(A[ks % 3][mm >> 1], R[2 * ks], a0[mm >> 1]);
      }
      if constexpr ((m & 1) == 1 && ks < 18) A[(ks + 2) % 3][m >> 1] = ldx(xl[m >> 1], (ks + 2 - (ks >= 8 ? 10 : 0)) * 4096);
      if constexpr (ks >= 18 && m < 8) WR_RD(8 * (ks - 18) + m, 16 + 8 * (ks - 18) + m);
      if constexpr ((m == 2 || m == 4 || m == 8) && ks < 4) X_DMA(8 + 3 * ks + (m >> 2));
      if constexpr ((m == 4 || m == 8) && ks >= 4 && !(ks >= FK && ks < FK + 2)) { constexpr int n = NR1 + 2 * (ks - 4 - (ks >= FK ? 2 : 0)) + (m == 8); if constexpr (n < 40) R_LD(n, z2s0, z2s1); else R_LD(n - 40, z1s0, z1s1); }
      if constexpr ((m == 4 || m == 8) && ks >= FK && ks < FK + 2)
        bst1(rs, pfo + (2 * (ks - FK) + (m == 8)) * (27 * 1024), v4u{SENT, SENT, SENT, SENT});
      SB();
    });
  }
  if (w == 3) {
    if (i16 == 0) {
#pragma unroll
      for (int i = 0; i < 4; ++i) *(f32x4*)(sxL + (i ^ wx) * 16 + 4 * g) = a1[i];
    }
#pragma unroll
    for (int i = 0; i < 4; ++i) a1[i] = f32x4{0.f, 0.f, 0.f, 0.f};
  }
  if ((i16 >> 2) == g) sxL[64 + wx * 16 + i16] = (i16 & 3) == 0 ? gram[0] : (i16 & 3) == 1 ? gram[1] : (i16 & 3) == 2 ? gram[2] : gram[3];
  __syncthreads();
  {
    const float m = sxL[lane] * (1.0f / D);
    const float var = fmaxf(sxL[64 + lane] * (1.0f / D) - m * m, 0.f);
    const float r = __builtin_amdgcn_rsqf(var + 1e-5f);
    st1[lane] = r; st1[64 + lane] = r * m;
  }
  v4u A1[3][4];
  {
    const f32x2* cU2 = (const f32x2*)(smem + L_DUMMY);
    f32x2 u0 = cU2[16 * lb0 + i16], u1 = cU2[16 * lb1 + i16];
    if (!has0) u0 = f32x2{0.f, 0.f};
    if (!has1) u1 = f32x2{0.f, 0.f};
    float s1[16], s2[16];
    f32x4 rr[4], rm[4];
#pragma unroll
    for (int i = 0; i < 4; ++i) { rr[i] = *(const f32x4*)(st1 + (i ^ wx) * 16 + 4 * g); rm[i] = *(const f32x4*)(st1 + 64 + (i ^ wx) * 16 + 4 * g); }
    sfor<8>(LAM {
      constexpr int i = ic_ >> 1, h = ic_ & 1;
      { constexpr int n0 = NR1 - 12, nn = 40 - n0;
#pragma unroll
        for (int q = n0 + nn * ic_ / 8; q < n0 + nn * (ic_ + 1) / 8; ++q) R_LD(q, z1s0, z1s1); }
      const f32x2 r2 = {rr[i][2 * h], rr[i][2 * h + 1]}, m2 = {rm[i][2 * h], rm[i][2 * h + 1]};
      const f32x2 v0 = gelu2(f32x2{a0[i][2 * h], a0[i][2 * h + 1]} * r2 + (u0[1] - m2 * u0[0]));
      const f32x2 v1 = gelu2(f32x2{a1[i][2 * h], a1[i][2 * h + 1]} * r2 + (u1[1] - m2 * u1[0]));
      a0[i][2 * h] = v0[0]; a0[i][2 * h + 1] = v0[1]; a1[i][2 * h] = v1[0]; a1[i][2 * h + 1] = v1[1];
      f32x2 q1 = v0 + v1, q2 = v0 * v0 + v1 * v1;
      PIN(q1); PIN(q2);
      s1[4 * i + 2 * h] = q1[0]; s1[4 * i + 2 * h + 1] = q1[1]; s2[4 * i + 2 * h] = q2[0]; s2[4 * i + 2 * h + 1] = q2[1];
      SB();
    });
    sfor<4>(LAM { xl[ic_] -= 40960; PIN(xl[ic_]); A1[0][ic_] = ldx(xl[ic_], 0); SB(); A1[1][ic_] = ldx(xl[ic_], 4096); SB(); });
    const float t1 = bfly16(s1, i16), t2 = bfly16(s2, i16);
    const int tp = ((i16 >> 2) ^ wx) * 16 + 4 * g + (i16 & 3);
    asm volatile("s_waitcnt vmcnt(%0)" :: "n"(52 - NR1) : "memory");
    bst1(rs, O_ZST + (((panel * 4 + w) * 8 + j) * 64 + tp) * 16, v4u{f2u(t1), tag, f2u(t2), tag});
  }
  SB();
  f32x4 c0[4], c1[4];
#pragma unroll
  for (int i = 0; i < 4; ++i) { c0[i] = f32x4{0.f, 0.f, 0.f, 0.f}; c1[i] = c0[i]; }
#define Z1M(ks, m) { if constexpr ((m) == 0 && (ks) >= 2 && (ks) < 19 && (ks) != KA && (ks) != KA + 1) __builtin_amdgcn_s_waitcnt(0xC47F); \
    if constexpr ((m) & 1) c1[(m) >> 1] = mfma_bf(R[2 * (ks) + 1], A1[(ks) % 3][(m) >> 1], c1[(m) >> 1]); else c0[(m) >> 1] = mfma_bf(R[2 * (ks)], A1[(ks) % 3][(m) >> 1], c0[(m) >> 1]); \
    if constexpr (((m) & 1) == 1 && (ks) < 18) A1[((ks) + 2) % 3][(m) >> 1] = ldx(xl[(m) >> 1], ((ks) + 2 - ((ks) >= KA ? KA : 0)) * 4096); }
  constexpr int KA = KAV;
  v4u zs[8];
  unsigned cfv = 0;
  const int zsoff = O_ZST + ((panel * 4 + w) * 8 * 64) * 16;
  sfor<KA * 8>(LAM {
    constexpr int ks = ic_ / 8, m = ic_ % 8;
    Z1M(ks, m);
    if constexpr ((m == 2 || m == 6) && ks < 8) { constexpr int q = 2 * ks + (m == 6); WSP_DMA(2, q, L_WL); }
    if constexpr (m == 4 && ks == 7) cfv = cnt_load(p, C_F, lane);
    if constexpr ((m & 1) == 0 && ks >= 8 && ks < 10) { constexpr int jj = 4 * (ks - 8) + (m >> 1); zs[jj] = bld1(rs, l16 + (jj & 3) * 1024, zsoff + (jj >> 2) * 4096); }
    SB();
  });
  v2u x1v[5];
#pragma unroll
  for (int db = 0; db < 5; ++db) {
    const int d = 80 * j + 16 * db + 4 * g;
    x1v[db] = *(const v2u*)(smem + (d >> 2) * 512 + (16 * w + i16) * 8);
  }
  {
#pragma unroll 1
    for (unsigned s = 0; s < SPIN; ++s) {
      unsigned bad = 0;
#pragma unroll
      for (int jj = 0; jj < 8; ++jj) bad |= (zs[jj][1] ^ tag) | (zs[jj][3] ^ tag);
      PIN(bad);
      if (__all(bad == 0u)) break;
      asm volatile("" ::: "memory");
#pragma unroll
      for (int jj = 0; jj < 8; ++jj) zs[jj] = bld1(rs, l16, zsoff + jj * 1024);
    }
    float a = 0.f, b = 0.f;
#pragma unroll
    for (int jj = 0; jj < 8; ++jj) { a += u2f(zs[jj][0]); b += u2f(zs[jj][2]); }
    *(f32x2*)(gsum + (w * 64 + lane) * 2) = f32x2{a, b};
  }
  if (!__all(cfv >= 5u)) cnt_wait<1>(p, C_F, 5u, lane);
  ORDER();
  __syncthreads();
  {
    const f32x2 s = (*(const f32x2*)(gsum + lane * 2) + *(const f32x2*)(gsum + 128 + lane * 2)) + (*(const f32x2*)(gsum + 256 + lane * 2) + *(const f32x2*)(gsum + 384 + lane * 2));
    const float m = s[0] * (1.0f / FH);
    const float var = fmaxf(s[1] * (1.0f / FH) - m * m, 0.f);
    const float r = __builtin_amdgcn_rsqf(var + 1e-5f);
    st2[lane] = r; st2[64 + lane] = r * m;
  }
  {
    unsigned o0[4][2], o1[4][2];
#pragma unroll
    for (int i = 0; i < 4; ++i) {
      const f32x4 rr = *(const f32x4*)(st2 + (i ^ wx) * 16 + 4 * g), rm = *(const f32x4*)(st2 + 64 + (i ^ wx) * 16 + 4 * g);
      const f32x4 n0 = a0[i] * rr - rm, n1 = a1[i] * rr - rm;
      o0[i][0] = pk_bf16(n0[0], n0[1]); o0[i][1] = pk_bf16(n0[2], n0[3]);
      o1[i][0] = pk_bf16(n1[0], n1[1]); o1[i][1] = pk_bf16(n1[2], n1[3]);
    }
    const bool sw = wx & 1;
    const int ph = (wx >> 1) * 1024;
#pragma unroll
    for (int pp = 0; pp < 2; ++pp) {
      const int e = 2 * pp, o = 2 * pp + 1;
      bst1(rs, zu0 + ((pp * 1024) ^ ph) + l16, sw ? v4u{o0[o][0], o0[o][1], o0[e][0], o0[e][1]} : v4u{o0[e][0], o0[e][1], o0[o][0], o0[o][1]});
      bst1(rs, zu1 + ((pp * 1024) ^ ph) + l16, sw ? v4u{o1[o][0], o1[o][1], o1[e][0], o1[e][1]} : v4u{o1[e][0], o1[e][1], o1[o][0], o1[o][1]});
    }
  }
  sfor<4>(LAM { xl[ic_] += KA * 4096; PIN(xl[ic_]); });
  SB();
  sfor<(20 - KA) * 8>(LAM {
    constexpr int ks = KA + ic_ / 8, m = ic_ % 8;
    Z1M(ks, m);
    SB();
  });
  DRAIN();
  __syncthreads();
  GAS unsigned* flags = (GAS unsigned*)(p.ws + O_FLAG);
  if (tid < 4) __hip_atomic_fetch_or(flags + ((j * 4 + (panel >> 3)) * 4 + tid) * 64, 1u << (panel & 7), RLX_AGENT);
  if (w == 2) dma16s(rs, l16, O_MODG + (5 * D + 80 * j) * 8, lds0 + L_DUMMY + 2048);
  int zof[7], rof[7], kof[7];
#pragma unroll
  for (int q = 0; q < 7; ++q) { zof[q] = __builtin_amdgcn_readlane(otab, q); rof[q] = __builtin_amdgcn_readlane(otab, 8 + q); kof[q] = __builtin_amdgcn_readlane(otab, 16 + q); }
  constexpr int ZD = ZDV;
  v4u Z[16][7];
#define Z_LD(i, f) Z[i][f] = bld1(rs, l16 + ((i) & 3) * 1024, zof[f] + ((i) >> 2) * 4096)
  unsigned zmask = 0u, zfv = 0u;
  GAS unsigned* zfp = flags + ((j * 4 + w) * 4 + (panel >> 3)) * 64;
  auto zneed = [&](unsigned bits) __attribute__((always_inline)) {
    if (__builtin_expect((zmask & bits) != bits, 0)) {
#pragma unroll 1
      for (unsigned s = 0; s < SPIN; ++s) {
        zmask = __builtin_amdgcn_readfirstlane(__hip_atomic_load(zfp, RLX_AGENT));
        if ((zmask & bits) == bits) break;
        __builtin_amdgcn_s_sleep(1);
      }
    }
    ORDER();
  };
  unsigned z1p[2][4][2];
  {
    const v4u* cU1 = (const v4u*)(smem + L_DUMMY + 1024);
    const v4u ua0 = cU1[8 * lb0 + 2 * g], ua1 = cU1[8 * lb0 + 2 * g + 1], ub0 = cU1[8 * lb1 + 2 * g], ub1 = cU1[8 * lb1 + 2 * g + 1];
    float sr[4], sm[4];
#pragma unroll
    for (int i = 0; i < 4; ++i) { sr[i] = st1[16 * (i ^ wx) + i16]; sm[i] = st1[64 + 16 * (i ^ wx) + i16]; }
    sfor<8>(LAM {
      constexpr int i = ic_ >> 1, h = ic_ & 1;
      if constexpr (ic_ < 4) { WSP_DMA(3, 4 * ic_, 0); WSP_DMA(3, 4 * ic_ + 1, 0); WSP_DMA(3, 4 * ic_ + 2, 0); WSP_DMA(3, 4 * ic_ + 3, 0); }
      if constexpr (ic_ == ZPOLL - 2) { zfv = __hip_atomic_load(zfp, RLX_AGENT); SB(); }
      if constexpr (ic_ == ZPOLL) { zmask = __builtin_amdgcn_readfirstlane(zfv); zneed(1u); }
      if constexpr (ic_ >= ZPOLL) {
        constexpr int n = 8 - ZPOLL, k = ic_ - ZPOLL;
#pragma unroll
        for (int q = 14 * k / n; q < 14 * (k + 1) / n; ++q) Z_LD(q / 7, q % 7);
        SB();
      }
      const v4u qa = h ? ua1 : ua0, qb = h ? ub1 : ub0;
      const f32x2 v0 = gelu2(f32x2{c0[i][2 * h], c0[i][2 * h + 1]} * sr[i] + (f32x2{u2f(qa[1]), u2f(qa[3])} - f32x2{u2f(qa[0]), u2f(qa[2])} * sm[i]));
      const f32x2 v1 = gelu2(f32x2{c1[i][2 * h], c1[i][2 * h + 1]} * sr[i] + (f32x2{u2f(qb[1]), u2f(qb[3])} - f32x2{u2f(qb[0]), u2f(qb[2])} * sm[i]));
      z1p[0][i][h] = pk_bf16(v0[0], v0[1]); z1p[1][i][h] = pk_bf16(v1[0], v1[1]);
      PIN(z1p[0][i][h]); PIN(z1p[1][i][h]);
    });
  }
  SB();
  f32x4 acc[7][4];
  const int woso = O_WOUTF + (5 * j) * 27 * 1024;
  v4u WO[35];
  v4u WL[4][2];
#define WO_LD(q) WO[q] = bld1(rs, l16, woso + ((q) % 5) * (27 * 1024) + kof[(q) / 5])
#define WL_RD(i, h) WL[(i) & 3][h] = *(const v4u*)(smem + ((h) ? 0 : L_WL) + W_ADDR(i))
  WL_RD(0, 0); WL_RD(0, 1);
  sfor<13 * 28>(LAM {
    constexpr int i = ic_ / 28, f = (ic_ % 28) / 4, t = ic_ % 4;
    constexpr int lo = zs_lo(i, ZD), hi = zs_lo(i + 1, ZD);
    if constexpr (f == 0 && t == 0 && hi > lo && ((lo & 1) == 0 || hi - lo > 1)) zneed((1u << ((hi - 1) >> 1)) | (1u << (lo >> 1)));
    if constexpr (t < 2) mfma_pin<i == 0, true>(acc[f][t], Z[i][f], WR[t * 16 + i]);
    else mfma_pin<i == 0, false>(acc[f][t], Z[i][f], WL[i & 3][t - 2]);
    if constexpr (hi - lo == 2 && (t == 0 || t == 2)) { constexpr int q = 2 * f + (t >> 1); Z_LD(lo + q / 7, q % 7); }
    if constexpr (hi - lo == 1 && t == 0) Z_LD(lo, f);
    if constexpr (t == 1 && f < 2 && i < 12) WL_RD(i + 1, f);
    if constexpr (t == 1 && f < 6 && i == 12) WL_RD(13 + (f >> 1), f & 1);
    SB();
  });
  asm volatile("s_nop 7\n\ts_waitcnt lgkmcnt(0)\n\ts_barrier" ::: "memory");
  sfor<7 * 12>(LAM {
    constexpr int fo = ic_ / 12, f = (fo + 2) % 7, i = 13 + (ic_ % 12) / 4, t = ic_ % 4, m = ic_ % 12;
    if constexpr (t < 2) acc[f][t] = mfma_bf(Z[i][f], WR[t * 16 + i], acc[f][t]);
    else acc[f][t] = mfma_bf(Z[i][f], WL[i & 3][t - 2], acc[f][t]);
    if constexpr (m % 4 == 0 && 3 * fo + m / 4 < NWO) WO_LD(3 * fo + m / 4);
    if constexpr (fo >= 1 && fo <= 5 && m % 3 == 2) { constexpr int pf = fo + 1, pt = m / 3; *(f32x4*)(smem + ((rof[pf] + l16) ^ (pt << 10))) = acc[pf][pt]; }
    SB();
  });
  if (w == 3) {
#pragma unroll
    for (int t = 0; t < 4; ++t) *(f32x4*)(smem + ((rof[1] + l16) ^ (t << 10))) = acc[1][t];
  }
  f32x4 o0[4], o1[4];
#pragma unroll
  for (int t = 0; t < 4; ++t) {
    const float bias = bf2f(((const u16*)(smem + L_DUMMY + 3072))[8 * (i16 >> 2) + (i16 & 3) + 32 * ((t ^ wx) >> 1) + 4 * ((t ^ wx) & 1)]);
    o0[t] = acc[0][t] + bias; o1[t] = acc[1][t] + bias; PIN(o0[t]); PIN(o1[t]);
  }
  __syncthreads();
  v4u PO[4];
  {
    f32x4 pa[4][3], pb[4][3];
#pragma unroll
    for (int s = 0; s < 3; ++s) {
      const int s0 = (2 * w * 3 + s) * 4096, s1 = ((w < 3 ? 2 * w + 1 : 6) * 3 + s) * 4096;
      const unsigned char* r0 = smem + (s0 < 65536 ? L_WL + s0 : s0) + l16;
      const unsigned char* r1 = smem + (s1 < 65536 ? L_WL + s1 : s1) + l16;
#pragma unroll
      for (int t = 0; t < 4; ++t) { pa[t][s] = *(const f32x4*)(r0 + t * 1024); pb[t][s] = *(const f32x4*)(r1 + t * 1024); }
    }
    SB();
#pragma unroll
    for (int t = 0; t < 4; ++t) {
      const f32x4 x0 = (o0[t] + pa[t][0]) + (pa[t][1] + pa[t][2]), x1 = (o1[t] + pb[t][0]) + (pb[t][1] + pb[t][2]);
      const f32x2 qa = unp(z1p[0][t][0]) * f32x2{x0[0], x0[1]}, qb = unp(z1p[0][t][1]) * f32x2{x0[2], x0[3]};
      const f32x2 qc = unp(z1p[1][t][0]) * f32x2{x1[0], x1[1]}, qd = unp(z1p[1][t][1]) * f32x2{x1[2], x1[3]};
      PO[t] = v4u{min(pk_bf16(qa[0], qa[1]), SENT - 1u), min(pk_bf16(qb[0], qb[1]), SENT - 1u), min(pk_bf16(qc[0], qc[1]), SENT - 1u), min(pk_bf16(qd[0], qd[1]), SENT - 1u)};
      bst1(rs, pu16 + (t ^ wx) * (27 * 1024) + l16, PO[t]);
      bst1_64(rs, pu8 + (t ^ wx) * (27 * 1024) + l16, v2u{PO[t][0], PO[t][1]});
    }
  }
  SB();
  if (w == 3) {
#pragma unroll
    for (int t = 0; t < 4; ++t) PO[t] = v4u{0u, 0u, 0u, 0u};
  }
  if (j & 1) { const v4u a = PO[0], c = PO[2]; PO[0] = PO[1]; PO[1] = a; PO[2] = PO[3]; PO[3] = c; }
  if (j & 2) { const v4u a = PO[0], c = PO[1]; PO[0] = PO[2]; PO[2] = a; PO[1] = PO[3]; PO[3] = c; }
  f32x4 e0[5][4], e[5][4];
#pragma unroll
  for (int t = 0; t < 4; ++t)
#pragma unroll
    for (int d = 0; d < 5; ++d) e0[d][t] = mfma_bf(WO[d], PO[t], f32x4{0.f, 0.f, 0.f, 0.f});
  const int trow = 64 * panel + 8 * (i16 >> 2) + (i16 & 3) + 32 * (w >> 1) + 4 * (w & 1);
  int pof[4], eof[4];
#pragma unroll
  for (int q = 0; q < 4; ++q) { pof[q] = __builtin_amdgcn_readlane(otab, 24 + q); eof[q] = __builtin_amdgcn_readlane(otab, 28 + q); }
  const int ppo = lane < 48 ? O_P + (4 * panel + ((lane >> 1) & 3)) * (27 * 1024) + __builtin_amdgcn_ds_bpermute((17 + (lane >> 3)) * 4, otab) + 1012 + (lane & 1) * 8 : OOB;
  v4u gb0[5], gb1[5];
#pragma unroll
  for (int d = 0; d < 5; ++d) { gb0[d] = *(const v4u*)(smem + L_DUMMY + 2048 + (16 * d + 4 * g) * 8); gb1[d] = *(const v4u*)(smem + L_DUMMY + 2048 + (16 * d + 4 * g) * 8 + 16); }
  SB();
#pragma unroll
  for (int q = NWO; q < 35; ++q) WO_LD(q);
  SB();
  v4u P[24];
#define P_LD(q) P[q] = bld1(rs, l16, pof[((q) / 6 + 1) & 3] + kof[1 + (q) % 6])
  constexpr int PDN = PDNV;
#pragma unroll 1
  for (unsigned tries = 0; tries < 4u; ++tries) {
#pragma unroll 1
    for (unsigned s = 0; s < SPIN; ++s) {
      asm volatile("" ::: "memory");
      const unsigned m = __builtin_amdgcn_raw_buffer_load_b32(rs, ppo, 0, AUX_SC1);
#pragma unroll
      for (int q = 0; q < PDN; ++q) P_LD(q);
      if (!__any(m == SENT)) break;
    }
    asm volatile("" ::: "memory");
    unsigned pm = 0u;
    sfor<24 * 5>(LAM {
      constexpr int q = ic_ / 5, d = ic_ % 5, lo = q / 6, lt = (lo + 1) & 3, kk = 1 + q % 6;
      e[d][lt] = mfma_bf(WO[5 * kk + d], P[q], kk == 1 ? e0[d][lt] : e[d][lt]);
      if constexpr (d == 0 && q + PDN < 24) P_LD(q + PDN);
      if constexpr (d == 1) { pm = mx4(pm, P[q]); PIN(pm); }
      if constexpr (lo >= 1 && d == 3 && q % 6 < 5) *(f32x4*)(smem + eof[lo] + (q % 6) * 1024 + l16) = e[q % 6][lo];
      SB();
    });
    if (__builtin_expect(!__any(pm == SENT), 1)) break;
  }
  __syncthreads();
  u16* orow = p.out + (size_t)trow * D + 80 * j + 4 * g;
  f32x4 ep[5][3];
#pragma unroll
  for (int d = 0; d < 5; ++d)
#pragma unroll
    for (int s = 0; s < 3; ++s) ep[d][s] = *(const f32x4*)(smem + ((w * 3 + s) * 5 + d) * 1024 + l16);
  SB();
#pragma unroll
  for (int d = 0; d < 5; ++d) {
    const f32x4 eo = (e[d][0] + ep[d][0]) + (ep[d][1] + ep[d][2]);
    const float r0 = bflo(x1v[d][0]) + u2f(gb0[d][0]) * eo[0], r1 = bfhi(x1v[d][0]) + u2f(gb0[d][2]) * eo[1];
    const float r2 = bflo(x1v[d][1]) + u2f(gb1[d][0]) * eo[2], r3 = bfhi(x1v[d][1]) + u2f(gb1[d][2]) * eo[3];
    *(v2u*)(orow + 16 * d) = v2u{pk_bf16(r0, r1), pk_bf16(r2, r3)};
  }
  if (blockIdx.x == 0 && tid == 0) __hip_atomic_store((GAS unsigned*)(p.ws + O_EPOCH), tag, RLX_AGENT);
  asm volatile(".Lkf_end:");
}

extern "C" void kernel_launch(void* const* d_in, const int* in_sizes, int n_in, void* d_out, int out_size, void* d_ws,
                              size_t ws_size, hipStream_t stream) {
  Params p;
  p.u = (const u16*)d_in[0]; p.a_re = (const u16*)d_in[1]; p.a_im = (const u16*)d_in[2];
  p.b_re = (const u16*)d_in[3]; p.b_im = (const u16*)d_in[4]; p.c_re = (const u16*)d_in[5]; p.c_im = (const u16*)d_in[6];
  p.log_dt = (const u16*)d_in[7]; p.dskip = (const u16*)d_in[8];
  p.w_in = (const u16*)d_in[10]; p.w_sp = (const u16*)d_in[11]; p.b_sp = (const u16*)d_in[12]; p.w_out = (const u16*)d_in[13];
  p.cvec = (const u16*)d_in[14]; p.w_mod = (const u16*)d_in[15];
  p.out = (u16*)d_out; p.ws = (unsigned char*)d_ws;
  (void)hipFuncSetAttribute((const void*)k_fused, hipFuncAttributeMaxDynamicSharedMemorySize, L_TOTAL);
  k_fused<<<256, 256, L_TOTAL, stream>>>(p);
}
```

```cpp
#include <hip/hip_runtime.h>
#include <hip/hip_bf16.h>
#include <stdint.h>
#include <utility>

typedef unsigned v4u __attribute__((ext_vector_type(4)));
typedef unsigned v2u __attribute__((ext_vector_type(2)));
typedef float f32x4 __attribute__((ext_vector_type(4)));
typedef float f32x2 __attribute__((ext_vector_type(2)));
typedef short bf16x8 __attribute__((ext_vector_type(8)));
typedef _Float16 f16x8 __attribute__((ext_vector_type(8)));
typedef _Float16 f16x2 __attribute__((ext_vector_type(2)));
typedef __bf16 bf16x2_t __attribute__((ext_vector_type(2)));
typedef unsigned short u16;
typedef unsigned long long u64;
#define GAS __attribute__((address_space(1)))
#define LAS __attribute__((address_space(3)))
#define RLX_AGENT __ATOMIC_RELAXED, __HIP_MEMORY_SCOPE_AGENT
#define DEVI __device__ __forceinline__
#define SB() __builtin_amdgcn_sched_barrier(0)
template <int... I, class F> __device__ __forceinline__ void sfor_impl(std::integer_sequence<int, I...>, F&& f) { (f(std::integral_constant<int, I>{}), ...); }
template <int N, class F> __device__ __forceinline__ void sfor(F&& f) { sfor_impl(std::make_integer_sequence<int, N>{}, f); }
#define LAM [&](auto ic_) __attribute__((always_inline))
#define NTL(p) __builtin_nontemporal_load(p)
#define DRAIN() asm volatile("s_waitcnt vmcnt(0)" ::: "memory")

constexpr int T = 2048, D = 640, NST = 128, FH = 864, F2 = 1728;
constexpr int KL = 2080, KIMG_B = 2 * KL * 2, HL = 2304;
constexpr unsigned SPIN = 1u << 15;
constexpr int AUX_SC1 = 16, AUX_NT = 2;
constexpr int O_EPOCH = 0;
constexpr int O_INIT = 256;
constexpr int O_CNT = 4096;
constexpr int O_FLAG = O_CNT + 3 * 64 * 256;
constexpr int O_MODG = O_FLAG + 256 * 256;
constexpr int O_MODP = O_MODG + 3840 * 8 + 2048;
constexpr int O_STG = O_MODP + 4 * 2560 * 8;
constexpr int O_U = O_STG + 2048 * 16;
constexpr int O_ZST = O_U + 16384;
constexpr int O_X1G = O_ZST + 32 * 32 * 64 * 16;
constexpr int O_WINF = O_X1G + T * D * 2;
constexpr int O_WOUTF = O_WINF + 109 * 20 * 1024;
constexpr int O_Z2N = O_WOUTF + 40 * 27 * 1024;
constexpr int O_P = O_Z2N + 54 * 64 * 1024;
constexpr int O_END = O_P + 128 * 27 * 1024;
constexpr int C_X1 = 0, C_S = 1, C_F = 2;
constexpr unsigned SENT = 0xffffffffu;

DEVI float u2f(unsigned u) { return __builtin_bit_cast(float, u); }
DEVI unsigned f2u(float f) { return __builtin_bit_cast(unsigned, f); }
DEVI float bf2f(unsigned b) { return u2f(b << 16); }
DEVI float bflo(unsigned u) { return u2f(u << 16); }
DEVI float bfhi(unsigned u) { return u2f(u & 0xffff0000u); }
DEVI unsigned pk_bf16(float a, float b) {
  f32x2 v = {a, b};
  bf16x2_t r = __builtin_convertvector(v, bf16x2_t);
  return __builtin_bit_cast(unsigned, r);
}
DEVI float rbf(float a) { return bflo(pk_bf16(a, 0.f)); }
DEVI unsigned pk_f16(float a, float b) {
  f32x2 v = {a, b};
  f16x2 r = __builtin_convertvector(v, f16x2);
  return __builtin_bit_cast(unsigned, r);
}
DEVI f32x4 mfma_bf(v4u a, v4u b, f32x4 c) {
  return __builtin_amdgcn_mfma_f32_16x16x32_bf16(__builtin_bit_cast(bf16x8, a), __builtin_bit_cast(bf16x8, b), c, 0, 0, 0);
}
DEVI f32x4 mfma_h(v4u a, v4u b, f32x4 c) {
  return __builtin_amdgcn_mfma_f32_16x16x32_f16(__builtin_bit_cast(f16x8, a), __builtin_bit_cast(f16x8, b), c, 0, 0, 0);
}
template <bool FIRST, bool BA> DEVI void mfma_pin(f32x4& acc, const v4u& a, const v4u& bb) {
  if constexpr (FIRST) {
    if constexpr (BA) asm volatile("v_mfma_f32_16x16x32_bf16 %0, %1, %2, 0" : "=a"(acc) : "v"(a), "a"(bb));
    else asm volatile("v_mfma_f32_16x16x32_bf16 %0, %1, %2, 0" : "=a"(acc) : "v"(a), "v"(bb));
  } else {
    if constexpr (BA) asm volatile("v_mfma_f32_16x16x32_bf16 %0, %1, %2, %0" : "+a"(acc) : "v"(a), "a"(bb));
    else asm volatile("v_mfma_f32_16x16x32_bf16 %0, %1, %2, %0" : "+a"(acc) : "v"(a), "v"(bb));
  }
}
typedef __amdgpu_buffer_rsrc_t rsrc_t;
DEVI v4u bld1(rsrc_t rs, int voff, int soff = 0) { return __builtin_amdgcn_raw_buffer_load_b128(rs, voff, soff, AUX_SC1); }
DEVI void bst1(rsrc_t rs, int voff, v4u v) { __builtin_amdgcn_raw_buffer_store_b128(v, rs, voff, 0, AUX_SC1); }
DEVI void bst1_64(rsrc_t rs, int voff, v2u v) { __builtin_amdgcn_raw_buffer_store_b64(v, rs, voff, 0, AUX_SC1); }
template <int CTRL> DEVI float dppf(float x) { return u2f((unsigned)__builtin_amdgcn_update_dpp(0, (int)f2u(x), CTRL, 0xf, 0xf, true)); }
DEVI float dot2(unsigned a, unsigned b, float c) { return __builtin_amdgcn_fdot2_f32_bf16(__builtin_bit_cast(bf16x2_t, a), __builtin_bit_cast(bf16x2_t, b), c, false); }
DEVI unsigned mx4(unsigned m, v4u v) { return max(max(max(m, v[0]), v[1]), max(v[2], v[3])); }
#define PIN(x) asm volatile("" : "+v"(x))
#define ORDER() __builtin_amdgcn_fence(__ATOMIC_ACQUIRE, "wavefront")

struct Params {
  const u16 *u, *a_re, *a_im, *b_re, *b_im, *c_re, *c_im, *log_dt, *dskip, *w_in, *w_sp, *b_sp, *w_out, *cvec, *w_mod;
  u16* out;
  unsigned char* ws;
};

DEVI int blk0(int j) { return j < 6 ? 7 * j : 42 + 6 * (j - 6); }
DEVI int pair_blk(int ks3, int h) {
  if (ks3 < 24) { const int j = ks3 / 3, w = ks3 - 3 * j; return blk0(j) + 2 * w + h; }
  return 7 * (2 * (ks3 - 24) + h) + 6;
}
DEVI void arrive(const Params& p, int c, int tid) {
  if (tid < 8) __hip_atomic_fetch_add((GAS unsigned*)(p.ws + O_CNT) + ((c * 8 + tid) * 8 + (blockIdx.x & 7)) * 64, 1u, RLX_AGENT);
}
DEVI unsigned cnt_load(const Params& p, int c, int lane) {
  return __hip_atomic_load((GAS unsigned*)(p.ws + O_CNT) + ((c * 8 + ((blockIdx.x >> 3) & 7)) * 8 + (lane & 7)) * 64, RLX_AGENT);
}
template <int SLP> DEVI void cnt_wait(const Params& p, int c, unsigned want, int lane) {
#pragma unroll 1
  for (unsigned s = 0; s < SPIN; ++s) {
    if (__all(cnt_load(p, c, lane) >= want)) break;
    __builtin_amdgcn_s_sleep(SLP);
  }
  ORDER();
}
struct C2 { f32x2 re, im; };
DEVI C2 cmul2(C2 a, C2 b) { return C2{a.re * b.re - a.im * b.im, a.re * b.im + a.im * b.re}; }
DEVI C2 cexp2(f32x2 are2, f32x2 arev, float l) {
  const f32x2 e = are2 * l, q = arev * l;
  const f32x2 m = {__builtin_amdgcn_exp2f(e[0]), __builtin_amdgcn_exp2f(e[1])};
  const float p0 = __builtin_amdgcn_fractf(q[0]), p1 = __builtin_amdgcn_fractf(q[1]);
  return C2{m * f32x2{__builtin_amdgcn_cosf(p0), __builtin_amdgcn_cosf(p1)}, m * f32x2{__builtin_amdgcn_sinf(p0), __builtin_amdgcn_sinf(p1)}};
}
constexpr int RS = 132;
DEVI void put_row(unsigned* buf, int row, int lane, C2 v) { *(v2u*)(buf + row * RS + 2 * lane) = v2u{pk_f16(v.re[0], v.im[0]), pk_f16(v.re[1], v.im[1])}; }
DEVI f32x2 unp(unsigned u) { return f32x2{bflo(u), bfhi(u)}; }

template <class HK> DEVI void kgen_wave(unsigned par[6], float dt, float dsk, unsigned* buf0, unsigned char* img, int lane, HK&& hook) {
  unsigned* buf1 = (unsigned*)img;
  u16* tab = (u16*)buf0;
  const int c = lane & 15, g = lane >> 4;
  const f32x2 are2 = unp(par[0]) * (dt * 1.4426950408889634f), arev = unp(par[1]) * (dt * 0.15915494309189535f), nrev = -arev;
  C2 W = cmul2(C2{unp(par[4]), unp(par[5])}, C2{unp(par[2]) * dt, unp(par[3]) * dt});
  float mx = fmaxf(fmaxf(fabsf(W.re[0]), fabsf(W.re[1])), fmaxf(fabsf(W.im[0]), fabsf(W.im[1])));
  unsigned mu = f2u(mx);
  mu = max(mu, f2u(dppf<0x128>(u2f(mu)))); mu = max(mu, f2u(dppf<0x124>(u2f(mu)))); mu = max(mu, f2u(dppf<0x122>(u2f(mu)))); mu = max(mu, f2u(dppf<0x121>(u2f(mu))));
  const unsigned m0 = __builtin_amdgcn_readlane(mu, 0), m1 = __builtin_amdgcn_readlane(mu, 16), m2 = __builtin_amdgcn_readlane(mu, 32), m3 = __builtin_amdgcn_readlane(mu, 48);
  unsigned eb = (max(max(m0, m1), max(m2, m3)) >> 23) & 0xffu;
  eb = eb < 2u ? 2u : (eb > 250u ? 250u : eb);
  const float sc = u2f((eb + 1u) << 23), isc = u2f((253u - eb) << 23);
  W.re *= isc; W.im *= isc;
  {
    const C2 e8 = cexp2(are2, arev, 8.f);
    C2 W8 = cmul2(W, cexp2(are2, arev, 64.f));
#pragma unroll
    for (int b = 0; b < 8; ++b) {
      put_row(buf0, b, lane, W); put_row(buf0, 8 + b, lane, W8);
      if (b < 7) { W = cmul2(W, e8); W8 = cmul2(W8, e8); }
      if (b == 0) hook(2);
      if (b == 4) hook(3);
    }
  }
  v4u A[8];
#pragma unroll
  for (int ks = 0; ks < 8; ++ks) A[ks] = *(const v4u*)(buf0 + c * RS + 16 * ks + 4 * g);
  C2 er[8];
  er[0] = C2{f32x2{1.f, 1.f}, f32x2{0.f, 0.f}};
  er[1] = cexp2(are2, nrev, 1.f);
  er[2] = cmul2(er[1], er[1]); er[3] = cmul2(er[2], er[1]); er[4] = cmul2(er[2], er[2]);
  er[5] = cmul2(er[4], er[1]); er[6] = cmul2(er[3], er[3]); er[7] = cmul2(er[4], er[3]);
  hook(4);
  const C2 e128 = cexp2(are2, nrev, 128.f);
#pragma unroll
  for (int r = 0; r < 8; ++r) { put_row(buf0, r, lane, er[r]); put_row(buf0, 8 + r, lane, r ? cmul2(e128, er[r]) : e128); }
  f32x4 acc0 = {0.f, 0.f, 0.f, 0.f}, acc1 = acc0, acc2 = acc0;
#pragma unroll
  for (int ks = 0; ks < 8; ++ks) acc0 = mfma_h(A[ks], *(const v4u*)(buf0 + c * RS + 16 * ks + 4 * g), acc0);
  hook(5);
  C2 e = cmul2(e128, e128);
  C2 f = cexp2(are2, nrev, 1024.f);
#pragma unroll
  for (int a = 0; a < 2; ++a) {
#pragma unroll
    for (int r = 0; r < 4; ++r) put_row(buf0, 4 * a + r, lane, r ? cmul2(e, er[2 * r]) : e);
    e = cmul2(e, e128);
  }
  hook(6);
#pragma unroll
  for (int a = 0; a < 4; ++a) {
    put_row(buf0, 8 + 2 * a, lane, e); put_row(buf0, 9 + 2 * a, lane, cmul2(e, er[4])); if (a < 3) e = cmul2(e, e128);
    put_row(buf1, 2 * a, lane, f); f = cmul2(f, e128); put_row(buf1, 2 * a + 1, lane, f); f = cmul2(f, e128);
    if (a == 1) hook(7);
  }
  put_row(buf1, 8, lane, f);
  hook(8);
  {
    v4u B1[8], B2[8];
#pragma unroll
    for (int ks = 0; ks < 8; ++ks) { B1[ks] = *(const v4u*)(buf0 + c * RS + 16 * ks + 4 * g); B2[ks] = *(const v4u*)(buf1 + c * RS + 16 * ks + 4 * g); }
    SB();
#pragma unroll
    for (int ks = 0; ks < 8; ++ks) { acc1 = mfma_h(A[ks], B1[ks], acc1); acc2 = mfma_h(A[ks], B2[ks], acc2); }
  }
  hook(9);
  const int cv0 = 128 * (c >> 3) + (c & 7);
  const int cv1 = c < 8 ? 256 + 128 * (c >> 2) + 2 * (c & 3) : 512 + 128 * ((c - 8) >> 1) + 4 * (c & 1);
  const int cv2 = c < 9 ? 1024 + 128 * c : 2048;
#pragma unroll
  for (int r = 0; r < 4; ++r) {
    const int lb = 8 * (4 * g + r);
    const unsigned q01 = pk_bf16(acc0[r] * sc + ((r == 0 && lane == 0) ? dsk : 0.f), acc1[r] * sc);
    tab[lb + cv0] = (u16)q01;
    tab[lb + cv1] = (u16)(q01 >> 16);
    tab[lb + cv2 + (c > 8 ? 1 : 0)] = (u16)pk_bf16(acc2[r] * sc, 0.f);
  }
  {
    const u16* t0 = tab + 2048 - 8 * lane;
    const int o = lane < 32 ? 1 : 0;
    const unsigned H0 = t0[0], L0 = t0[-8], H1 = t0[-512], L1 = t0[-520], H2 = t0[-1024], M2 = t0[-1028], L2 = t0[-1032];
    const u16* t3 = t0 - 1536;
    const unsigned v0 = t3[0], v1 = t3[-1 + o], v2 = t3[-2], v3 = t3[-3 - o], v4 = t3[-4], v5 = t3[-5 + o], v6 = t3[-6], v7 = t3[-7 - o], v8 = t3[-8];
    const unsigned k0 = tab[0];
    SB();
    unsigned char* i0 = img + lane * 16;
    unsigned char* i1 = i0 + KL * 2;
    {
      const unsigned X = (lane & 1) ? L0 : H0, HH = H0 | (H0 << 16), LL = L0 | (L0 << 16);
      *(v4u*)i0 = v4u{HH, HH, X | (L0 << 16), LL};
      *(v4u*)i1 = v4u{HH, H0 | (X << 16), LL, LL};
    }
    {
      const unsigned X = (lane & 1) ? L1 : H1, HH = H1 | (H1 << 16), LL = L1 | (L1 << 16);
      *(v4u*)(i0 + 1024) = v4u{HH, HH, X | (L1 << 16), LL};
      *(v4u*)(i1 + 1024) = v4u{HH, H1 | (X << 16), LL, LL};
    }
    {
      const unsigned HH = H2 | (H2 << 16), MM = M2 | (M2 << 16), LL = L2 | (L2 << 16);
      *(v4u*)(i0 + 2048) = v4u{HH, H2 | (M2 << 16), MM, LL};
      *(v4u*)(i1 + 2048) = v4u{HH, MM, M2 | (L2 << 16), LL};
    }
    *(v4u*)(i0 + 3072) = v4u{v0 | (v1 << 16), v2 | (v3 << 16), v4 | (v5 << 16), v6 | (v7 << 16)};
    *(v4u*)(i1 + 3072) = v4u{v1 | (v2 << 16), v3 | (v4 << 16), v5 | (v6 << 16), v7 | (v8 << 16)};
    if (lane < 8) *(v4u*)(img + (lane >> 2) * (KL * 2) + (256 + (lane & 3)) * 16) = v4u{lane == 0 ? k0 : 0u, 0u, 0u, 0u};
  }
}

constexpr int L_KIMG = 0, L_HIMG = 4 * KIMG_B, L_KSCR = L_HIMG + 4 * HL * 2;
constexpr int L_MISC = 86016, L_WL = L_MISC + 8192, L_DUMMY = L_WL + 65536, L_TOTAL = L_DUMMY + 4096;
static_assert(L_TOTAL <= 163840 && L_KSCR + 4 * 16 * RS * 4 <= L_MISC, "lds");
constexpr int MODHOOK = 4;
constexpr int ASP = 8;
constexpr int KAV = 11;
constexpr int NR1 = 24;
constexpr int FK = 12;
constexpr int ZDV = 3;
constexpr int NWO = 21;
constexpr int PDNV = 8;
constexpr int ZPOLL = 6;
constexpr int OOB = 64 << 20;
constexpr int CA[36] = {3,3,3,3,3,4,4,4,4, 2,2,2,2,2,2,5,5,5, 1,1,1,1,6,6,1,1,1, 0,0,0,0,7,0,0,0,0};
constexpr int CB[36] = {4,3,2,1,0,3,2,1,0, 5,4,3,2,1,0,2,1,0, 6,5,4,3,1,0,2,1,0, 7,6,5,4,0,3,2,1,0};
constexpr int CA7[36] = {3,4,2,5,1,6,0,7, 3,4,2,5,1,6,0, 3,4,2,5,1,0, 3,4,2,1,0, 3,2,1,0, 2,1,0, 1,0, 0};
constexpr int CB7[36] = {4,3,5,2,6,1,7,0, 3,2,4,1,5,0,6, 2,1,3,0,4,5, 1,0,2,3,4, 0,1,2,3, 0,1,2, 0,1, 0};
constexpr int CL[16] = {8+4,3,8+3,8+2,8+1,8+0,4,8+5,2,5,8+6,1,6,8+7,0,7};

constexpr bool cs_dma(int ic) { return (ic % ASP) == 5 && ic / ASP < 16; }
constexpr int zs_lo(int i, int zd) {
  int v = i + zd; if (v > 2 + 2 * i) v = 2 + 2 * i; if (i >= 13 || v > 16) v = 16; return v;
}
DEVI void dma16s(rsrc_t r, int voff, int soff, unsigned ldsaddr) {
  asm volatile("s_mov_b32 m0, %2\n\ts_nop 0\n\tbuffer_load_dwordx4 %0, %1, %3 offen sc1 lds" :: "v"(voff), "s"(r), "s"(ldsaddr), "s"(soff) : "memory");
}
template <int X> DEVI void dma16x(rsrc_t r, int voff, int soff, unsigned ldsaddr) {
  int tmp;
  asm volatile("s_mov_b32 m0, %3\n\tv_xor_b32 %0, %5, %1\n\tbuffer_load_dwordx4 %0, %2, %4 offen lds" : "=&v"(tmp) : "v"(voff), "s"(r), "s"(ldsaddr), "s"(soff), "n"(X) : "memory");
}
DEVI void dma16(rsrc_t r, int voff, int soff, unsigned ldsaddr) {
  asm volatile("s_mov_b32 m0, %2\n\ts_nop 0\n\tbuffer_load_dwordx4 %0, %1, %3 offen lds" :: "v"(voff), "s"(r), "s"(ldsaddr), "s"(soff) : "memory");
}

DEVI unsigned plo(unsigned a, unsigned b) { return __builtin_amdgcn_perm(b, a, 0x05040100u); }
DEVI unsigned phi(unsigned a, unsigned b) { return __builtin_amdgcn_perm(b, a, 0x07060302u); }
DEVI unsigned silu_bf(float c) { return pk_bf16(c * __builtin_amdgcn_rcpf(1.0f + __builtin_amdgcn_exp2f(-1.4426950408889634f * c)), 0.f); }
DEVI float col_reduce(float* fl, const float (&acc)[8], int tid) {
  const int rg = tid >> 3, cl = tid & 7;
  *(f32x4*)(fl + rg * 68 + cl * 8) = f32x4{acc[0], acc[1], acc[2], acc[3]};
  *(f32x4*)(fl + rg * 68 + cl * 8 + 4) = f32x4{acc[4], acc[5], acc[6], acc[7]};
  __syncthreads();
  float s = 0.f;
#pragma unroll
  for (int r = 0; r < 8; ++r) s += fl[(8 * (tid & 3) + r) * 68 + (tid >> 2)];
  s += dppf<0xB1>(s); s += dppf<0x4E>(s);
  return s;
}
DEVI void wout_unit(const Params& p, rsrc_t rs, unsigned char* lw, int unit, int lane) {
  const int ks = unit / 5, dc = unit - 5 * ks, d = 128 * dc + 2 * lane;
  const int f0 = 16 * pair_blk(ks, 0), f1 = 16 * pair_blk(ks, 1);
  unsigned wv[4][8];
#pragma unroll
  for (int g = 0; g < 4; ++g)
#pragma unroll
    for (int j = 0; j < 8; ++j) wv[g][j] = NTL((const unsigned*)(p.w_out + (size_t)((j < 4 ? f0 : f1) + 2 * ((4 * g + (j & 3)) & 7) + (g >> 1)) * D + d));
#pragma unroll
  for (int g = 0; g < 4; ++g) {
    unsigned char* dst = lw + (lane >> 3) * 1024 + (g * 16 + 2 * (lane & 7)) * 16;
    *(v4u*)dst = v4u{plo(wv[g][0], wv[g][1]), plo(wv[g][2], wv[g][3]), plo(wv[g][4], wv[g][5]), plo(wv[g][6], wv[g][7])};
    *(v4u*)(dst + 16) = v4u{phi(wv[g][0], wv[g][1]), phi(wv[g][2], wv[g][3]), phi(wv[g][4], wv[g][5]), phi(wv[g][6], wv[g][7])};
  }
#pragma unroll
  for (int i = 0; i < 8; ++i) bst1(rs, O_WOUTF + ((8 * dc + i) * 27 + ks) * 1024 + lane * 16, *(const v4u*)(lw + i * 1024 + lane * 16));
}

DEVI void aux_role(const Params& p, rsrc_t rs, unsigned char* smem, int a, unsigned& tag) {
  const int tid = threadIdx.x, lane = tid & 63, w = __builtin_amdgcn_readfirstlane(tid >> 6);
  float* fl = (float*)smem;
  constexpr unsigned ONES = 0x3f803f80u;
  unsigned wv[5][8];
  const int blk = (a & 7) * 7 + (a >> 3), fp = tid & 15, rgp = tid >> 4;
  const u16* wsrc = p.w_in + (size_t)(8 * rgp) * F2 + 32 * min(blk, 53) + 2 * fp;
#define WIN_LD(i) { _Pragma("unroll") for (int j_ = 0; j_ < 8; ++j_) wv[i][j_] = NTL((const unsigned*)(wsrc + (128 * (i) + j_) * F2)); }
  if (a < 64) {
    const int row = 32 * a + 8 * w + (lane >> 3), c = lane & 7;
    v4u xv[10];
#pragma unroll
    for (int i = 0; i < 10; ++i) xv[i] = *(const v4u*)(p.u + (size_t)row * D + 64 * i + 8 * c);
    tag = __hip_atomic_load((GAS unsigned*)(p.ws + O_EPOCH), RLX_AGENT);
    SB();
    WIN_LD(0); WIN_LD(1);
    SB();
    float s0 = 0.f, s1 = 0.f, q0 = 0.f, q1 = 0.f;
#pragma unroll
    for (int i = 0; i < 10; ++i)
#pragma unroll
      for (int j = 0; j < 4; j += 2) {
        s0 = dot2(xv[i][j], ONES, s0); q0 = dot2(xv[i][j], xv[i][j], q0);
        s1 = dot2(xv[i][j + 1], ONES, s1); q1 = dot2(xv[i][j + 1], xv[i][j + 1], q1);
      }
    float m = s0 + s1, v = q0 + q1;
    m += dppf<0xB1>(m); v += dppf<0xB1>(v); m += dppf<0x4E>(m); v += dppf<0x4E>(v); m += dppf<0x141>(m); v += dppf<0x141>(v);
    m *= (1.0f / D);
    const float r = __builtin_amdgcn_rsqf(fmaxf(v * (1.0f / D) - m * m, 0.f) + 1e-5f);
    tag += 1u;
    if (c == 0) bst1(rs, O_STG + row * 16, v4u{f2u(m), tag, f2u(r), tag});
    SB();
    WIN_LD(2); WIN_LD(3); WIN_LD(4);
  } else if (a < 84) {
    const int rg = tid >> 3, cl = tid & 7, col0 = (a < 74 ? 2 * D + 64 * (a - 64) : 5 * D + 64 * (a - 74));
    unsigned* sL = (unsigned*)(smem + 12288);
    v4u mv[20];
    const v2u cv = *(const v2u*)(p.cvec + 4 * min(tid, 159));
#pragma unroll
    for (int i = 0; i < 20; ++i) mv[i] = __builtin_nontemporal_load((const v4u*)(p.w_mod + (size_t)(20 * rg + i) * 3840 + col0 + 8 * cl));
    tag = __hip_atomic_load((GAS unsigned*)(p.ws + O_EPOCH), RLX_AGENT);
    SB();
    if (tid < 160) *(v4u*)(sL + 4 * tid) = v4u{silu_bf(bflo(cv[0])), silu_bf(bfhi(cv[0])), silu_bf(bflo(cv[1])), silu_bf(bfhi(cv[1]))};
    __syncthreads();
    v4u s4[5];
#pragma unroll
    for (int i = 0; i < 5; ++i) s4[i] = *(const v4u*)(sL + 20 * rg + 4 * i);
    float acc[8];
#pragma unroll
    for (int j = 0; j < 8; ++j) acc[j] = 0.f;
#pragma unroll
    for (int i = 0; i < 20; ++i) {
      const unsigned slo = s4[i >> 2][i & 3], shi = slo << 16;
#pragma unroll
      for (int j = 0; j < 4; ++j) { acc[2 * j] = dot2(mv[i][j], slo, acc[2 * j]); acc[2 * j + 1] = dot2(mv[i][j], shi, acc[2 * j + 1]); }
    }
    const float s = col_reduce(fl, acc, tid);
    tag += 1u;
    if ((tid & 3) == 0) bst1_64(rs, O_MODG + (col0 + (tid >> 2)) * 8, v2u{f2u(rbf(s)), tag});
  } else {
    tag = __hip_atomic_load((GAS unsigned*)(p.ws + O_EPOCH), RLX_AGENT) + 1u;
    if (a == 92) {
      if (tid < 192) __hip_atomic_store((GAS unsigned*)(p.ws + O_CNT) + tid * 64, 0u, RLX_AGENT);
      __hip_atomic_store((GAS unsigned*)(p.ws + O_FLAG) + tid * 64, 0u, RLX_AGENT);
      DRAIN();
      __syncthreads();
      if (tid == 0) bst1_64(rs, O_INIT, v2u{0u, tag});
    }
  }
  if (a < 56) {
    float* c1L = (float*)(smem + 61440);
    u16* sbL = (u16*)(smem + 61440 + 2560);
    float* red = (float*)(smem + 16384);
#pragma unroll 1
    for (unsigned s = 0; s < SPIN; ++s) {
      asm volatile("" ::: "memory");
      v4u v[3][4];
#pragma unroll
      for (int i = 0; i < 3; ++i)
#pragma unroll
        for (int k = 0; k < 4; ++k) v[i][k] = bld1(rs, min(256 * i + tid, 639) * 16, O_MODP + (k * 2560 + 1280) * 8);
      const u64 vi = __hip_atomic_load((GAS u64*)(p.ws + O_INIT), RLX_AGENT);
      unsigned bad = (unsigned)(vi >> 32) ^ tag;
#pragma unroll
      for (int i = 0; i < 3; ++i) {
        const int e = min(256 * i + tid, 639);
#pragma unroll
        for (int k = 0; k < 4; ++k) bad |= (v[i][k][1] ^ tag) | (v[i][k][3] ^ tag);
        const unsigned pr = pk_bf16((u2f(v[i][0][0]) + u2f(v[i][1][0])) + (u2f(v[i][2][0]) + u2f(v[i][3][0])), (u2f(v[i][0][2]) + u2f(v[i][1][2])) + (u2f(v[i][2][2]) + u2f(v[i][3][2])));
        if (e < 320) *(unsigned*)(sbL + 2 * e) = pr; else *(f32x2*)(c1L + 2 * (e - 320)) = f32x2{rbf(1.0f + bflo(pr)), rbf(1.0f + bfhi(pr))};
      }
      PIN(bad);
      if (__all(bad == 0u)) break;
      __builtin_amdgcn_s_sleep(1);
    }
    ORDER();
    __syncthreads();
    f32x4 us[5];
    const int so = blk < 54 ? O_WINF + (2 * blk + (fp >> 3)) * 20480 + (rgp >> 2) * 1024 + ((rgp & 3) * 16 + (fp & 7)) * 16 : OOB;
    auto unit = [&](auto ic_) __attribute__((always_inline)) {
      constexpr int i = ic_;
      const int e = rgp + 16 * i, uo = so + i * 4096;
      const f32x4 ca = *(const f32x4*)(c1L + 8 * e), cb = *(const f32x4*)(c1L + 8 * e + 4);
      const v4u sb = *(const v4u*)(sbL + 8 * e);
      unsigned ta[4], tb[4], ra[4], rb[4];
#pragma unroll
      for (int j = 0; j < 4; ++j) {
        const f32x2 cc = j == 0 ? f32x2{ca[0], ca[1]} : j == 1 ? f32x2{ca[2], ca[3]} : j == 2 ? f32x2{cb[0], cb[1]} : f32x2{cb[2], cb[3]};
        const f32x2 ya = f32x2{bflo(wv[i][2 * j]), bflo(wv[i][2 * j + 1])} * cc, yb = f32x2{bfhi(wv[i][2 * j]), bfhi(wv[i][2 * j + 1])} * cc;
        ra[j] = pk_bf16(ya[0], ya[1]); rb[j] = pk_bf16(yb[0], yb[1]);
      }
      bst1(rs, uo, v4u{ra[0], ra[1], ra[2], ra[3]});
      bst1(rs, uo + 128, v4u{rb[0], rb[1], rb[2], rb[3]});
      float u1a = 0.f, u1b = 0.f, u2a = 0.f, u2b = 0.f;
#pragma unroll
      for (int j = 0; j < 4; ++j) {
        ta[j] = plo(wv[i][2 * j], wv[i][2 * j + 1]); tb[j] = phi(wv[i][2 * j], wv[i][2 * j + 1]);
        u1a = dot2(ra[j], ONES, u1a); u1b = dot2(rb[j], ONES, u1b);
        u2a = dot2(ta[j], sb[j], u2a); u2b = dot2(tb[j], sb[j], u2b);
      }
      us[i] = f32x4{u1a, u2a, u1b, u2b};
    };
    sfor<5>(unit);
    if (blk == 54) {
#pragma unroll
      for (int i = 0; i < 5; ++i) bst1(rs, O_WINF + 108 * 20480 + i * 4096 + tid * 16, v4u{ONES, ONES, ONES, ONES});
    }
    *(f32x4*)(red + tid * 4) = ((us[0] + us[1]) + (us[2] + us[3])) + us[4];
    __syncthreads();
    if (tid < 64) {
      const int c = (tid >> 1) & 15;
      float s = 0.f;
#pragma unroll
      for (int r = 0; r < 16; ++r) s += red[r * 64 + ((tid >> 5) * 8 + (c & 7)) * 4 + (c >> 3) * 2 + (tid & 1)];
      if (blk < 54) __builtin_amdgcn_raw_buffer_store_b32(f2u(s), rs, O_U + 32 * blk * 8 + tid * 4, 0, AUX_SC1);
    }
  } else {
    const int wi = (a >= 84 ? a - 84 : a - 44) * 4 + w;
    if (wi < 135) wout_unit(p, rs, smem + 16384 + w * 8192, wi, lane);
#pragma unroll 1
    for (unsigned s = 0; s < SPIN; ++s) {
      const u64 v = __hip_atomic_load((GAS u64*)(p.ws + O_INIT), RLX_AGENT);
      if (__all((unsigned)(v >> 32) == tag)) break;
      __builtin_amdgcn_s_sleep(1);
    }
    ORDER();
  }
  DRAIN();
  __syncthreads();
  arrive(p, a < 56 ? C_S : C_F, tid);
  asm volatile(".Lkf_aend:");
}

template <int CTRL> DEVI float dpp_add(float keep, float send) { return keep + dppf<CTRL>(send); }
DEVI float bfly16(const float (&v)[16], int i16) {
  float a[8], b[4], c[2];
  const bool h8 = i16 & 8, h4 = i16 & 4, h2 = i16 & 2, h1 = i16 & 1;
#pragma unroll
  for (int k = 0; k < 8; ++k) a[k] = dpp_add<0x128>(h8 ? v[k + 8] : v[k], h8 ? v[k] : v[k + 8]);
#pragma unroll
  for (int k = 0; k < 4; ++k) b[k] = dpp_add<0x141>(h4 ? a[k + 4] : a[k], h4 ? a[k] : a[k + 4]);
#pragma unroll
  for (int k = 0; k < 2; ++k) c[k] = dpp_add<0x4E>(h2 ? b[k + 2] : b[k], h2 ? b[k] : b[k + 2]);
  return dpp_add<0xB1>(h1 ? c[1] : c[0], h1 ? c[0] : c[1]);
}
DEVI f32x2 gelu2(f32x2 v) {
  const float c0 = -2.0f * 0.7978845608028654f * 1.4426950408889634f, c1 = c0 * 0.044715f;
  const f32x2 u = v * (v * v * c1 + c0);
  const f32x2 d = f32x2{__builtin_amdgcn_exp2f(u[0]), __builtin_amdgcn_exp2f(u[1])} + 1.0f;
  return v * f32x2{__builtin_amdgcn_rcpf(d[0]), __builtin_amdgcn_rcpf(d[1])};
}
__global__ void __launch_bounds__(256, 1) k_fused(Params p) {
  extern __shared__ __attribute__((aligned(16))) unsigned char smem[];
  const int tid = threadIdx.x, lane = tid & 63, w = __builtin_amdgcn_readfirstlane(tid >> 6);
  const int i16 = lane & 15, g = lane >> 4, l16 = lane * 16;
  const int bx = blockIdx.x & 7, bi = blockIdx.x >> 3;
  const rsrc_t rs = __builtin_amdgcn_make_buffer_rsrc((void*)p.ws, 0, O_END, 0x00020000);
  const rsrc_t rw = __builtin_amdgcn_make_buffer_rsrc((void*)p.w_sp, 0, T * T * 2, 0x00020000);
  unsigned tag;
  const unsigned lds0 = (unsigned)(size_t)(LAS unsigned char*)smem;
  u64 pc_; unsigned csz_, csz2_;
  asm volatile("s_getpc_b64 %0\n.Lkf_beg:\n\ts_mov_b32 %1, .Lkf_end-.Lkf_beg\n\ts_mov_b32 %2, .Lkf_aend-.Lkf_beg" : "=s"(pc_), "=s"(csz_), "=s"(csz2_));
  if (bi == 31) {
    const rsrc_t rc = __builtin_amdgcn_make_buffer_rsrc((void*)(pc_ & ~15ull), 0, (int)max(csz_, csz2_), 0x00020000);
#pragma unroll 1
    for (int q = w; q < 96; q += 4) dma16(rc, l16, q * 1024, lds0 + L_DUMMY + w * 1024);
  }
  auto code_prefetch = [&]() __attribute__((always_inline)) {
    const rsrc_t rc = __builtin_amdgcn_make_buffer_rsrc((void*)(pc_ & ~15ull), 0, (int)csz_, 0x00020000);
    if (w < 3) dma16(rc, l16, (bi * 3 + w) * 1024, lds0 + L_DUMMY + w * 1024);
  };
  const bool is_conv = bi < 20;
  const int q4 = bx * 20 + bi;
  const int panel = (bx >> 1) * 8 + (bi >> 2), j = (bx & 1) * 4 + (bi & 3);
  const int b0 = blk0(j);
  const int lb0 = (w < 3) ? 2 * w : 6, lb1 = 2 * w + 1;
  const bool has0 = (w < 3) || (j < 6), has1 = (w < 3);
  const int z2s0 = has0 ? O_WINF + (54 + b0 + lb0) * 20480 : OOB, z2s1 = O_WINF + (has1 ? 54 + b0 + lb1 : 108) * 20480;
  const int z1s0 = has0 ? O_WINF + (b0 + lb0) * 20480 : OOB, z1s1 = has1 ? O_WINF + (b0 + lb1) * 20480 : OOB;
  const int wx = w ^ (j & 3);
  const int wso = (64 * panel + 32 * (wx >> 1) + 4 * (wx & 1)) * 4096 + w * 1024;
#define WSP_DMA(lt, r, ldsoff) dma16x<(r) * 16>(rw, l16, (wso ^ ((32 * ((lt) >> 1) + 4 * ((lt) & 1)) * 4096)) + (8 * ((r) >> 2) + ((r) & 3)) * 4096, lds0 + (ldsoff) + w * 16384 + (r) * 1024)
  const int wrd = w * 16384 + i16 * 1024 + (g ^ (i16 & 3)) * 16;
#define W_ADDR(s) (wrq[(s) & 3] + ((s) >> 2) * 256)
  const int zu0 = has0 ? O_Z2N + (2 * panel) * 1024 + (b0 + lb0) * 65536 : OOB, zu1 = has1 ? O_Z2N + (2 * panel) * 1024 + (b0 + lb1) * 65536 : OOB;
  const int pu = O_P + ((4 * panel) * 27 + (w < 3 ? 3 * j + w : 24 + (j >> 1))) * 1024;
  const int pu16 = w < 3 ? pu : OOB, pu8 = (w == 3 && j < 6) ? pu + (j & 1) * 8 : OOB;
  v4u WR[32];
  int wrq[4];
#pragma unroll
  for (int q = 0; q < 4; ++q) wrq[q] = wrd + ((q ^ (i16 >> 2)) * 64);
#define WR_RD(q, dst) WR[dst] = *(const v4u*)(smem + L_WL + W_ADDR(q))
  v4u R[40];
#define R_LD(q, s0, s1) R[q] = bld1(rs, l16 + (((q) >> 1) & 3) * 1024, (((q) & 1) ? (s1) : (s0)) + ((q) >> 3) * 4096)
  bool oks = false;
  if (__builtin_expect(is_conv, 1)) {
    const int d = 4 * q4 + w;
    unsigned par[6];
    par[0] = NTL((const unsigned*)(p.a_re + d * NST + 2 * lane)); par[1] = NTL((const unsigned*)(p.a_im + d * NST + 2 * lane));
    par[2] = NTL((const unsigned*)(p.b_re + d * NST + 2 * lane)); par[3] = NTL((const unsigned*)(p.b_im + d * NST + 2 * lane));
    par[4] = NTL((const unsigned*)(p.c_re + d * NST + 2 * lane)); par[5] = NTL((const unsigned*)(p.c_im + d * NST + 2 * lane));
    const float ldt = bf2f(p.log_dt[d]);
    SB();
    v4u mv[5]; unsigned cv[5];
    const int mcg = q4 >> 2, mkq = q4 & 3;
#pragma unroll
    for (int i = 0; i < 5; ++i) {
      const int row = 160 * mkq + 32 * i + (tid >> 3);
      mv[i] = __builtin_nontemporal_load((const v4u*)(p.w_mod + (size_t)row * 3840 + 64 * mcg + (mcg >= 20 ? D : 0) + 8 * (tid & 7)));
      cv[i] = p.cvec[row];
    }
    const unsigned ep = __hip_atomic_load((GAS unsigned*)(p.ws + O_EPOCH), RLX_AGENT);
    const unsigned dku = p.dskip[d];
    v2u xa[8];
    const int ro = 64 * (tid >> 5) + 32 * ((tid >> 4) & 1) + 8 * ((tid >> 1) & 3) + 4 * ((tid >> 3) & 1) + 2 * (tid & 1);
    v4u st[4][2]; u64 mg;
    const int mo = lane < 32 ? O_MODP + ((lane & 3) * 2560 + (lane >> 4) * D + 4 * q4 + ((lane >> 2) & 3)) * 8 : (lane < 36 ? O_MODG + (2 * D + 4 * q4 + (lane & 3)) * 8 : O_INIT);
    auto gather = [&]() __attribute__((always_inline)) {
      mg = __hip_atomic_load((GAS u64*)(p.ws + mo), RLX_AGENT);
#pragma unroll
      for (int i = 0; i < 4; ++i) { st[i][0] = bld1(rs, ro * 16, O_STG + i * 8192); st[i][1] = bld1(rs, ro * 16 + 16, O_STG + i * 8192); }
    };
    const rsrc_t ru = __builtin_amdgcn_make_buffer_rsrc((void*)p.u, 0, T * D * 2, 0x00020000);
    auto xld = [&](int i) __attribute__((always_inline)) { xa[i] = __builtin_amdgcn_raw_buffer_load_b64(ru, ro * (2 * D), (512 * (i >> 1) + (i & 1)) * (2 * D) + 8 * q4, 0); };
    auto hook = [&](int i) __attribute__((always_inline)) {
      SB();
      if (i == MODHOOK) {
        tag = ep + 1u;
        float acc[8];
#pragma unroll
        for (int k = 0; k < 8; ++k) acc[k] = 0.f;
#pragma unroll
        for (int q = 0; q < 5; ++q) {
          const unsigned slo = silu_bf(bf2f(cv[q])), shi = slo << 16;
#pragma unroll
          for (int k = 0; k < 4; ++k) { acc[2 * k] = dot2(mv[q][k], slo, acc[2 * k]); acc[2 * k + 1] = dot2(mv[q][k], shi, acc[2 * k + 1]); }
        }
        const float s = col_reduce((float*)(smem + L_WL), acc, tid);
        if ((tid & 3) == 0) bst1_64(rs, O_MODP + (mkq * 2560 + 64 * mcg + (tid >> 2)) * 8, v2u{f2u(s), tag});
        SB();
        code_prefetch();
      }
      if (i == 2) xld(2);
      if (i == 4) { xld(3); xld(4); }
      if (i >= 5 && i <= 7) xld(i);
      if (i == 9) gather();
      SB();
    };
    SB();
    asm volatile("s_barrier" ::: "memory");
    xld(0); xld(1);
    SB();
    u16* himg = (u16*)(smem + L_HIMG);
    if (tid < 128) *(v4u*)(himg + (tid >> 5) * HL + (tid & 31) * 8) = v4u{0, 0, 0, 0};
    const float dt = rbf(__builtin_amdgcn_exp2f(ldt * 1.4426950408889634f));
    kgen_wave(par, dt, bf2f(dku), (unsigned*)(smem + L_KSCR) + w * 16 * RS, smem + L_KIMG + w * KIMG_B, lane, hook);
#pragma unroll 1
    for (unsigned s = 0; s < SPIN; ++s) {
      unsigned bad = (lane >> 2) == 8 ? 0u : (unsigned)(mg >> 32) ^ tag;
#pragma unroll
      for (int i = 0; i < 4; ++i) bad |= (st[i][0][1] ^ tag) | (st[i][0][3] ^ tag) | (st[i][1][1] ^ tag) | (st[i][1][3] ^ tag);
      PIN(bad);
      if (__all(bad == 0u)) break;
      asm volatile("" ::: "memory");
      gather();
    }
    ORDER();
    float sa[4], c1[4];
    {
      float ms = u2f((unsigned)mg);
      ms += dppf<0xB1>(ms); ms += dppf<0x4E>(ms);
      const unsigned mr = f2u(rbf(ms));
#pragma unroll
      for (int c = 0; c < 4; ++c) {
        sa[c] = u2f(__builtin_amdgcn_readlane(mr, 4 * c));
        c1[c] = rbf(1.0f + u2f(__builtin_amdgcn_readlane(mr, 16 + 4 * c)));
      }
    }
    if (!__all((unsigned)(mg >> 32) == tag)) mg = __hip_atomic_load((GAS u64*)(p.ws + mo), RLX_AGENT);
    SB();
#pragma unroll
    for (int i = 0; i < 4; ++i) {
      const int pr = 256 * i + tid;
      const f32x2 m = {u2f(st[i][0][0]), u2f(st[i][1][0])}, r = {u2f(st[i][0][2]), u2f(st[i][1][2])};
      const f32x2 x[4] = {f32x2{bflo(xa[2 * i][0]), bflo(xa[2 * i + 1][0])}, f32x2{bfhi(xa[2 * i][0]), bfhi(xa[2 * i + 1][0])},
                          f32x2{bflo(xa[2 * i][1]), bflo(xa[2 * i + 1][1])}, f32x2{bfhi(xa[2 * i][1]), bfhi(xa[2 * i + 1][1])}};
#pragma unroll
      for (int c = 0; c < 4; ++c) {
        const f32x2 h = (x[c] - m) * r * c1[c] + sa[c];
        *(unsigned*)(himg + c * HL + 256 + 512 * i + ro) = pk_bf16(h[0], h[1]);
      }
    }
    unsigned ab[8];
#pragma unroll
    for (int a = 0; a < 8; ++a) { ab[a] = lds0 + L_KIMG + w * KIMG_B + (i16 & 1) * (KL * 2) + (2032 - (i16 & ~1) - 2 * (i16 & 1) + 8 * g) * 2 - 64 * (8 * a + 7); PIN(ab[a]); }
    unsigned bb = lds0 + L_HIMG + (w * HL + 256 - 16 + 16 * i16 + 8 * g) * 2 - 64 * 7; PIN(bb);
#pragma unroll 1
    for (unsigned s = 0; s < SPIN; ++s) {
      if (__all((unsigned)(mg >> 32) == tag)) break;
      mg = __hip_atomic_load((GAS u64*)(p.ws + mo), RLX_AGENT);
    }
    float ga[4];
#pragma unroll
    for (int c = 0; c < 4; ++c) ga[c] = u2f(__builtin_amdgcn_readlane((unsigned)mg, 32 + c));
    __syncthreads();
    f32x4 acc[8];
#pragma unroll
    for (int c = 0; c < 8; ++c) acc[c] = f32x4{0.f, 0.f, 0.f, 0.f};
    v4u O[2][16];
#define CLOAD1(buf, r, k) { if constexpr (CL[k] < 8) { const LAS unsigned* ap = (const LAS unsigned*)(size_t)ab[CL[k] & 7] + 16 * (7 - (r)); O[buf][CL[k]] = v4u{ap[0], ap[1], ap[2], ap[3]}; } \
                            else O[buf][CL[k]] = *((const LAS v4u*)(size_t)bb + 4 * (8 * (CL[k] - 8) + 7 - (r))); }
    sfor<16>(LAM { constexpr int k = ic_; CLOAD1(0, 0, k); });
    SB();
    unsigned cav = 0;
    u16* ybuf = (u16*)(smem + L_KSCR);
    f32x2 xf[4][4];
#define Y_OUT(c) *(v2u*)(ybuf + w * T + 256 * (c) + 16 * i16 + 4 * g) = v2u{pk_bf16(acc[c][0], acc[c][1]), pk_bf16(acc[c][2], acc[c][3])}
    sfor<8 * 36>(LAM {
      constexpr int r = ic_ / 36, m = ic_ % 36;
      constexpr int ca = r < 7 ? CA[m] : CA7[m], cb = r < 7 ? CB[m] : CB7[m];
      acc[ca + cb] = mfma_bf(O[r & 1][ca], O[r & 1][8 + cb], acc[ca + cb]);
      if constexpr (r < 7 && (m & 1) == 0 && m < 32) CLOAD1((r + 1) & 1, r + 1, m >> 1);
      if constexpr (cs_dma(ic_)) WSP_DMA(0, ic_ / ASP, L_WL);
      if constexpr ((r == 5 || r == 6) && (m & 3) == 3 && m < 32) {
        constexpr int q = 8 * (r - 5) + (m >> 2), i = q >> 2, c = q & 3;
        xf[i][c] = (c & 1) ? f32x2{bfhi(xa[2 * i][c >> 1]), bfhi(xa[2 * i + 1][c >> 1])} : f32x2{bflo(xa[2 * i][c >> 1]), bflo(xa[2 * i + 1][c >> 1])};
        PIN(xf[i][c]);
      }
      if constexpr (r == 7) {
        if constexpr (m == 11) Y_OUT(7);
        if constexpr (m == 18) Y_OUT(6);
        if constexpr (m == 24) Y_OUT(5);
        if constexpr (m == 29) Y_OUT(4);
        if constexpr (m == 33) Y_OUT(3);
        if constexpr (m == 34) cav = cnt_load(p, C_S, lane);
      }
      SB();
    });
    Y_OUT(2); Y_OUT(1); Y_OUT(0);
    __syncthreads();
#pragma unroll
    for (int i = 0; i < 4; ++i) {
      const int pr = 256 * i + tid;
      f32x2 o[4];
#pragma unroll
      for (int c = 0; c < 4; ++c) o[c] = xf[i][c] + unp(*(const unsigned*)(ybuf + c * T + 512 * i + ro)) * ga[c];
      bst1(rs, O_X1G + (q4 * T + 2 * pr) * 8, v4u{pk_bf16(o[0][0], o[1][0]), pk_bf16(o[2][0], o[3][0]), pk_bf16(o[0][1], o[1][1]), pk_bf16(o[2][1], o[3][1])});
    }
    SB();
    oks = __all(cav >= 7u);
    ORDER();
  } else {
    aux_role(p, rs, smem, (bi - 20) * 8 + bx, tag);
    code_prefetch();
    sfor<16>(LAM { WSP_DMA(0, ic_, L_WL); });
    DRAIN();
  }
#pragma unroll
  for (int q = 0; q < 16; ++q) WR_RD(q, q);
  int otab;
  {
    const int lf = lane & 7;
    int ab = 2 * w + lf; ab = ab >= 7 ? ab - 7 : ab;
    const int zo = (ab == 6 && j >= 6) ? OOB : O_Z2N + (b0 + ab) * 65536 + 16 * w * 1024;
    const int sl = (ab * 3 + ((w - (ab >> 1) - 1) & 3)) * 4096;
    const int ro = (sl < 65536 ? L_WL + sl : sl) + ((w ^ (ab >> 1)) << 10);
    const int n = 6 * w + lf - 1;
    const int ko = (lf == 0 ? 3 * j + min(w, 2) : (n < 3 * j ? n : n + 3)) * 1024;
    const int lt = lane & 3;
    const int po = (lane & 4) ? ((lt ^ w) * 3 + lt - 1) * 5120 : O_P + (4 * panel + (lt ^ w)) * (27 * 1024);
    const int sel = (lane >> 3) & 3;
    otab = sel == 0 ? zo : (sel == 1 ? ro : (sel == 2 ? ko : po));
    PIN(otab);
  }
  asm volatile("s_waitcnt lgkmcnt(0)" ::: "memory");
  if (is_conv) {
    DRAIN();
    __syncthreads();
    arrive(p, C_X1, tid);
  }
  if (!oks) cnt_wait<4>(p, C_S, 7u, lane);
  {
    const rsrc_t rb = __builtin_amdgcn_make_buffer_rsrc((void*)p.b_sp, 0, T * 2, 0x00020000);
    if (w < 3) dma16s(rs, l16, w == 0 ? O_U + (FH + 16 * b0) * 8 : (w == 1 ? O_U + 16 * b0 * 8 : OOB), lds0 + L_DUMMY + w * 1024);
    else dma16(rb, l16, 128 * panel, lds0 + L_DUMMY + 3072);
  }
#pragma unroll
  for (int q = 0; q < NR1; ++q) R_LD(q, z2s0, z2s1);
  SB();
  sfor<16>(LAM { WSP_DMA(1, ic_, L_WL); });
  SB();
  const int xoff = ((lane >> 5) * T + 2 * (lane & 31)) * 8;
  const int xso = O_X1G + (2 * w * T + 64 * panel) * 8;
#define X_DMA(ks) dma16s(rs, xoff, xso + (ks) * (8 * T * 8), lds0 + ((ks) * 4 + w) * 1024)
  float* sxL = (float*)(smem + L_MISC);
  float* st1 = (float*)(smem + L_MISC + 512) + w * 128;
  float* gsum = (float*)(smem + L_MISC + 2560);
  float* st2 = (float*)(smem + L_MISC + 4608) + w * 128;
  unsigned xl[4];
#pragma unroll
  for (int i = 0; i < 4; ++i) { xl[i] = lds0 + g * 1024 + (i ^ wx) * 128 + i16 * 8; PIN(xl[i]); }
  auto ldx = [&](unsigned lo, int off) __attribute__((always_inline)) {
    const v2u a = *(const LAS v2u*)(size_t)(lo + off), c = *(const LAS v2u*)(size_t)(lo + off + 512);
    return v4u{a[0], a[1], c[0], c[1]};
  };
  const int pfo = ((w < 3 || j < 6) ? pu : OOB) + l16;
  f32x4 a0[4], a1[4], gram = {0.f, 0.f, 0.f, 0.f};
#pragma unroll
  for (int i = 0; i < 4; ++i) { a0[i] = f32x4{0.f, 0.f, 0.f, 0.f}; a1[i] = a0[i]; }
  if (w == 3) cnt_wait<2>(p, C_X1, 20u, lane);
  __syncthreads();
#pragma unroll
  for (int ks = 0; ks < 8; ++ks) X_DMA(ks);
  asm volatile("s_waitcnt vmcnt(4)\n\ts_barrier" ::: "memory");
  {
    v4u A[3][4];
    sfor<8>(LAM { A[ic_ >> 2][ic_ & 3] = ldx(xl[ic_ & 3], (ic_ >> 2) * 4096); SB(); });
    sfor<20 * 9>(LAM {
      constexpr int ks = ic_ / 9, m = ic_ % 9;
      if constexpr (m == 0 && ks == 2) asm volatile("s_waitcnt vmcnt(6)\n\ts_barrier" ::: "memory");
      if constexpr (m == 0 && (ks == 6 || ks == 10)) asm volatile("s_waitcnt vmcnt(12)\n\ts_barrier" ::: "memory");
      if constexpr (m == 0 && ks == 8) { sfor<4>([&](auto i_) __attribute__((always_inline)) { xl[i_] += 40960; PIN(xl[i_]); }); }
      if constexpr (m == 0 && ks < 19) __builtin_amdgcn_s_waitcnt(0xC47F);
      if constexpr (m == 2) gram = mfma_bf(A[ks % 3][0], A[ks % 3][0], gram);
      else {
        constexpr int mm = m < 2 ? m : m - 1;
        if constexpr (mm & 1) a1[mm >> 1] = mfma_bf(A[ks % 3][mm >> 1], R[2 * ks + 1], a1[mm >> 1]);
        else a0[mm >> 1] = mfma_bf(A[ks % 3][mm >> 1], R[2 * ks], a0[mm >> 1]);
      }
      if constexpr ((m & 1) == 1 && ks < 18) A[(ks + 2) % 3][m >> 1] = ldx(xl[m >> 1], (ks + 2 - (ks >= 8 ? 10 : 0)) * 4096);
      if constexpr (ks >= 18 && m < 8) WR_RD(8 * (ks - 18) + m, 16 + 8 * (ks - 18) + m);
      if constexpr ((m == 2 || m == 4 || m == 8) && ks < 4) X_DMA(8 + 3 * ks + (m >> 2));
      if constexpr ((m == 4 || m == 8) && ks >= 4 && !(ks >= FK && ks < FK + 2)) { constexpr int n = NR1 + 2 * (ks - 4 - (ks >= FK ? 2 : 0)) + (m == 8); if constexpr (n < 40) R_LD(n, z2s0, z2s1); else R_LD(n - 40, z1s0, z1s1); }
      if constexpr ((m == 4 || m == 8) && ks >= FK && ks < FK + 2)
        bst1(rs, pfo + (2 * (ks - FK) + (m == 8)) * (27 * 1024), v4u{SENT, SENT, SENT, SENT});
      SB();
    });
  }
  if (w == 3) {
    if (i16 == 0) {
#pragma unroll
      for (int i = 0; i < 4; ++i) *(f32x4*)(sxL + (i ^ wx) * 16 + 4 * g) = a1[i];
    }
#pragma unroll
    for (int i = 0; i < 4; ++i) a1[i] = f32x4{0.f, 0.f, 0.f, 0.f};
  }
  if ((i16 >> 2) == g) sxL[64 + wx * 16 + i16] = (i16 & 3) == 0 ? gram[0] : (i16 & 3) == 1 ? gram[1] : (i16 & 3) == 2 ? gram[2] : gram[3];
  __syncthreads();
  {
    const float m = sxL[lane] * (1.0f / D);
    const float var = fmaxf(sxL[64 + lane] * (1.0f / D) - m * m, 0.f);
    const float r = __builtin_amdgcn_rsqf(var + 1e-5f);
    st1[lane] = r; st1[64 + lane] = r * m;
  }
  v4u A1[3][4];
  {
    const f32x2* cU2 = (const f32x2*)(smem + L_DUMMY);
    f32x2 u0 = cU2[16 * lb0 + i16], u1 = cU2[16 * lb1 + i16];
    if (!has0) u0 = f32x2{0.f, 0.f};
    if (!has1) u1 = f32x2{0.f, 0.f};
    float s1[16], s2[16];
    f32x4 rr[4], rm[4];
#pragma unroll
    for (int i = 0; i < 4; ++i) { rr[i] = *(const f32x4*)(st1 + (i ^ wx) * 16 + 4 * g); rm[i] = *(const f32x4*)(st1 + 64 + (i ^ wx) * 16 + 4 * g); }
    sfor<8>(LAM {
      constexpr int i = ic_ >> 1, h = ic_ & 1;
      { constexpr int n0 = NR1 - 12, nn = 40 - n0;
#pragma unroll
        for (int q = n0 + nn * ic_ / 8; q < n0 + nn * (ic_ + 1) / 8; ++q) R_LD(q, z1s0, z1s1); }
      const f32x2 r2 = {rr[i][2 * h], rr[i][2 * h + 1]}, m2 = {rm[i][2 * h], rm[i][2 * h + 1]};
      const f32x2 v0 = gelu2(f32x2{a0[i][2 * h], a0[i][2 * h + 1]} * r2 + (u0[1] - m2 * u0[0]));
      const f32x2 v1 = gelu2(f32x2{a1[i][2 * h], a1[i][2 * h + 1]} * r2 + (u1[1] - m2 * u1[0]));
      a0[i][2 * h] = v0[0]; a0[i][2 * h + 1] = v0[1]; a1[i][2 * h] = v1[0]; a1[i][2 * h + 1] = v1[1];
      f32x2 q1 = v0 + v1, q2 = v0 * v0 + v1 * v1;
      PIN(q1); PIN(q2);
      s1[4 * i + 2 * h] = q1[0]; s1[4 * i + 2 * h + 1] = q1[1]; s2[4 * i + 2 * h] = q2[0]; s2[4 * i + 2 * h + 1] = q2[1];
      SB();
    });
    sfor<4>(LAM { xl[ic_] -= 40960; PIN(xl[ic_]); A1[0][ic_] = ldx(xl[ic_], 0); SB(); A1[1][ic_] = ldx(xl[ic_], 4096); SB(); });
    const float t1 = bfly16(s1, i16), t2 = bfly16(s2, i16);
    const int tp = ((i16 >> 2) ^ wx) * 16 + 4 * g + (i16 & 3);
    asm volatile("s_waitcnt vmcnt(%0)" :: "n"(52 - NR1) : "memory");
    bst1(rs, O_ZST + (((panel * 4 + w) * 8 + j) * 64 + tp) * 16, v4u{f2u(t1), tag, f2u(t2), tag});
  }
  SB();
  f32x4 c0[4], c1[4];
#pragma unroll
  for (int i = 0; i < 4; ++i) { c0[i] = f32x4{0.f, 0.f, 0.f, 0.f}; c1[i] = c0[i]; }
#define Z1M(ks, m) { if constexpr ((m) == 0 && (ks) >= 2 && (ks) < 19 && (ks) != KA && (ks) != KA + 1) __builtin_amdgcn_s_waitcnt(0xC47F); \
    if constexpr ((m) & 1) c1[(m) >> 1] = mfma_bf(R[2 * (ks) + 1], A1[(ks) % 3][(m) >> 1], c1[(m) >> 1]); else c0[(m) >> 1] = mfma_bf(R[2 * (ks)], A1[(ks) % 3][(m) >> 1], c0[(m) >> 1]); \
    if constexpr (((m) & 1) == 1 && (ks) < 18) A1[((ks) + 2) % 3][(m) >> 1] = ldx(xl[(m) >> 1], ((ks) + 2 - ((ks) >= KA ? KA : 0)) * 4096); }
  constexpr int KA = KAV;
  v4u zs[8];
  unsigned cfv = 0;
  const int zsoff = O_ZST + ((panel * 4 + w) * 8 * 64) * 16;
  sfor<KA * 8>(LAM {
    constexpr int ks = ic_ / 8, m = ic_ % 8;
    Z1M(ks, m);
    if constexpr ((m == 2 || m == 6) && ks < 8) { constexpr int q = 2 * ks + (m == 6); WSP_DMA(2, q, L_WL); }
    if constexpr (m == 4 && ks == 7) cfv = cnt_load(p, C_F, lane);
    if constexpr ((m & 1) == 0 && ks >= 8 && ks < 10) { constexpr int jj = 4 * (ks - 8) + (m >> 1); zs[jj] = bld1(rs, l16 + (jj & 3) * 1024, zsoff + (jj >> 2) * 4096); }
    SB();
  });
  v2u x1v[5];
#pragma unroll
  for (int db = 0; db < 5; ++db) {
    const int d = 80 * j + 16 * db + 4 * g;
    x1v[db] = *(const v2u*)(smem + (d >> 2) * 512 + (16 * w + i16) * 8);
  }
  {
#pragma unroll 1
    for (unsigned s = 0; s < SPIN; ++s) {
      unsigned bad = 0;
#pragma unroll
      for (int jj = 0; jj < 8; ++jj) bad |= (zs[jj][1] ^ tag) | (zs[jj][3] ^ tag);
      PIN(bad);
      if (__all(bad == 0u)) break;
      asm volatile("" ::: "memory");
#pragma unroll
      for (int jj = 0; jj < 8; ++jj) zs[jj] = bld1(rs, l16, zsoff + jj * 1024);
    }
    float a = 0.f, b = 0.f;
#pragma unroll
    for (int jj = 0; jj < 8; ++jj) { a += u2f(zs[jj][0]); b += u2f(zs[jj][2]); }
    *(f32x2*)(gsum + (w * 64 + lane) * 2) = f32x2{a, b};
  }
  if (!__all(cfv >= 5u)) cnt_wait<1>(p, C_F, 5u, lane);
  ORDER();
  __syncthreads();
  {
    const f32x2 s = (*(const f32x2*)(gsum + lane * 2) + *(const f32x2*)(gsum + 128 + lane * 2)) + (*(const f32x2*)(gsum + 256 + lane * 2) + *(const f32x2*)(gsum + 384 + lane * 2));
    const float m = s[0] * (1.0f / FH);
    const float var = fmaxf(s[1] * (1.0f / FH) - m * m, 0.f);
    const float r = __builtin_amdgcn_rsqf(var + 1e-5f);
    st2[lane] = r; st2[64 + lane] = r * m;
  }
  {
    unsigned o0[4][2], o1[4][2];
#pragma unroll
    for (int i = 0; i < 4; ++i) {
      const f32x4 rr = *(const f32x4*)(st2 + (i ^ wx) * 16 + 4 * g), rm = *(const f32x4*)(st2 + 64 + (i ^ wx) * 16 + 4 * g);
      const f32x4 n0 = a0[i] * rr - rm, n1 = a1[i] * rr - rm;
      o0[i][0] = pk_bf16(n0[0], n0[1]); o0[i][1] = pk_bf16(n0[2], n0[3]);
      o1[i][0] = pk_bf16(n1[0], n1[1]); o1[i][1] = pk_bf16(n1[2], n1[3]);
    }
    const bool sw = wx & 1;
    const int ph = (wx >> 1) * 1024;
#pragma unroll
    for (int pp = 0; pp < 2; ++pp) {
      const int e = 2 * pp, o = 2 * pp + 1;
      bst1(rs, zu0 + ((pp * 1024) ^ ph) + l16, sw ? v4u{o0[o][0], o0[o][1], o0[e][0], o0[e][1]} : v4u{o0[e][0], o0[e][1], o0[o][0], o0[o][1]});
      bst1(rs, zu1 + ((pp * 1024) ^ ph) + l16, sw ? v4u{o1[o][0], o1[o][1], o1[e][0], o1[e][1]} : v4u{o1[e][0], o1[e][1], o1[o][0], o1[o][1]});
    }
  }
  sfor<4>(LAM { xl[ic_] += KA * 4096; PIN(xl[ic_]); });
  SB();
  sfor<(20 - KA) * 8>(LAM {
    constexpr int ks = KA + ic_ / 8, m = ic_ % 8;
    Z1M(ks, m);
    SB();
  });
  DRAIN();
  __syncthreads();
  GAS unsigned* flags = (GAS unsigned*)(p.ws + O_FLAG);
  if (tid < 4) __hip_atomic_fetch_or(flags + ((j * 4 + (panel >> 3)) * 4 + tid) * 64, 1u << (panel & 7), RLX_AGENT);
  if (w == 2) dma16s(rs, l16, O_MODG + (5 * D + 80 * j) * 8, lds0 + L_DUMMY + 2048);
  int zof[7], rof[7], kof[7];
#pragma unroll
  for (int q = 0; q < 7; ++q) { zof[q] = __builtin_amdgcn_readlane(otab, q); rof[q] = __builtin_amdgcn_readlane(otab, 8 + q); kof[q] = __builtin_amdgcn_readlane(otab, 16 + q); }
  constexpr int ZD = ZDV;
  v4u Z[16][7];
#define Z_LD(i, f) Z[i][f] = bld1(rs, l16 + ((i) & 3) * 1024, zof[f] + ((i) >> 2) * 4096)
  unsigned zmask = 0u, zfv = 0u;
  GAS unsigned* zfp = flags + ((j * 4 + w) * 4 + (panel >> 3)) * 64;
  auto zneed = [&](unsigned bits) __attribute__((always_inline)) {
    if (__builtin_expect((zmask & bits) != bits, 0)) {
#pragma unroll 1
      for (unsigned s = 0; s < SPIN; ++s) {
        zmask = __builtin_amdgcn_readfirstlane(__hip_atomic_load(zfp, RLX_AGENT));
        if ((zmask & bits) == bits) break;
        __builtin_amdgcn_s_sleep(1);
      }
    }
    ORDER();
  };
  unsigned z1p[2][4][2];
  {
    const v4u* cU1 = (const v4u*)(smem + L_DUMMY + 1024);
    const v4u ua0 = cU1[8 * lb0 + 2 * g], ua1 = cU1[8 * lb0 + 2 * g + 1], ub0 = cU1[8 * lb1 + 2 * g], ub1 = cU1[8 * lb1 + 2 * g + 1];
    float sr[4], sm[4];
#pragma unroll
    for (int i = 0; i < 4; ++i) { sr[i] = st1[16 * (i ^ wx) + i16]; sm[i] = st1[64 + 16 * (i ^ wx) + i16]; }
    sfor<8>(LAM {
      constexpr int i = ic_ >> 1, h = ic_ & 1;
      if constexpr (ic_ < 4) { WSP_DMA(3, 4 * ic_, 0); WSP_DMA(3, 4 * ic_ + 1, 0); WSP_DMA(3, 4 * ic_ + 2, 0); WSP_DMA(3, 4 * ic_ + 3, 0); }
      if constexpr (ic_ == ZPOLL - 2) { zfv = __hip_atomic_load(zfp, RLX_AGENT); SB(); }
      if constexpr (ic_ == ZPOLL) { zmask = __builtin_amdgcn_readfirstlane(zfv); zneed(1u); }
      if constexpr (ic_ >= ZPOLL) {
        constexpr int n = 8 - ZPOLL, k = ic_ - ZPOLL;
#pragma unroll
        for (int q = 14 * k / n; q < 14 * (k + 1) / n; ++q) Z_LD(q / 7, q % 7);
        SB();
      }
      const v4u qa = h ? ua1 : ua0, qb = h ? ub1 : ub0;
      const f32x2 v0 = gelu2(f32x2{c0[i][2 * h], c0[i][2 * h + 1]} * sr[i] + (f32x2{u2f(qa[1]), u2f(qa[3])} - f32x2{u2f(qa[0]), u2f(qa[2])} * sm[i]));
      const f32x2 v1 = gelu2(f32x2{c1[i][2 * h], c1[i][2 * h + 1]} * sr[i] + (f32x2{u2f(qb[1]), u2f(qb[3])} - f32x2{u2f(qb[0]), u2f(qb[2])} * sm[i]));
      z1p[0][i][h] = pk_bf16(v0[0], v0[1]); z1p[1][i][h] = pk_bf16(v1[0], v1[1]);
      PIN(z1p[0][i][h]); PIN(z1p[1][i][h]);
    });
  }
  SB();
  f32x4 acc[7][4];
  const int woso = O_WOUTF + (5 * j) * 27 * 1024;
  v4u WO[35];
  v4u WL[4][2];
#define WO_LD(q) WO[q] = bld1(rs, l16, woso + ((q) % 5) * (27 * 1024) + kof[(q) / 5])
#define WL_RD(i, h) WL[(i) & 3][h] = *(const v4u*)(smem + ((h) ? 0 : L_WL) + W_ADDR(i))
  WL_RD(0, 0); WL_RD(0, 1);
  sfor<13 * 28>(LAM {
    constexpr int i = ic_ / 28, f = (ic_ % 28) / 4, t = ic_ % 4;
    constexpr int lo = zs_lo(i, ZD), hi = zs_lo(i + 1, ZD);
    if constexpr (f == 0 && t == 0 && hi > lo && ((lo & 1) == 0 || hi - lo > 1)) zneed((1u << ((hi - 1) >> 1)) | (1u << (lo >> 1)));
    if constexpr (t < 2) mfma_pin<i == 0, true>(acc[f][t], Z[i][f], WR[t * 16 + i]);
    else mfma_pin<i == 0, false>(acc[f][t], Z[i][f], WL[i & 3][t - 2]);
    if constexpr (hi - lo == 2 && (t == 0 || t == 2)) { constexpr int q = 2 * f + (t >> 1); Z_LD(lo + q / 7, q % 7); }
    if constexpr (hi - lo == 1 && t == 0) Z_LD(lo, f);
    if constexpr (t == 1 && f < 2 && i < 12) WL_RD(i + 1, f);
    if constexpr (t == 1 && f < 6 && i == 12) WL_RD(13 + (f >> 1), f & 1);
    SB();
  });
  asm volatile("s_nop 7\n\ts_waitcnt lgkmcnt(0)\n\ts_barrier" ::: "memory");
  sfor<7 * 12>(LAM {
    constexpr int fo = ic_ / 12, f = (fo + 2) % 7, i = 13 + (ic_ % 12) / 4, t = ic_ % 4, m = ic_ % 12;
    if constexpr (t < 2) acc[f][t] = mfma_bf(Z[i][f], WR[t * 16 + i], acc[f][t]);
    else acc[f][t] = mfma_bf(Z[i][f], WL[i & 3][t - 2], acc[f][t]);
    if constexpr (m % 4 == 0 && 3 * fo + m / 4 < NWO) WO_LD(3 * fo + m / 4);
    if constexpr (fo >= 1 && fo <= 5 && m % 3 == 2) { constexpr int pf = fo + 1, pt = m / 3; *(f32x4*)(smem + ((rof[pf] + l16) ^ (pt << 10))) = acc[pf][pt]; }
    SB();
  });
  if (w == 3) {
#pragma unroll
    for (int t = 0; t < 4; ++t) *(f32x4*)(smem + ((rof[1] + l16) ^ (t << 10))) = acc[1][t];
  }
  f32x4 o0[4], o1[4];
#pragma unroll
  for (int t = 0; t < 4; ++t) {
    const float bias = bf2f(((const u16*)(smem + L_DUMMY + 3072))[8 * (i16 >> 2) + (i16 & 3) + 32 * ((t ^ wx) >> 1) + 4 * ((t ^ wx) & 1)]);
    o0[t] = acc[0][t] + bias; o1[t] = acc[1][t] + bias; PIN(o0[t]); PIN(o1[t]);
  }
  __syncthreads();
  v4u PO[4];
  {
    f32x4 pa[4][3], pb[4][3];
#pragma unroll
    for (int s = 0; s < 3; ++s) {
      const int s0 = (2 * w * 3 + s) * 4096, s1 = ((w < 3 ? 2 * w + 1 : 6) * 3 + s) * 4096;
      const unsigned char* r0 = smem + (s0 < 65536 ? L_WL + s0 : s0) + l16;
      const unsigned char* r1 = smem + (s1 < 65536 ? L_WL + s1 : s1) + l16;
#pragma unroll
      for (int t = 0; t < 4; ++t) { pa[t][s] = *(const f32x4*)(r0 + t * 1024); pb[t][s] = *(const f32x4*)(r1 + t * 1024); }
    }
    SB();
#pragma unroll
    for (int t = 0; t < 4; ++t) {
      const f32x4 x0 = (o0[t] + pa[t][0]) + (pa[t][1] + pa[t][2]), x1 = (o1[t] + pb[t][0]) + (pb[t][1] + pb[t][2]);
      const f32x2 qa = unp(z1p[0][t][0]) * f32x2{x0[0], x0[1]}, qb = unp(z1p[0][t][1]) * f32x2{x0[2], x0[3]};
      const f32x2 qc = unp(z1p[1][t][0]) * f32x2{x1[0], x1[1]}, qd = unp(z1p[1][t][1]) * f32x2{x1[2], x1[3]};
      PO[t] = v4u{min(pk_bf16(qa[0], qa[1]), SENT - 1u), min(pk_bf16(qb[0], qb[1]), SENT - 1u), min(pk_bf16(qc[0], qc[1]), SENT - 1u), min(pk_bf16(qd[0], qd[1]), SENT - 1u)};
      bst1(rs, pu16 + (t ^ wx) * (27 * 1024) + l16, PO[t]);
      bst1_64(rs, pu8 + (t ^ wx) * (27 * 1024) + l16, v2u{PO[t][0], PO[t][1]});
    }
  }
  SB();
  if (w == 3) {
#pragma unroll
    for (int t = 0; t < 4; ++t) PO[t] = v4u{0u, 0u, 0u, 0u};
  }
  if (j & 1) { const v4u a = PO[0], c = PO[2]; PO[0] = PO[1]; PO[1] = a; PO[2] = PO[3]; PO[3] = c; }
  if (j & 2) { const v4u a = PO[0], c = PO[1]; PO[0] = PO[2]; PO[2] = a; PO[1] = PO[3]; PO[3] = c; }
  f32x4 e0[5][4], e[5][4];
#pragma unroll
  for (int t = 0; t < 4; ++t)
#pragma unroll
    for (int d = 0; d < 5; ++d) e0[d][t] = mfma_bf(WO[d], PO[t], f32x4{0.f, 0.f, 0.f, 0.f});
  const int trow = 64 * panel + 8 * (i16 >> 2) + (i16 & 3) + 32 * (w >> 1) + 4 * (w & 1);
  int pof[4], eof[4];
#pragma unroll
  for (int q = 0; q < 4; ++q) { pof[q] = __builtin_amdgcn_readlane(otab, 24 + q); eof[q] = __builtin_amdgcn_readlane(otab, 28 + q); }
  const int ppo = lane < 48 ? O_P + (4 * panel + ((lane >> 1) & 3)) * (27 * 1024) + __builtin_amdgcn_ds_bpermute((17 + (lane >> 3)) * 4, otab) + 1012 + (lane & 1) * 8 : OOB;
  v4u gb0[5], gb1[5];
#pragma unroll
  for (int d = 0; d < 5; ++d) { gb0[d] = *(const v4u*)(smem + L_DUMMY + 2048 + (16 * d + 4 * g) * 8); gb1[d] = *(const v4u*)(smem + L_DUMMY + 2048 + (16 * d + 4 * g) * 8 + 16); }
  SB();
#pragma unroll
  for (int q = NWO; q < 35; ++q) WO_LD(q);
  SB();
  v4u P[24];
#define P_LD(q) P[q] = bld1(rs, l16, pof[((q) / 6 + 1) & 3] + kof[1 + (q) % 6])
  constexpr int PDN = PDNV;
#pragma unroll 1
  for (unsigned tries = 0; tries < 4u; ++tries) {
#pragma unroll 1
    for (unsigned s = 0; s < SPIN; ++s) {
      asm volatile("" ::: "memory");
      const unsigned m = __builtin_amdgcn_raw_buffer_load_b32(rs, ppo, 0, AUX_SC1);
#pragma unroll
      for (int q = 0; q < PDN; ++q) P_LD(q);
      if (!__any(m == SENT)) break;
    }
    asm volatile("" ::: "memory");
    unsigned pm = 0u;
    sfor<24 * 5>(LAM {
      constexpr int q = ic_ / 5, d = ic_ % 5, lo = q / 6, lt = (lo + 1) & 3, kk = 1 + q % 6;
      e[d][lt] = mfma_bf(WO[5 * kk + d], P[q], kk == 1 ? e0[d][lt] : e[d][lt]);
      if constexpr (d == 0 && q + PDN < 24) P_LD(q + PDN);
      if constexpr (d == 1) { pm = mx4(pm, P[q]); PIN(pm); }
      if constexpr (lo >= 1 && d == 3 && q % 6 < 5) *(f32x4*)(smem + eof[lo] + (q % 6) * 1024 + l16) = e[q % 6][lo];
      SB();
    });
    if (__builtin_expect(!__any(pm == SENT), 1)) break;
  }
  __syncthreads();
  u16* orow = p.out + (size_t)trow * D + 80 * j + 4 * g;
  f32x4 ep[5][3];
#pragma unroll
  for (int d = 0; d < 5; ++d)
#pragma unroll
    for (int s = 0; s < 3; ++s) ep[d][s] = *(const f32x4*)(smem + ((w * 3 + s) * 5 + d) * 1024 + l16);
  SB();
#pragma unroll
  for (int d = 0; d < 5; ++d) {
    const f32x4 eo = (e[d][0] + ep[d][0]) + (ep[d][1] + ep[d][2]);
    const float r0 = bflo(x1v[d][0]) + u2f(gb0[d][0]) * eo[0], r1 = bfhi(x1v[d][0]) + u2f(gb0[d][2]) * eo[1];
    const float r2 = bflo(x1v[d][1]) + u2f(gb1[d][0]) * eo[2], r3 = bfhi(x1v[d][1]) + u2f(gb1[d][2]) * eo[3];
    *(v2u*)(orow + 16 * d) = v2u{pk_bf16(r0, r1), pk_bf16(r2, r3)};
  }
  if (blockIdx.x == 0 && tid == 0) __hip_atomic_store((GAS unsigned*)(p.ws + O_EPOCH), tag, RLX_AGENT);
  asm volatile(".Lkf_end:");
}

extern "C" void kernel_launch(void* const* d_in, const int* in_sizes, int n_in, void* d_out, int out_size, void* d_ws,
                              size_t ws_size, hipStream_t stream) {
  Params p;
  p.u = (const u16*)d_in[0]; p.a_re = (const u16*)d_in[1]; p.a_im = (const u16*)d_in[2];
  p.b_re = (const u16*)d_in[3]; p.b_im = (const u16*)d_in[4]; p.c_re = (const u16*)d_in[5]; p.c_im = (const u16*)d_in[6];
  p.log_dt = (const u16*)d_in[7]; p.dskip = (const u16*)d_in[8];
  p.w_in = (const u16*)d_in[10]; p.w_sp = (const u16*)d_in[11]; p.b_sp = (const u16*)d_in[12]; p.w_out = (const u16*)d_in[13];
  p.cvec = (const u16*)d_in[14]; p.w_mod = (const u16*)d_in[15];
  p.out = (u16*)d_out; p.ws = (unsigned char*)d_ws;
  (void)hipFuncSetAttribute((const void*)k_fused, hipFuncAttributeMaxDynamicSharedMemorySize, L_TOTAL);
  k_fused<<<256, 256, L_TOTAL, stream>>>(p);
}
```

```cpp
#include <hip/hip_runtime.h>
#include <hip/hip_bf16.h>
#include <stdint.h>
#include <utility>

typedef unsigned v4u __attribute__((ext_vector_type(4)));
typedef unsigned v2u __attribute__((ext_vector_type(2)));
typedef float f32x4 __attribute__((ext_vector_type(4)));
typedef float f32x2 __attribute__((ext_vector_type(2)));
typedef short bf16x8 __attribute__((ext_vector_type(8)));
typedef _Float16 f16x8 __attribute__((ext_vector_type(8)));
typedef _Float16 f16x2 __attribute__((ext_vector_type(2)));
typedef __bf16 bf16x2_t __attribute__((ext_vector_type(2)));
typedef unsigned short u16;
typedef unsigned long long u64;
#define GAS __attribute__((address_space(1)))
#define LAS __attribute__((address_space(3)))
#define RLX_AGENT __ATOMIC_RELAXED, __HIP_MEMORY_SCOPE_AGENT
#define DEVI __device__ __forceinline__
#define SB() __builtin_amdgcn_sched_barrier(0)
template <int... I, class F> __device__ __forceinline__ void sfor_impl(std::integer_sequence<int, I...>, F&& f) { (f(std::integral_constant<int, I>{}), ...); }
template <int N, class F> __device__ __forceinline__ void sfor(F&& f) { sfor_impl(std::make_integer_sequence<int, N>{}, f); }
#define LAM [&](auto ic_) __attribute__((always_inline))
#define NTL(p) __builtin_nontemporal_load(p)
#define DRAIN() asm volatile("s_waitcnt vmcnt(0)" ::: "memory")

constexpr int T = 2048, D = 640, NST = 128, FH = 864, F2 = 1728;
constexpr int KL = 2080, KIMG_B = 2 * KL * 2, HL = 2304;
constexpr unsigned SPIN = 1u << 15;
constexpr int AUX_SC1 = 16, AUX_NT = 2;
constexpr int O_EPOCH = 0;
constexpr int O_INIT = 256;
constexpr int O_CNT = 4096;
constexpr int O_FLAG = O_CNT + 3 * 64 * 256;
constexpr int O_MODG = O_FLAG + 256 * 256;
constexpr int O_MODP = O_MODG + 3840 * 8 + 2048;
constexpr int O_STG = O_MODP + 4 * 2560 * 8;
constexpr int O_U = O_STG + 2048 * 16;
constexpr int O_ZST = O_U + 16384;
constexpr int O_X1G = O_ZST + 32 * 32 * 64 * 16;
constexpr int O_WINF = O_X1G + T * D * 2;
constexpr int O_WOUTF = O_WINF + 109 * 20 * 1024;
constexpr int O_Z2N = O_WOUTF + 40 * 27 * 1024;
constexpr int O_P = O_Z2N + 54 * 64 * 1024;
constexpr int O_END = O_P + 128 * 27 * 1024;
constexpr int C_X1 = 0, C_S = 1, C_F = 2;
constexpr unsigned SENT = 0xffffffffu;

DEVI float u2f(unsigned u) { return __builtin_bit_cast(float, u); }
DEVI unsigned f2u(float f) { return __builtin_bit_cast(unsigned, f); }
DEVI float bf2f(unsigned b) { return u2f(b << 16); }
DEVI float bflo(unsigned u) { return u2f(u << 16); }
DEVI float bfhi(unsigned u) { return u2f(u & 0xffff0000u); }
DEVI unsigned pk_bf16(float a, float b) {
  f32x2 v = {a, b};
  bf16x2_t r = __builtin_convertvector(v, bf16x2_t);
  return __builtin_bit_cast(unsigned, r);
}
DEVI float rbf(float a) { return bflo(pk_bf16(a, 0.f)); }
DEVI unsigned pk_f16(float a, float b) {
  f32x2 v = {a, b};
  f16x2 r = __builtin_convertvector(v, f16x2);
  return __builtin_bit_cast(unsigned, r);
}
DEVI f32x4 mfma_bf(v4u a, v4u b, f32x4 c) {
  return __builtin_amdgcn_mfma_f32_16x16x32_bf16(__builtin_bit_cast(bf16x8, a), __builtin_bit_cast(bf16x8, b), c, 0, 0, 0);
}
DEVI f32x4 mfma_h(v4u a, v4u b, f32x4 c) {
  return __builtin_amdgcn_mfma_f32_16x16x32_f16(__builtin_bit_cast(f16x8, a), __builtin_bit_cast(f16x8, b), c, 0, 0, 0);
}
template <bool FIRST, bool BA> DEVI void mfma_pin(f32x4& acc, const v4u& a, const v4u& bb) {
  if constexpr (FIRST) {
    if constexpr (BA) asm volatile("v_mfma_f32_16x16x32_bf16 %0, %1, %2, 0" : "=a"(acc) : "v"(a), "a"(bb));
    else asm volatile("v_mfma_f32_16x16x32_bf16 %0, %1, %2, 0" : "=a"(acc) : "v"(a), "v"(bb));
  } else {
    if constexpr (BA) asm volatile("v_mfma_f32_16x16x32_bf16 %0, %1, %2, %0" : "+a"(acc) : "v"(a), "a"(bb));
    else asm volatile("v_mfma_f32_16x16x32_bf16 %0, %1, %2, %0" : "+a"(acc) : "v"(a), "v"(bb));
  }
}
typedef __amdgpu_buffer_rsrc_t rsrc_t;
DEVI v4u bld1(rsrc_t rs, int voff, int soff = 0) { return __builtin_amdgcn_raw_buffer_load_b128(rs, voff, soff, AUX_SC1); }
DEVI void bst1(rsrc_t rs, int voff, v4u v) { __builtin_amdgcn_raw_buffer_store_b128(v, rs, voff, 0, AUX_SC1); }
DEVI void bst1_64(rsrc_t rs, int voff, v2u v) { __builtin_amdgcn_raw_buffer_store_b64(v, rs, voff, 0, AUX_SC1); }
template <int CTRL> DEVI float dppf(float x) { return u2f((unsigned)__builtin_amdgcn_update_dpp(0, (int)f2u(x), CTRL, 0xf, 0xf, true)); }
DEVI float dot2(unsigned a, unsigned b, float c) { return __builtin_amdgcn_fdot2_f32_bf16(__builtin_bit_cast(bf16x2_t, a), __builtin_bit_cast(bf16x2_t, b), c, false); }
DEVI unsigned mx4(unsigned m, v4u v) { return max(max(max(m, v[0]), v[1]), max(v[2], v[3])); }
#define PIN(x) asm volatile("" : "+v"(x))
#define ORDER() __builtin_amdgcn_fence(__ATOMIC_ACQUIRE, "wavefront")

struct Params {
  const u16 *u, *a_re, *a_im, *b_re, *b_im, *c_re, *c_im, *log_dt, *dskip, *w_in, *w_sp, *b_sp, *w_out, *cvec, *w_mod;
  u16* out;
  unsigned char* ws;
};

DEVI int blk0(int j) { return j < 6 ? 7 * j : 42 + 6 * (j - 6); }
DEVI int pair_blk(int ks3, int h) {
  if (ks3 < 24) { const int j = ks3 / 3, w = ks3 - 3 * j; return blk0(j) + 2 * w + h; }
  return 7 * (2 * (ks3 - 24) + h) + 6;
}
DEVI void arrive(const Params& p, int c, int tid) {
  if (tid < 8) __hip_atomic_fetch_add((GAS unsigned*)(p.ws + O_CNT) + ((c * 8 + tid) * 8 + (blockIdx.x & 7)) * 64, 1u, RLX_AGENT);
}
DEVI unsigned cnt_load(const Params& p, int c, int lane) {
  return __hip_atomic_load((GAS unsigned*)(p.ws + O_CNT) + ((c * 8 + ((blockIdx.x >> 3) & 7)) * 8 + (lane & 7)) * 64, RLX_AGENT);
}
template <int SLP> DEVI void cnt_wait(const Params& p, int c, unsigned want, int lane) {
#pragma unroll 1
  for (unsigned s = 0; s < SPIN; ++s) {
    if (__all(cnt_load(p, c, lane) >= want)) break;
    __builtin_amdgcn_s_sleep(SLP);
  }
  ORDER();
}
struct C2 { f32x2 re, im; };
DEVI C2 cmul2(C2 a, C2 b) { return C2{a.re * b.re - a.im * b.im, a.re * b.im + a.im * b.re}; }
DEVI C2 cexp2(f32x2 are2, f32x2 arev, float l) {
  const f32x2 e = are2 * l, q = arev * l;
  const f32x2 m = {__builtin_amdgcn_exp2f(e[0]), __builtin_amdgcn_exp2f(e[1])};
  const float p0 = __builtin_amdgcn_fractf(q[0]), p1 = __builtin_amdgcn_fractf(q[1]);
  return C2{m * f32x2{__builtin_amdgcn_cosf(p0), __builtin_amdgcn_cosf(p1)}, m * f32x2{__builtin_amdgcn_sinf(p0), __builtin_amdgcn_sinf(p1)}};
}
constexpr int RS = 132;
DEVI void put_row(unsigned* buf, int row, int lane, C2 v) { *(v2u*)(buf + row * RS + 2 * lane) = v2u{pk_f16(v.re[0], v.im[0]), pk_f16(v.re[1], v.im[1])}; }
DEVI f32x2 unp(unsigned u) { return f32x2{bflo(u), bfhi(u)}; }

template <class HK> DEVI void kgen_wave(unsigned par[6], float dt, float dsk, unsigned* buf0, unsigned char* img, int lane, HK&& hook) {
  unsigned* buf1 = (unsigned*)img;
  u16* tab = (u16*)buf0;
  const int c = lane & 15, g = lane >> 4;
  const f32x2 are2 = unp(par[0]) * (dt * 1.4426950408889634f), arev = unp(par[1]) * (dt * 0.15915494309189535f), nrev = -arev;
  C2 W = cmul2(C2{unp(par[4]), unp(par[5])}, C2{unp(par[2]) * dt, unp(par[3]) * dt});
  float mx = fmaxf(fmaxf(fabsf(W.re[0]), fabsf(W.re[1])), fmaxf(fabsf(W.im[0]), fabsf(W.im[1])));
  unsigned mu = f2u(mx);
  mu = max(mu, f2u(dppf<0x128>(u2f(mu)))); mu = max(mu, f2u(dppf<0x124>(u2f(mu)))); mu = max(mu, f2u(dppf<0x122>(u2f(mu)))); mu = max(mu, f2u(dppf<0x121>(u2f(mu))));
  const unsigned m0 = __builtin_amdgcn_readlane(mu, 0), m1 = __builtin_amdgcn_readlane(mu, 16), m2 = __builtin_amdgcn_readlane(mu, 32), m3 = __builtin_amdgcn_readlane(mu, 48);
  unsigned eb = (max(max(m0, m1), max(m2, m3)) >> 23) & 0xffu;
  eb = eb < 2u ? 2u : (eb > 250u ? 250u : eb);
  const float sc = u2f((eb + 1u) << 23), isc = u2f((253u - eb) << 23);
  W.re *= isc; W.im *= isc;
  {
    const C2 e8 = cexp2(are2, arev, 8.f);
    C2 W8 = cmul2(W, cexp2(are2, arev, 64.f));
#pragma unroll
    for (int b = 0; b < 8; ++b) {
      put_row(buf0, b, lane, W); put_row(buf0, 8 + b, lane, W8);
      if (b < 7) { W = cmul2(W, e8); W8 = cmul2(W8, e8); }
      if (b == 0) hook(2);
      if (b == 4) hook(3);
    }
  }
  v4u A[8];
#pragma unroll
  for (int ks = 0; ks < 8; ++ks) A[ks] = *(const v4u*)(buf0 + c * RS + 16 * ks + 4 * g);
  C2 er[8];
  er[0] = C2{f32x2{1.f, 1.f}, f32x2{0.f, 0.f}};
  er[1] = cexp2(are2, nrev, 1.f);
  er[2] = cmul2(er[1], er[1]); er[3] = cmul2(er[2], er[1]); er[4] = cmul2(er[2], er[2]);
  er[5] = cmul2(er[4], er[1]); er[6] = cmul2(er[3], er[3]); er[7] = cmul2(er[4], er[3]);
  hook(4);
  const C2 e128 = cexp2(are2, nrev, 128.f);
#pragma unroll
  for (int r = 0; r < 8; ++r) { put_row(buf0, r, lane, er[r]); put_row(buf0, 8 + r, lane, r ? cmul2(e128, er[r]) : e128); }
  f32x4 acc0 = {0.f, 0.f, 0.f, 0.f}, acc1 = acc0, acc2 = acc0;
#pragma unroll
  for (int ks = 0; ks < 8; ++ks) acc0 = mfma_h(A[ks], *(const v4u*)(buf0 + c * RS + 16 * ks + 4 * g), acc0);
  hook(5);
  C2 e = cmul2(e128, e128);
  C2 f = cexp2(are2, nrev, 1024.f);
#pragma unroll
  for (int a = 0; a < 2; ++a) {
#pragma unroll
    for (int r = 0; r < 4; ++r) put_row(buf0, 4 * a + r, lane, r ? cmul2(e, er[2 * r]) : e);
    e = cmul2(e, e128);
  }
  hook(6);
#pragma unroll
  for (int a = 0; a < 4; ++a) {
    put_row(buf0, 8 + 2 * a, lane, e); put_row(buf0, 9 + 2 * a, lane, cmul2(e, er[4])); if (a < 3) e = cmul2(e, e128);
    put_row(buf1, 2 * a, lane, f); f = cmul2(f, e128); put_row(buf1, 2 * a + 1, lane, f); f = cmul2(f, e128);
    if (a == 1) hook(7);
  }
  put_row(buf1, 8, lane, f);
  hook(8);
  {
    v4u B1[8], B2[8];
#pragma unroll
    for (int ks = 0; ks < 8; ++ks) { B1[ks] = *(const v4u*)(buf0 + c * RS + 16 * ks + 4 * g); B2[ks] = *(const v4u*)(buf1 + c * RS + 16 * ks + 4 * g); }
    SB();
#pragma unroll
    for (int ks = 0; ks < 8; ++ks) { acc1 = mfma_h(A[ks], B1[ks], acc1); acc2 = mfma_h(A[ks], B2[ks], acc2); }
  }
  hook(9);
  const int cv0 = 128 * (c >> 3) + (c & 7);
  const int cv1 = c < 8 ? 256 + 128 * (c >> 2) + 2 * (c & 3) : 512 + 128 * ((c - 8) >> 1) + 4 * (c & 1);
  const int cv2 = c < 9 ? 1024 + 128 * c : 2048;
#pragma unroll
  for (int r = 0; r < 4; ++r) {
    const int lb = 8 * (4 * g + r);
    const unsigned q01 = pk_bf16(acc0[r] * sc + ((r == 0 && lane == 0) ? dsk : 0.f), acc1[r] * sc);
    tab[lb + cv0] = (u16)q01;
    tab[lb + cv1] = (u16)(q01 >> 16);
    tab[lb + cv2 + (c > 8 ? 1 : 0)] = (u16)pk_bf16(acc2[r] * sc, 0.f);
  }
  {
    const u16* t0 = tab + 2048 - 8 * lane;
    const int o = lane < 32 ? 1 : 0;
    const unsigned H0 = t0[0], L0 = t0[-8], H1 = t0[-512], L1 = t0[-520], H2 = t0[-1024], M2 = t0[-1028], L2 = t0[-1032];
    const u16* t3 = t0 - 1536;
    const unsigned v0 = t3[0], v1 = t3[-1 + o], v2 = t3[-2], v3 = t3[-3 - o], v4 = t3[-4], v5 = t3[-5 + o], v6 = t3[-6], v7 = t3[-7 - o], v8 = t3[-8];
    const unsigned k0 = tab[0];
    SB();
    unsigned char* i0 = img + lane * 16;
    unsigned char* i1 = i0 + KL * 2;
    {
      const unsigned X = (lane & 1) ? L0 : H0, HH = H0 | (H0 << 16), LL = L0 | (L0 << 16);
      *(v4u*)i0 = v4u{HH, HH, X | (L0 << 16), LL};
      *(v4u*)i1 = v4u{HH, H0 | (X << 16), LL, LL};
    }
    {
      const unsigned X = (lane & 1) ? L1 : H1, HH = H1 | (H1 << 16), LL = L1 | (L1 << 16);
      *(v4u*)(i0 + 1024) = v4u{HH, HH, X | (L1 << 16), LL};
      *(v4u*)(i1 + 1024) = v4u{HH, H1 | (X << 16), LL, LL};
    }
    {
      const unsigned HH = H2 | (H2 << 16), MM = M2 | (M2 << 16), LL = L2 | (L2 << 16);
      *(v4u*)(i0 + 2048) = v4u{HH, H2 | (M2 << 16), MM, LL};
      *(v4u*)(i1 + 2048) = v4u{HH, MM, M2 | (L2 << 16), LL};
    }
    *(v4u*)(i0 + 3072) = v4u{v0 | (v1 << 16), v2 | (v3 << 16), v4 | (v5 << 16), v6 | (v7 << 16)};
    *(v4u*)(i1 + 3072) = v4u{v1 | (v2 << 16), v3 | (v4 << 16), v5 | (v6 << 16), v7 | (v8 << 16)};
    if (lane < 8) *(v4u*)(img + (lane >> 2) * (KL * 2) + (256 + (lane & 3)) * 16) = v4u{lane == 0 ? k0 : 0u, 0u, 0u, 0u};
  }
}

constexpr int L_KIMG = 0, L_HIMG = 4 * KIMG_B, L_KSCR = L_HIMG + 4 * HL * 2;
constexpr int L_MISC = 86016, L_WL = L_MISC + 8192, L_DUMMY = L_WL + 65536, L_TOTAL = L_DUMMY + 4096;
static_assert(L_TOTAL <= 163840 && L_KSCR + 4 * 16 * RS * 4 <= L_MISC, "lds");
constexpr int MODHOOK = 4;
constexpr int ASP = 8;
constexpr int KAV = 11;
constexpr int NR1 = 24;
constexpr int FK = 12;
constexpr int ZDV = 3;
constexpr int NWO = 21;
constexpr int PDNV = 8;
constexpr int RHO = 3;
constexpr int ZPOLL = 6;
constexpr int OOB = 64 << 20;
constexpr int CA[36] = {3,3,3,3,3,4,4,4,4, 2,2,2,2,2,2,5,5,5, 1,1,1,1,6,6,1,1,1, 0,0,0,0,7,0,0,0,0};
constexpr int CB[36] = {4,3,2,1,0,3,2,1,0, 5,4,3,2,1,0,2,1,0, 6,5,4,3,1,0,2,1,0, 7,6,5,4,0,3,2,1,0};
constexpr int CA7[36] = {3,4,2,5,1,6,0,7, 3,4,2,5,1,6,0, 3,4,2,5,1,0, 3,4,2,1,0, 3,2,1,0, 2,1,0, 1,0, 0};
constexpr int CB7[36] = {4,3,5,2,6,1,7,0, 3,2,4,1,5,0,6, 2,1,3,0,4,5, 1,0,2,3,4, 0,1,2,3, 0,1,2, 0,1, 0};
constexpr int CL[16] = {8+4,3,8+3,8+2,8+1,8+0,4,8+5,2,5,8+6,1,6,8+7,0,7};

constexpr bool cs_dma(int ic) { return (ic % ASP) == 5 && ic / ASP < 16; }
constexpr int zs_lo(int i, int zd) {
  int v = i + zd; if (v > 2 + 2 * i) v = 2 + 2 * i; if (i >= 13 || v > 16) v = 16; return v;
}
DEVI void dma16s(rsrc_t r, int voff, int soff, unsigned ldsaddr) {
  asm volatile("s_mov_b32 m0, %2\n\ts_nop 0\n\tbuffer_load_dwordx4 %0, %1, %3 offen sc1 lds" :: "v"(voff), "s"(r), "s"(ldsaddr), "s"(soff) : "memory");
}
template <int X> DEVI void dma16x(rsrc_t r, int voff, int soff, unsigned ldsaddr) {
  int tmp;
  asm volatile("s_mov_b32 m0, %3\n\tv_xor_b32 %0, %5, %1\n\tbuffer_load_dwordx4 %0, %2, %4 offen lds" : "=&v"(tmp) : "v"(voff), "s"(r), "s"(ldsaddr), "s"(soff), "n"(X) : "memory");
}
DEVI void dma16(rsrc_t r, int voff, int soff, unsigned ldsaddr) {
  asm volatile("s_mov_b32 m0, %2\n\ts_nop 0\n\tbuffer_load_dwordx4 %0, %1, %3 offen lds" :: "v"(voff), "s"(r), "s"(ldsaddr), "s"(soff) : "memory");
}

DEVI unsigned plo(unsigned a, unsigned b) { return __builtin_amdgcn_perm(b, a, 0x05040100u); }
DEVI unsigned phi(unsigned a, unsigned b) { return __builtin_amdgcn_perm(b, a, 0x07060302u); }
DEVI unsigned silu_bf(float c) { return pk_bf16(c * __builtin_amdgcn_rcpf(1.0f + __builtin_amdgcn_exp2f(-1.4426950408889634f * c)), 0.f); }
DEVI float col_reduce(float* fl, const float (&acc)[8], int tid) {
  const int rg = tid >> 3, cl = tid & 7;
  *(f32x4*)(fl + rg * 68 + cl * 8) = f32x4{acc[0], acc[1], acc[2], acc[3]};
  *(f32x4*)(fl + rg * 68 + cl * 8 + 4) = f32x4{acc[4], acc[5], acc[6], acc[7]};
  __syncthreads();
  float s = 0.f;
#pragma unroll
  for (int r = 0; r < 8; ++r) s += fl[(8 * (tid & 3) + r) * 68 + (tid >> 2)];
  s += dppf<0xB1>(s); s += dppf<0x4E>(s);
  return s;
}
DEVI void wout_unit(const Params& p, rsrc_t rs, unsigned char* lw, int unit, int lane) {
  const int ks = unit / 5, dc = unit - 5 * ks, d = 128 * dc + 2 * lane;
  const int f0 = 16 * pair_blk(ks, 0), f1 = 16 * pair_blk(ks, 1);
  unsigned wv[4][8];
#pragma unroll
  for (int g = 0; g < 4; ++g)
#pragma unroll
    for (int j = 0; j < 8; ++j) wv[g][j] = NTL((const unsigned*)(p.w_out + (size_t)((j < 4 ? f0 : f1) + 2 * ((4 * g + (j & 3)) & 7) + (g >> 1)) * D + d));
#pragma unroll
  for (int g = 0; g < 4; ++g) {
    unsigned char* dst = lw + (lane >> 3) * 1024 + (g * 16 + 2 * (lane & 7)) * 16;
    *(v4u*)dst = v4u{plo(wv[g][0], wv[g][1]), plo(wv[g][2], wv[g][3]), plo(wv[g][4], wv[g][5]), plo(wv[g][6], wv[g][7])};
    *(v4u*)(dst + 16) = v4u{phi(wv[g][0], wv[g][1]), phi(wv[g][2], wv[g][3]), phi(wv[g][4], wv[g][5]), phi(wv[g][6], wv[g][7])};
  }
#pragma unroll
  for (int i = 0; i < 8; ++i) bst1(rs, O_WOUTF + ((8 * dc + i) * 27 + ks) * 1024 + lane * 16, *(const v4u*)(lw + i * 1024 + lane * 16));
}

DEVI void aux_role(const Params& p, rsrc_t rs, unsigned char* smem, int a, unsigned& tag) {
  const int tid = threadIdx.x, lane = tid & 63, w = __builtin_amdgcn_readfirstlane(tid >> 6);
  float* fl = (float*)smem;
  constexpr unsigned ONES = 0x3f803f80u;
  unsigned wv[5][8];
  const int blk = (a & 7) * 7 + (a >> 3), fp = tid & 15, rgp = tid >> 4;
  const u16* wsrc = p.w_in + (size_t)(8 * rgp) * F2 + 32 * min(blk, 53) + 2 * fp;
#define WIN_LD(i) { _Pragma("unroll") for (int j_ = 0; j_ < 8; ++j_) wv[i][j_] = NTL((const unsigned*)(wsrc + (128 * (i) + j_) * F2)); }
  if (a < 64) {
    const int row = 32 * a + 8 * w + (lane >> 3), c = lane & 7;
    v4u xv[10];
#pragma unroll
    for (int i = 0; i < 10; ++i) xv[i] = *(const v4u*)(p.u + (size_t)row * D + 64 * i + 8 * c);
    tag = __hip_atomic_load((GAS unsigned*)(p.ws + O_EPOCH), RLX_AGENT);
    SB();
    WIN_LD(0); WIN_LD(1);
    SB();
    float s0 = 0.f, s1 = 0.f, q0 = 0.f, q1 = 0.f;
#pragma unroll
    for (int i = 0; i < 10; ++i)
#pragma unroll
      for (int j = 0; j < 4; j += 2) {
        s0 = dot2(xv[i][j], ONES, s0); q0 = dot2(xv[i][j], xv[i][j], q0);
        s1 = dot2(xv[i][j + 1], ONES, s1); q1 = dot2(xv[i][j + 1], xv[i][j + 1], q1);
      }
    float m = s0 + s1, v = q0 + q1;
    m += dppf<0xB1>(m); v += dppf<0xB1>(v); m += dppf<0x4E>(m); v += dppf<0x4E>(v); m += dppf<0x141>(m); v += dppf<0x141>(v);
    m *= (1.0f / D);
    const float r = __builtin_amdgcn_rsqf(fmaxf(v * (1.0f / D) - m * m, 0.f) + 1e-5f);
    tag += 1u;
    if (c == 0) bst1(rs, O_STG + row * 16, v4u{f2u(m), tag, f2u(r), tag});
    SB();
    WIN_LD(2); WIN_LD(3); WIN_LD(4);
  } else if (a < 84) {
    const int rg = tid >> 3, cl = tid & 7, col0 = (a < 74 ? 2 * D + 64 * (a - 64) : 5 * D + 64 * (a - 74));
    unsigned* sL = (unsigned*)(smem + 12288);
    v4u mv[20];
    const v2u cv = *(const v2u*)(p.cvec + 4 * min(tid, 159));
#pragma unroll
    for (int i = 0; i < 20; ++i) mv[i] = __builtin_nontemporal_load((const v4u*)(p.w_mod + (size_t)(20 * rg + i) * 3840 + col0 + 8 * cl));
    tag = __hip_atomic_load((GAS unsigned*)(p.ws + O_EPOCH), RLX_AGENT);
    SB();
    if (tid < 160) *(v4u*)(sL + 4 * tid) = v4u{silu_bf(bflo(cv[0])), silu_bf(bfhi(cv[0])), silu_bf(bflo(cv[1])), silu_bf(bfhi(cv[1]))};
    __syncthreads();
    v4u s4[5];
#pragma unroll
    for (int i = 0; i < 5; ++i) s4[i] = *(const v4u*)(sL + 20 * rg + 4 * i);
    float acc[8];
#pragma unroll
    for (int j = 0; j < 8; ++j) acc[j] = 0.f;
#pragma unroll
    for (int i = 0; i < 20; ++i) {
      const unsigned slo = s4[i >> 2][i & 3], shi = slo << 16;
#pragma unroll
      for (int j = 0; j < 4; ++j) { acc[2 * j] = dot2(mv[i][j], slo, acc[2 * j]); acc[2 * j + 1] = dot2(mv[i][j], shi, acc[2 * j + 1]); }
    }
    const float s = col_reduce(fl, acc, tid);
    tag += 1u;
    if ((tid & 3) == 0) bst1_64(rs, O_MODG + (col0 + (tid >> 2)) * 8, v2u{f2u(rbf(s)), tag});
  } else {
    tag = __hip_atomic_load((GAS unsigned*)(p.ws + O_EPOCH), RLX_AGENT) + 1u;
    if (a == 92) {
      if (tid < 192) __hip_atomic_store((GAS unsigned*)(p.ws + O_CNT) + tid * 64, 0u, RLX_AGENT);
      __hip_atomic_store((GAS unsigned*)(p.ws + O_FLAG) + tid * 64, 0u, RLX_AGENT);
      DRAIN();
      __syncthreads();
      if (tid == 0) bst1_64(rs, O_INIT, v2u{0u, tag});
    }
  }
  if (a < 56) {
    float* c1L = (float*)(smem + 61440);
    u16* sbL = (u16*)(smem + 61440 + 2560);
    float* red = (float*)(smem + 16384);
#pragma unroll 1
    for (unsigned s = 0; s < SPIN; ++s) {
      asm volatile("" ::: "memory");
      v4u v[3][4];
#pragma unroll
      for (int i = 0; i < 3; ++i)
#pragma unroll
        for (int k = 0; k < 4; ++k) v[i][k] = bld1(rs, min(256 * i + tid, 639) * 16, O_MODP + (k * 2560 + 1280) * 8);
      const u64 vi = __hip_atomic_load((GAS u64*)(p.ws + O_INIT), RLX_AGENT);
      unsigned bad = (unsigned)(vi >> 32) ^ tag;
#pragma unroll
      for (int i = 0; i < 3; ++i) {
        const int e = min(256 * i + tid, 639);
#pragma unroll
        for (int k = 0; k < 4; ++k) bad |= (v[i][k][1] ^ tag) | (v[i][k][3] ^ tag);
        const unsigned pr = pk_bf16((u2f(v[i][0][0]) + u2f(v[i][1][0])) + (u2f(v[i][2][0]) + u2f(v[i][3][0])), (u2f(v[i][0][2]) + u2f(v[i][1][2])) + (u2f(v[i][2][2]) + u2f(v[i][3][2])));
        if (e < 320) *(unsigned*)(sbL + 2 * e) = pr; else *(f32x2*)(c1L + 2 * (e - 320)) = f32x2{rbf(1.0f + bflo(pr)), rbf(1.0f + bfhi(pr))};
      }
      PIN(bad);
      if (__all(bad == 0u)) break;
      __builtin_amdgcn_s_sleep(1);
    }
    ORDER();
    __syncthreads();
    f32x4 us[5];
    const int so = blk < 54 ? O_WINF + (2 * blk + (fp >> 3)) * 20480 + (rgp >> 2) * 1024 + ((rgp & 3) * 16 + (fp & 7)) * 16 : OOB;
    auto unit = [&](auto ic_) __attribute__((always_inline)) {
      constexpr int i = ic_;
      const int e = rgp + 16 * i, uo = so + i * 4096;
      const f32x4 ca = *(const f32x4*)(c1L + 8 * e), cb = *(const f32x4*)(c1L + 8 * e + 4);
      const v4u sb = *(const v4u*)(sbL + 8 * e);
      unsigned ta[4], tb[4], ra[4], rb[4];
#pragma unroll
      for (int j = 0; j < 4; ++j) {
        const f32x2 cc = j == 0 ? f32x2{ca[0], ca[1]} : j == 1 ? f32x2{ca[2], ca[3]} : j == 2 ? f32x2{cb[0], cb[1]} : f32x2{cb[2], cb[3]};
        const f32x2 ya = f32x2{bflo(wv[i][2 * j]), bflo(wv[i][2 * j + 1])} * cc, yb = f32x2{bfhi(wv[i][2 * j]), bfhi(wv[i][2 * j + 1])} * cc;
        ra[j] = pk_bf16(ya[0], ya[1]); rb[j] = pk_bf16(yb[0], yb[1]);
      }
      bst1(rs, uo, v4u{ra[0], ra[1], ra[2], ra[3]});
      bst1(rs, uo + 128, v4u{rb[0], rb[1], rb[2], rb[3]});
      float u1a = 0.f, u1b = 0.f, u2a = 0.f, u2b = 0.f;
#pragma unroll
      for (int j = 0; j < 4; ++j) {
        ta[j] = plo(wv[i][2 * j], wv[i][2 * j + 1]); tb[j] = phi(wv[i][2 * j], wv[i][2 * j + 1]);
        u1a = dot2(ra[j], ONES, u1a); u1b = dot2(rb[j], ONES, u1b);
        u2a = dot2(ta[j], sb[j], u2a); u2b = dot2(tb[j], sb[j], u2b);
      }
      us[i] = f32x4{u1a, u2a, u1b, u2b};
    };
    sfor<5>(unit);
    if (blk == 54) {
#pragma unroll
      for (int i = 0; i < 5; ++i) bst1(rs, O_WINF + 108 * 20480 + i * 4096 + tid * 16, v4u{ONES, ONES, ONES, ONES});
    }
    *(f32x4*)(red + tid * 4) = ((us[0] + us[1]) + (us[2] + us[3])) + us[4];
    __syncthreads();
    if (tid < 64) {
      const int c = (tid >> 1) & 15;
      float s = 0.f;
#pragma unroll
      for (int r = 0; r < 16; ++r) s += red[r * 64 + ((tid >> 5) * 8 + (c & 7)) * 4 + (c >> 3) * 2 + (tid & 1)];
      if (blk < 54) __builtin_amdgcn_raw_buffer_store_b32(f2u(s), rs, O_U + 32 * blk * 8 + tid * 4, 0, AUX_SC1);
    }
  } else {
    const int wi = (a >= 84 ? a - 84 : a - 44) * 4 + w;
    if (wi < 135) wout_unit(p, rs, smem + 16384 + w * 8192, wi, lane);
#pragma unroll 1
    for (unsigned s = 0; s < SPIN; ++s) {
      const u64 v = __hip_atomic_load((GAS u64*)(p.ws + O_INIT), RLX_AGENT);
      if (__all((unsigned)(v >> 32) == tag)) break;
      __builtin_amdgcn_s_sleep(1);
    }
    ORDER();
  }
  DRAIN();
  __syncthreads();
  arrive(p, a < 56 ? C_S : C_F, tid);
}

template <int CTRL> DEVI float dpp_add(float keep, float send) { return keep + dppf<CTRL>(send); }
DEVI float bfly16(const float (&v)[16], int i16) {
  float a[8], b[4], c[2];
  const bool h8 = i16 & 8, h4 = i16 & 4, h2 = i16 & 2, h1 = i16 & 1;
#pragma unroll
  for (int k = 0; k < 8; ++k) a[k] = dpp_add<0x128>(h8 ? v[k + 8] : v[k], h8 ? v[k] : v[k + 8]);
#pragma unroll
  for (int k = 0; k < 4; ++k) b[k] = dpp_add<0x141>(h4 ? a[k + 4] : a[k], h4 ? a[k] : a[k + 4]);
#pragma unroll
  for (int k = 0; k < 2; ++k) c[k] = dpp_add<0x4E>(h2 ? b[k + 2] : b[k], h2 ? b[k] : b[k + 2]);
  return dpp_add<0xB1>(h1 ? c[1] : c[0], h1 ? c[0] : c[1]);
}
DEVI f32x2 gelu2(f32x2 v) {
  const float c0 = -2.0f * 0.7978845608028654f * 1.4426950408889634f, c1 = c0 * 0.044715f;
  const f32x2 u = v * (v * v * c1 + c0);
  const f32x2 d = f32x2{__builtin_amdgcn_exp2f(u[0]), __builtin_amdgcn_exp2f(u[1])} + 1.0f;
  return v * f32x2{__builtin_amdgcn_rcpf(d[0]), __builtin_amdgcn_rcpf(d[1])};
}
__global__ void __launch_bounds__(256, 1) k_fused(Params p) {
  extern __shared__ __attribute__((aligned(16))) unsigned char smem[];
  const int tid = threadIdx.x, lane = tid & 63, w = __builtin_amdgcn_readfirstlane(tid >> 6);
  const int i16 = lane & 15, g = lane >> 4, l16 = lane * 16;
  const int bx = blockIdx.x & 7, bi = blockIdx.x >> 3;
  const rsrc_t rs = __builtin_amdgcn_make_buffer_rsrc((void*)p.ws, 0, O_END, 0x00020000);
  const rsrc_t rw = __builtin_amdgcn_make_buffer_rsrc((void*)p.w_sp, 0, T * T * 2, 0x00020000);
  unsigned tag;
  const unsigned lds0 = (unsigned)(size_t)(LAS unsigned char*)smem;
  u64 pc_; unsigned csz_;
  asm volatile("s_getpc_b64 %0\n.Lkf_beg:\n\ts_mov_b32 %1, .Lkf_end-.Lkf_beg" : "=s"(pc_), "=s"(csz_));
  auto code_prefetch = [&]() __attribute__((always_inline)) {
    const rsrc_t rc = __builtin_amdgcn_make_buffer_rsrc((void*)(pc_ & ~15ull), 0, (int)csz_, 0x00020000);
    if (w < 3) dma16(rc, l16, (bi * 3 + w) * 1024, lds0 + L_DUMMY + w * 1024);
  };
  const bool is_conv = bi < 20;
  const int q4 = bx * 20 + bi;
  const int panel = (bx >> 1) * 8 + (bi >> 2), j = (bx & 1) * 4 + (bi & 3);
  const int b0 = blk0(j);
  const int lb0 = (w < 3) ? 2 * w : 6, lb1 = 2 * w + 1;
  const bool has0 = (w < 3) || (j < 6), has1 = (w < 3);
  const int z2s0 = has0 ? O_WINF + (54 + b0 + lb0) * 20480 : OOB, z2s1 = O_WINF + (has1 ? 54 + b0 + lb1 : 108) * 20480;
  const int z1s0 = has0 ? O_WINF + (b0 + lb0) * 20480 : OOB, z1s1 = has1 ? O_WINF + (b0 + lb1) * 20480 : OOB;
  const int wx = w ^ (j & 3);
  const int wso = (64 * panel + 32 * (wx >> 1) + 4 * (wx & 1)) * 4096 + w * 1024;
#define WSP_DMA(lt, r, ldsoff) dma16x<((r) * 16) ^ (RHO << 8)>(rw, l16, (wso ^ ((32 * ((lt) >> 1) + 4 * ((lt) & 1)) * 4096)) + (8 * ((r) >> 2) + ((r) & 3)) * 4096, lds0 + (ldsoff) + w * 16384 + (r) * 1024)
  const int wrd = w * 16384 + i16 * 1024 + (g ^ (i16 & 3)) * 16;
#define W_ADDR(s) (wrq[(s) & 3] + ((s) >> 2) * 256)
  const int zu0 = has0 ? O_Z2N + (2 * panel) * 1024 + (b0 + lb0) * 65536 : OOB, zu1 = has1 ? O_Z2N + (2 * panel) * 1024 + (b0 + lb1) * 65536 : OOB;
  const int pu = O_P + ((4 * panel) * 27 + (w < 3 ? 3 * j + w : 24 + (j >> 1))) * 1024;
  const int pu16 = w < 3 ? pu : OOB, pu8 = (w == 3 && j < 6) ? pu + (j & 1) * 8 : OOB;
  v4u WR[32];
  int wrq[4];
#pragma unroll
  for (int q = 0; q < 4; ++q) wrq[q] = wrd + ((q ^ (i16 >> 2)) * 64);
#define WR_RD(q, dst) WR[dst] = *(const v4u*)(smem + L_WL + W_ADDR(q))
  v4u R[40];
#define R_LD(q, s0, s1) R[q] = bld1(rs, l16 + (((q) >> 1) & 3) * 1024, (((q) & 1) ? (s1) : (s0)) + ((q) >> 3) * 4096)
  bool oks = false;
  if (__builtin_expect(is_conv, 1)) {
    const int d = 4 * q4 + w;
    unsigned par[6];
    par[0] = NTL((const unsigned*)(p.a_re + d * NST + 2 * lane)); par[1] = NTL((const unsigned*)(p.a_im + d * NST + 2 * lane));
    par[2] = NTL((const unsigned*)(p.b_re + d * NST + 2 * lane)); par[3] = NTL((const unsigned*)(p.b_im + d * NST + 2 * lane));
    par[4] = NTL((const unsigned*)(p.c_re + d * NST + 2 * lane)); par[5] = NTL((const unsigned*)(p.c_im + d * NST + 2 * lane));
    const float ldt = bf2f(p.log_dt[d]);
    SB();
    v4u mv[5]; unsigned cv[5];
    const int mcg = q4 >> 2, mkq = q4 & 3;
#pragma unroll
    for (int i = 0; i < 5; ++i) {
      const int row = 160 * mkq + 32 * i + (tid >> 3);
      mv[i] = __builtin_nontemporal_load((const v4u*)(p.w_mod + (size_t)row * 3840 + 64 * mcg + (mcg >= 20 ? D : 0) + 8 * (tid & 7)));
      cv[i] = p.cvec[row];
    }
    const unsigned ep = __hip_atomic_load((GAS unsigned*)(p.ws + O_EPOCH), RLX_AGENT);
    const unsigned dku = p.dskip[d];
    v2u xa[8];
    const int ro = 64 * (tid >> 5) + 32 * ((tid >> 4) & 1) + 8 * ((tid >> 1) & 3) + 4 * ((tid >> 3) & 1) + 2 * (tid & 1);
    v4u st[4][2]; u64 mg;
    const int mo = lane < 32 ? O_MODP + ((lane & 3) * 2560 + (lane >> 4) * D + 4 * q4 + ((lane >> 2) & 3)) * 8 : (lane < 36 ? O_MODG + (2 * D + 4 * q4 + (lane & 3)) * 8 : O_INIT);
    auto gather = [&]() __attribute__((always_inline)) {
      mg = __hip_atomic_load((GAS u64*)(p.ws + mo), RLX_AGENT);
#pragma unroll
      for (int i = 0; i < 4; ++i) { st[i][0] = bld1(rs, ro * 16, O_STG + i * 8192); st[i][1] = bld1(rs, ro * 16 + 16, O_STG + i * 8192); }
    };
    const rsrc_t ru = __builtin_amdgcn_make_buffer_rsrc((void*)p.u, 0, T * D * 2, 0x00020000);
    auto xld = [&](int i) __attribute__((always_inline)) { xa[i] = __builtin_amdgcn_raw_buffer_load_b64(ru, ro * (2 * D), (512 * (i >> 1) + (i & 1)) * (2 * D) + 8 * q4, 0); };
    auto hook = [&](int i) __attribute__((always_inline)) {
      SB();
      if (i == MODHOOK) {
        tag = ep + 1u;
        float acc[8];
#pragma unroll
        for (int k = 0; k < 8; ++k) acc[k] = 0.f;
#pragma unroll
        for (int q = 0; q < 5; ++q) {
          const unsigned slo = silu_bf(bf2f(cv[q])), shi = slo << 16;
#pragma unroll
          for (int k = 0; k < 4; ++k) { acc[2 * k] = dot2(mv[q][k], slo, acc[2 * k]); acc[2 * k + 1] = dot2(mv[q][k], shi, acc[2 * k + 1]); }
        }
        const float s = col_reduce((float*)(smem + L_WL), acc, tid);
        if ((tid & 3) == 0) bst1_64(rs, O_MODP + (mkq * 2560 + 64 * mcg + (tid >> 2)) * 8, v2u{f2u(s), tag});
        SB();
        code_prefetch();
      }
      if (i == 2) xld(2);
      if (i == 4) { xld(3); xld(4); }
      if (i >= 5 && i <= 7) xld(i);
      if (i == 9) gather();
      SB();
    };
    SB();
    asm volatile("s_barrier" ::: "memory");
    xld(0); xld(1);
    SB();
    u16* himg = (u16*)(smem + L_HIMG);
    if (tid < 128) *(v4u*)(himg + (tid >> 5) * HL + (tid & 31) * 8) = v4u{0, 0, 0, 0};
    const float dt = rbf(__builtin_amdgcn_exp2f(ldt * 1.4426950408889634f));
    kgen_wave(par, dt, bf2f(dku), (unsigned*)(smem + L_KSCR) + w * 16 * RS, smem + L_KIMG + w * KIMG_B, lane, hook);
#pragma unroll 1
    for (unsigned s = 0; s < SPIN; ++s) {
      unsigned bad = (lane >> 2) == 8 ? 0u : (unsigned)(mg >> 32) ^ tag;
#pragma unroll
      for (int i = 0; i < 4; ++i) bad |= (st[i][0][1] ^ tag) | (st[i][0][3] ^ tag) | (st[i][1][1] ^ tag) | (st[i][1][3] ^ tag);
      PIN(bad);
      if (__all(bad == 0u)) break;
      asm volatile("" ::: "memory");
      gather();
    }
    ORDER();
    float sa[4], c1[4];
    {
      float ms = u2f((unsigned)mg);
      ms += dppf<0xB1>(ms); ms += dppf<0x4E>(ms);
      const unsigned mr = f2u(rbf(ms));
#pragma unroll
      for (int c = 0; c < 4; ++c) {
        sa[c] = u2f(__builtin_amdgcn_readlane(mr, 4 * c));
        c1[c] = rbf(1.0f + u2f(__builtin_amdgcn_readlane(mr, 16 + 4 * c)));
      }
    }
    if (!__all((unsigned)(mg >> 32) == tag)) mg = __hip_atomic_load((GAS u64*)(p.ws + mo), RLX_AGENT);
    SB();
#pragma unroll
    for (int i = 0; i < 4; ++i) {
      const int pr = 256 * i + tid;
      const f32x2 m = {u2f(st[i][0][0]), u2f(st[i][1][0])}, r = {u2f(st[i][0][2]), u2f(st[i][1][2])};
      const f32x2 x[4] = {f32x2{bflo(xa[2 * i][0]), bflo(xa[2 * i + 1][0])}, f32x2{bfhi(xa[2 * i][0]), bfhi(xa[2 * i + 1][0])},
                          f32x2{bflo(xa[2 * i][1]), bflo(xa[2 * i + 1][1])}, f32x2{bfhi(xa[2 * i][1]), bfhi(xa[2 * i + 1][1])}};
#pragma unroll
      for (int c = 0; c < 4; ++c) {
        const f32x2 h = (x[c] - m) * r * c1[c] + sa[c];
        *(unsigned*)(himg + c * HL + 256 + 512 * i + ro) = pk_bf16(h[0], h[1]);
      }
    }
    unsigned ab[8];
#pragma unroll
    for (int a = 0; a < 8; ++a) { ab[a] = lds0 + L_KIMG + w * KIMG_B + (i16 & 1) * (KL * 2) + (2032 - (i16 & ~1) - 2 * (i16 & 1) + 8 * g) * 2 - 64 * (8 * a + 7); PIN(ab[a]); }
    unsigned bb = lds0 + L_HIMG + (w * HL + 256 - 16 + 16 * i16 + 8 * g) * 2 - 64 * 7; PIN(bb);
#pragma unroll 1
    for (unsigned s = 0; s < SPIN; ++s) {
      if (__all((unsigned)(mg >> 32) == tag)) break;
      mg = __hip_atomic_load((GAS u64*)(p.ws + mo), RLX_AGENT);
    }
    float ga[4];
#pragma unroll
    for (int c = 0; c < 4; ++c) ga[c] = u2f(__builtin_amdgcn_readlane((unsigned)mg, 32 + c));
    __syncthreads();
    f32x4 acc[8];
#pragma unroll
    for (int c = 0; c < 8; ++c) acc[c] = f32x4{0.f, 0.f, 0.f, 0.f};
    v4u O[2][16];
#define CLOAD1(buf, r, k) { if constexpr (CL[k] < 8) { const LAS unsigned* ap = (const LAS unsigned*)(size_t)ab[CL[k] & 7] + 16 * (7 - (r)); O[buf][CL[k]] = v4u{ap[0], ap[1], ap[2], ap[3]}; } \
                            else O[buf][CL[k]] = *((const LAS v4u*)(size_t)bb + 4 * (8 * (CL[k] - 8) + 7 - (r))); }
    sfor<16>(LAM { constexpr int k = ic_; CLOAD1(0, 0, k); });
    SB();
    unsigned cav = 0;
    u16* ybuf = (u16*)(smem + L_KSCR);
    f32x2 xf[4][4];
#define Y_OUT(c) *(v2u*)(ybuf + w * T + 256 * (c) + 16 * i16 + 4 * g) = v2u{pk_bf16(acc[c][0], acc[c][1]), pk_bf16(acc[c][2], acc[c][3])}
    sfor<8 * 36>(LAM {
      constexpr int r = ic_ / 36, m = ic_ % 36;
      constexpr int ca = r < 7 ? CA[m] : CA7[m], cb = r < 7 ? CB[m] : CB7[m];
      acc[ca + cb] = mfma_bf(O[r & 1][ca], O[r & 1][8 + cb], acc[ca + cb]);
      if constexpr (r < 7 && (m & 1) == 0 && m < 32) CLOAD1((r + 1) & 1, r + 1, m >> 1);
      if constexpr (cs_dma(ic_)) WSP_DMA(0, ic_ / ASP, L_WL);
      if constexpr ((r == 5 || r == 6) && (m & 3) == 3 && m < 32) {
        constexpr int q = 8 * (r - 5) + (m >> 2), i = q >> 2, c = q & 3;
        xf[i][c] = (c & 1) ? f32x2{bfhi(xa[2 * i][c >> 1]), bfhi(xa[2 * i + 1][c >> 1])} : f32x2{bflo(xa[2 * i][c >> 1]), bflo(xa[2 * i + 1][c >> 1])};
        PIN(xf[i][c]);
      }
      if constexpr (r == 7) {
        if constexpr (m == 11) Y_OUT(7);
        if constexpr (m == 18) Y_OUT(6);
        if constexpr (m == 24) Y_OUT(5);
        if constexpr (m == 29) Y_OUT(4);
        if constexpr (m == 33) Y_OUT(3);
        if constexpr (m == 34) cav = cnt_load(p, C_S, lane);
      }
      SB();
    });
    Y_OUT(2); Y_OUT(1); Y_OUT(0);
    __syncthreads();
#pragma unroll
    for (int i = 0; i < 4; ++i) {
      const int pr = 256 * i + tid;
      f32x2 o[4];
#pragma unroll
      for (int c = 0; c < 4; ++c) o[c] = xf[i][c] + unp(*(const unsigned*)(ybuf + c * T + 512 * i + ro)) * ga[c];
      bst1(rs, O_X1G + (q4 * T + 2 * pr) * 8, v4u{pk_bf16(o[0][0], o[1][0]), pk_bf16(o[2][0], o[3][0]), pk_bf16(o[0][1], o[1][1]), pk_bf16(o[2][1], o[3][1])});
    }
    SB();
    oks = __all(cav >= 7u);
    ORDER();
  } else {
    aux_role(p, rs, smem, (bi - 20) * 8 + bx, tag);
    code_prefetch();
    sfor<16>(LAM { WSP_DMA(0, ic_, L_WL); });
    DRAIN();
  }
#pragma unroll
  for (int q = 0; q < 16; ++q) WR_RD(q, q);
  int otab;
  {
    const int lf = lane & 7;
    int ab = 2 * w + lf; ab = ab >= 7 ? ab - 7 : ab;
    const int zo = (ab == 6 && j >= 6) ? OOB : O_Z2N + (b0 + ab) * 65536 + 16 * w * 1024;
    const int sl = (ab * 3 + ((w - (ab >> 1) - 1) & 3)) * 4096;
    const int ro = (sl < 65536 ? L_WL + sl : sl) + ((w ^ (ab >> 1)) << 10);
    const int n = 6 * w + lf - 1;
    const int ko = (lf == 0 ? 3 * j + min(w, 2) : (n < 3 * j ? n : n + 3)) * 1024;
    const int lt = lane & 3;
    const int po = (lane & 4) ? ((lt ^ w) * 3 + lt - 1) * 5120 : O_P + (4 * panel + (lt ^ w)) * (27 * 1024);
    const int sel = (lane >> 3) & 3;
    otab = sel == 0 ? zo : (sel == 1 ? ro : (sel == 2 ? ko : po));
    PIN(otab);
  }
  asm volatile("s_waitcnt lgkmcnt(0)" ::: "memory");
  if (is_conv) {
    DRAIN();
    __syncthreads();
    arrive(p, C_X1, tid);
  }
  if (!oks) cnt_wait<4>(p, C_S, 7u, lane);
  {
    const rsrc_t rb = __builtin_amdgcn_make_buffer_rsrc((void*)p.b_sp, 0, T * 2, 0x00020000);
    if (w < 3) dma16s(rs, l16, w == 0 ? O_U + (FH + 16 * b0) * 8 : (w == 1 ? O_U + 16 * b0 * 8 : OOB), lds0 + L_DUMMY + w * 1024);
    else dma16(rb, l16, 128 * panel, lds0 + L_DUMMY + 3072);
  }
#pragma unroll
  for (int q = 0; q < NR1; ++q) R_LD(q, z2s0, z2s1);
  SB();
  sfor<16>(LAM { WSP_DMA(1, ic_, L_WL); });
  SB();
  const int xoff = ((lane >> 5) * T + 2 * (lane & 31)) * 8;
  const int xso = O_X1G + (2 * w * T + 64 * panel) * 8;
#define X_DMA(ks) dma16s(rs, xoff, xso + (ks) * (8 * T * 8), lds0 + ((ks) * 4 + w) * 1024)
  float* sxL = (float*)(smem + L_MISC);
  float* st1 = (float*)(smem + L_MISC + 512) + w * 128;
  float* gsum = (float*)(smem + L_MISC + 2560);
  float* st2 = (float*)(smem + L_MISC + 4608) + w * 128;
  unsigned xl[4];
#pragma unroll
  for (int i = 0; i < 4; ++i) { xl[i] = lds0 + g * 1024 + (i ^ wx) * 128 + i16 * 8; PIN(xl[i]); }
  auto ldx = [&](unsigned lo, int off) __attribute__((always_inline)) {
    const v2u a = *(const LAS v2u*)(size_t)(lo + off), c = *(const LAS v2u*)(size_t)(lo + off + 512);
    return v4u{a[0], a[1], c[0], c[1]};
  };
  const int pfo = ((w < 3 || j < 6) ? pu : OOB) + l16;
  f32x4 a0[4], a1[4], gram = {0.f, 0.f, 0.f, 0.f};
#pragma unroll
  for (int i = 0; i < 4; ++i) { a0[i] = f32x4{0.f, 0.f, 0.f, 0.f}; a1[i] = a0[i]; }
  if (w == 3) cnt_wait<2>(p, C_X1, 20u, lane);
  __syncthreads();
#pragma unroll
  for (int ks = 0; ks < 8; ++ks) X_DMA(ks);
  asm volatile("s_waitcnt vmcnt(4)\n\ts_barrier" ::: "memory");
  {
    v4u A[3][4];
    sfor<8>(LAM { A[ic_ >> 2][ic_ & 3] = ldx(xl[ic_ & 3], (ic_ >> 2) * 4096); SB(); });
    sfor<20 * 9>(LAM {
      constexpr int ks = ic_ / 9, m = ic_ % 9;
      if constexpr (m == 0 && ks == 2) asm volatile("s_waitcnt vmcnt(6)\n\ts_barrier" ::: "memory");
      if constexpr (m == 0 && (ks == 6 || ks == 10)) asm volatile("s_waitcnt vmcnt(12)\n\ts_barrier" ::: "memory");
      if constexpr (m == 0 && ks == 8) { sfor<4>([&](auto i_) __attribute__((always_inline)) { xl[i_] += 40960; PIN(xl[i_]); }); }
      if constexpr (m == 0 && ks < 19) __builtin_amdgcn_s_waitcnt(0xC47F);
      if constexpr (m == 2) gram = mfma_bf(A[ks % 3][0], A[ks % 3][0], gram);
      else {
        constexpr int mm = m < 2 ? m : m - 1;
        if constexpr (mm & 1) a1[mm >> 1] = mfma_bf(A[ks % 3][mm >> 1], R[2 * ks + 1], a1[mm >> 1]);
        else a0[mm >> 1] = mfma_bf(A[ks % 3][mm >> 1], R[2 * ks], a0[mm >> 1]);
      }
      if constexpr ((m & 1) == 1 && ks < 18) A[(ks + 2) % 3][m >> 1] = ldx(xl[m >> 1], (ks + 2 - (ks >= 8 ? 10 : 0)) * 4096);
      if constexpr (ks >= 18 && m < 8) WR_RD(8 * (ks - 18) + m, 16 + 8 * (ks - 18) + m);
      if constexpr ((m == 2 || m == 4 || m == 8) && ks < 4) X_DMA(8 + 3 * ks + (m >> 2));
      if constexpr ((m == 4 || m == 8) && ks >= 4 && !(ks >= FK && ks < FK + 2)) { constexpr int n = NR1 + 2 * (ks - 4 - (ks >= FK ? 2 : 0)) + (m == 8); if constexpr (n < 40) R_LD(n, z2s0, z2s1); else R_LD(n - 40, z1s0, z1s1); }
      if constexpr ((m == 4 || m == 8) && ks >= FK && ks < FK + 2)
        bst1(rs, pfo + (2 * (ks - FK) + (m == 8)) * (27 * 1024), v4u{SENT, SENT, SENT, SENT});
      SB();
    });
  }
  if (w == 3) {
    if (i16 == 0) {
#pragma unroll
      for (int i = 0; i < 4; ++i) *(f32x4*)(sxL + (i ^ wx) * 16 + 4 * g) = a1[i];
    }
#pragma unroll
    for (int i = 0; i < 4; ++i) a1[i] = f32x4{0.f, 0.f, 0.f, 0.f};
  }
  if ((i16 >> 2) == g) sxL[64 + wx * 16 + i16] = (i16 & 3) == 0 ? gram[0] : (i16 & 3) == 1 ? gram[1] : (i16 & 3) == 2 ? gram[2] : gram[3];
  __syncthreads();
  {
    const float m = sxL[lane] * (1.0f / D);
    const float var = fmaxf(sxL[64 + lane] * (1.0f / D) - m * m, 0.f);
    const float r = __builtin_amdgcn_rsqf(var + 1e-5f);
    st1[lane] = r; st1[64 + lane] = r * m;
  }
  v4u A1[3][4];
  {
    const f32x2* cU2 = (const f32x2*)(smem + L_DUMMY);
    f32x2 u0 = cU2[16 * lb0 + i16], u1 = cU2[16 * lb1 + i16];
    if (!has0) u0 = f32x2{0.f, 0.f};
    if (!has1) u1 = f32x2{0.f, 0.f};
    float s1[16], s2[16];
    f32x4 rr[4], rm[4];
#pragma unroll
    for (int i = 0; i < 4; ++i) { rr[i] = *(const f32x4*)(st1 + (i ^ wx) * 16 + 4 * g); rm[i] = *(const f32x4*)(st1 + 64 + (i ^ wx) * 16 + 4 * g); }
    sfor<8>(LAM {
      constexpr int i = ic_ >> 1, h = ic_ & 1;
      { constexpr int n0 = NR1 - 12, nn = 40 - n0;
#pragma unroll
        for (int q = n0 + nn * ic_ / 8; q < n0 + nn * (ic_ + 1) / 8; ++q) R_LD(q, z1s0, z1s1); }
      const f32x2 r2 = {rr[i][2 * h], rr[i][2 * h + 1]}, m2 = {rm[i][2 * h], rm[i][2 * h + 1]};
      const f32x2 v0 = gelu2(f32x2{a0[i][2 * h], a0[i][2 * h + 1]} * r2 + (u0[1] - m2 * u0[0]));
      const f32x2 v1 = gelu2(f32x2{a1[i][2 * h], a1[i][2 * h + 1]} * r2 + (u1[1] - m2 * u1[0]));
      a0[i][2 * h] = v0[0]; a0[i][2 * h + 1] = v0[1]; a1[i][2 * h] = v1[0]; a1[i][2 * h + 1] = v1[1];
      f32x2 q1 = v0 + v1, q2 = v0 * v0 + v1 * v1;
      PIN(q1); PIN(q2);
      s1[4 * i + 2 * h] = q1[0]; s1[4 * i + 2 * h + 1] = q1[1]; s2[4 * i + 2 * h] = q2[0]; s2[4 * i + 2 * h + 1] = q2[1];
      SB();
    });
    sfor<4>(LAM { xl[ic_] -= 40960; PIN(xl[ic_]); A1[0][ic_] = ldx(xl[ic_], 0); SB(); A1[1][ic_] = ldx(xl[ic_], 4096); SB(); });
    const float t1 = bfly16(s1, i16), t2 = bfly16(s2, i16);
    const int tp = ((i16 >> 2) ^ wx) * 16 + 4 * g + (i16 & 3);
    asm volatile("s_waitcnt vmcnt(%0)" :: "n"(52 - NR1) : "memory");
    bst1(rs, O_ZST + (((panel * 4 + w) * 8 + j) * 64 + tp) * 16, v4u{f2u(t1), tag, f2u(t2), tag});
  }
  SB();
  f32x4 c0[4], c1[4];
#pragma unroll
  for (int i = 0; i < 4; ++i) { c0[i] = f32x4{0.f, 0.f, 0.f, 0.f}; c1[i] = c0[i]; }
#define Z1M(ks, m) { if constexpr ((m) == 0 && (ks) >= 2 && (ks) < 19 && (ks) != KA && (ks) != KA + 1) __builtin_amdgcn_s_waitcnt(0xC47F); \
    if constexpr ((m) & 1) c1[(m) >> 1] = mfma_bf(R[2 * (ks) + 1], A1[(ks) % 3][(m) >> 1], c1[(m) >> 1]); else c0[(m) >> 1] = mfma_bf(R[2 * (ks)], A1[(ks) % 3][(m) >> 1], c0[(m) >> 1]); \
    if constexpr (((m) & 1) == 1 && (ks) < 18) A1[((ks) + 2) % 3][(m) >> 1] = ldx(xl[(m) >> 1], ((ks) + 2 - ((ks) >= KA ? KA : 0)) * 4096); }
  constexpr int KA = KAV;
  v4u zs[8];
  unsigned cfv = 0;
  const int zsoff = O_ZST + ((panel * 4 + w) * 8 * 64) * 16;
  sfor<KA * 8>(LAM {
    constexpr int ks = ic_ / 8, m = ic_ % 8;
    Z1M(ks, m);
    if constexpr ((m == 2 || m == 6) && ks < 8) { constexpr int q = 2 * ks + (m == 6); WSP_DMA(2, q, L_WL); }
    if constexpr (m == 4 && ks == 7) cfv = cnt_load(p, C_F, lane);
    if constexpr ((m & 1) == 0 && ks >= 8 && ks < 10) { constexpr int jj = 4 * (ks - 8) + (m >> 1); zs[jj] = bld1(rs, l16 + (jj & 3) * 1024, zsoff + (jj >> 2) * 4096); }
    SB();
  });
  v2u x1v[5];
#pragma unroll
  for (int db = 0; db < 5; ++db) {
    const int d = 80 * j + 16 * db + 4 * g;
    x1v[db] = *(const v2u*)(smem + (d >> 2) * 512 + (16 * w + i16) * 8);
  }
  {
#pragma unroll 1
    for (unsigned s = 0; s < SPIN; ++s) {
      unsigned bad = 0;
#pragma unroll
      for (int jj = 0; jj < 8; ++jj) bad |= (zs[jj][1] ^ tag) | (zs[jj][3] ^ tag);
      PIN(bad);
      if (__all(bad == 0u)) break;
      asm volatile("" ::: "memory");
#pragma unroll
      for (int jj = 0; jj < 8; ++jj) zs[jj] = bld1(rs, l16, zsoff + jj * 1024);
    }
    float a = 0.f, b = 0.f;
#pragma unroll
    for (int jj = 0; jj < 8; ++jj) { a += u2f(zs[jj][0]); b += u2f(zs[jj][2]); }
    *(f32x2*)(gsum + (w * 64 + lane) * 2) = f32x2{a, b};
  }
  if (!__all(cfv >= 5u)) cnt_wait<1>(p, C_F, 5u, lane);
  ORDER();
  __syncthreads();
  {
    const f32x2 s = (*(const f32x2*)(gsum + lane * 2) + *(const f32x2*)(gsum + 128 + lane * 2)) + (*(const f32x2*)(gsum + 256 + lane * 2) + *(const f32x2*)(gsum + 384 + lane * 2));
    const float m = s[0] * (1.0f / FH);
    const float var = fmaxf(s[1] * (1.0f / FH) - m * m, 0.f);
    const float r = __builtin_amdgcn_rsqf(var + 1e-5f);
    st2[lane] = r; st2[64 + lane] = r * m;
  }
  {
    unsigned o0[4][2], o1[4][2];
#pragma unroll
    for (int i = 0; i < 4; ++i) {
      const f32x4 rr = *(const f32x4*)(st2 + (i ^ wx) * 16 + 4 * g), rm = *(const f32x4*)(st2 + 64 + (i ^ wx) * 16 + 4 * g);
      const f32x4 n0 = a0[i] * rr - rm, n1 = a1[i] * rr - rm;
      o0[i][0] = pk_bf16(n0[0], n0[1]); o0[i][1] = pk_bf16(n0[2], n0[3]);
      o1[i][0] = pk_bf16(n1[0], n1[1]); o1[i][1] = pk_bf16(n1[2], n1[3]);
    }
    const bool sw = wx & 1;
    const int ph = (wx >> 1) * 1024;
#pragma unroll
    for (int pp = 0; pp < 2; ++pp) {
      const int e = 2 * pp, o = 2 * pp + 1;
      bst1(rs, zu0 + ((pp * 1024) ^ ph) + l16, sw ? v4u{o0[o][0], o0[o][1], o0[e][0], o0[e][1]} : v4u{o0[e][0], o0[e][1], o0[o][0], o0[o][1]});
      bst1(rs, zu1 + ((pp * 1024) ^ ph) + l16, sw ? v4u{o1[o][0], o1[o][1], o1[e][0], o1[e][1]} : v4u{o1[e][0], o1[e][1], o1[o][0], o1[o][1]});
    }
  }
  sfor<4>(LAM { xl[ic_] += KA * 4096; PIN(xl[ic_]); });
  SB();
  sfor<(20 - KA) * 8>(LAM {
    constexpr int ks = KA + ic_ / 8, m = ic_ % 8;
    Z1M(ks, m);
    SB();
  });
  DRAIN();
  __syncthreads();
  GAS unsigned* flags = (GAS unsigned*)(p.ws + O_FLAG);
  if (tid < 4) __hip_atomic_fetch_or(flags + ((j * 4 + (panel >> 3)) * 4 + tid) * 64, 1u << ((panel & 7) ^ (2 * RHO)), RLX_AGENT);
  if (w == 2) dma16s(rs, l16, O_MODG + (5 * D + 80 * j) * 8, lds0 + L_DUMMY + 2048);
  int zof[7], rof[7], kof[7];
#pragma unroll
  for (int q = 0; q < 7; ++q) { zof[q] = __builtin_amdgcn_readlane(otab, q); rof[q] = __builtin_amdgcn_readlane(otab, 8 + q); kof[q] = __builtin_amdgcn_readlane(otab, 16 + q); }
  constexpr int ZD = ZDV;
  v4u Z[16][7];
#define Z_LD(i, f) Z[i][f] = bld1(rs, l16 + ((i) & 3) * 1024, zof[f] + ((((i) >> 2) ^ RHO) * 4096))
  unsigned zmask = 0u, zfv = 0u;
  GAS unsigned* zfp = flags + ((j * 4 + w) * 4 + (panel >> 3)) * 64;
  auto zneed = [&](unsigned bits) __attribute__((always_inline)) {
    if (__builtin_expect((zmask & bits) != bits, 0)) {
#pragma unroll 1
      for (unsigned s = 0; s < SPIN; ++s) {
        zmask = __builtin_amdgcn_readfirstlane(__hip_atomic_load(zfp, RLX_AGENT));
        if ((zmask & bits) == bits) break;
        __builtin_amdgcn_s_sleep(1);
      }
    }
    ORDER();
  };
  unsigned z1p[2][4][2];
  {
    const v4u* cU1 = (const v4u*)(smem + L_DUMMY + 1024);
    const v4u ua0 = cU1[8 * lb0 + 2 * g], ua1 = cU1[8 * lb0 + 2 * g + 1], ub0 = cU1[8 * lb1 + 2 * g], ub1 = cU1[8 * lb1 + 2 * g + 1];
    float sr[4], sm[4];
#pragma unroll
    for (int i = 0; i < 4; ++i) { sr[i] = st1[16 * (i ^ wx) + i16]; sm[i] = st1[64 + 16 * (i ^ wx) + i16]; }
    sfor<8>(LAM {
      constexpr int i = ic_ >> 1, h = ic_ & 1;
      if constexpr (ic_ < 4) { WSP_DMA(3, 4 * ic_, 0); WSP_DMA(3, 4 * ic_ + 1, 0); WSP_DMA(3, 4 * ic_ + 2, 0); WSP_DMA(3, 4 * ic_ + 3, 0); }
      if constexpr (ic_ == ZPOLL - 2) { zfv = __hip_atomic_load(zfp, RLX_AGENT); SB(); }
      if constexpr (ic_ == ZPOLL) { zmask = __builtin_amdgcn_readfirstlane(zfv); zneed(1u); }
      if constexpr (ic_ >= ZPOLL) {
        constexpr int n = 8 - ZPOLL, k = ic_ - ZPOLL;
#pragma unroll
        for (int q = 14 * k / n; q < 14 * (k + 1) / n; ++q) Z_LD(q / 7, q % 7);
        SB();
      }
      const v4u qa = h ? ua1 : ua0, qb = h ? ub1 : ub0;
      const f32x2 v0 = gelu2(f32x2{c0[i][2 * h], c0[i][2 * h + 1]} * sr[i] + (f32x2{u2f(qa[1]), u2f(qa[3])} - f32x2{u2f(qa[0]), u2f(qa[2])} * sm[i]));
      const f32x2 v1 = gelu2(f32x2{c1[i][2 * h], c1[i][2 * h + 1]} * sr[i] + (f32x2{u2f(qb[1]), u2f(qb[3])} - f32x2{u2f(qb[0]), u2f(qb[2])} * sm[i]));
      z1p[0][i][h] = pk_bf16(v0[0], v0[1]); z1p[1][i][h] = pk_bf16(v1[0], v1[1]);
      PIN(z1p[0][i][h]); PIN(z1p[1][i][h]);
    });
  }
  SB();
  f32x4 acc[7][4];
  const int woso = O_WOUTF + (5 * j) * 27 * 1024;
  v4u WO[35];
  v4u WL[4][2];
#define WO_LD(q) WO[q] = bld1(rs, l16, woso + ((q) % 5) * (27 * 1024) + kof[(q) / 5])
#define WL_RD(i, h) WL[(i) & 3][h] = *(const v4u*)(smem + ((h) ? 0 : L_WL) + W_ADDR(i))
  WL_RD(0, 0); WL_RD(0, 1);
  sfor<13 * 28>(LAM {
    constexpr int i = ic_ / 28, f = (ic_ % 28) / 4, t = ic_ % 4;
    constexpr int lo = zs_lo(i, ZD), hi = zs_lo(i + 1, ZD);
    if constexpr (f == 0 && t == 0 && hi > lo && ((lo & 1) == 0 || hi - lo > 1)) zneed((1u << ((hi - 1) >> 1)) | (1u << (lo >> 1)));
    if constexpr (t < 2) mfma_pin<i == 0, true>(acc[f][t], Z[i][f], WR[t * 16 + i]);
    else mfma_pin<i == 0, false>(acc[f][t], Z[i][f], WL[i & 3][t - 2]);
    if constexpr (hi - lo == 2 && (t == 0 || t == 2)) { constexpr int q = 2 * f + (t >> 1); Z_LD(lo + q / 7, q % 7); }
    if constexpr (hi - lo == 1 && t == 0) Z_LD(lo, f);
    if constexpr (t == 1 && f < 2 && i < 12) WL_RD(i + 1, f);
    if constexpr (t == 1 && f < 6 && i == 12) WL_RD(13 + (f >> 1), f & 1);
    SB();
  });
  asm volatile("s_nop 7\n\ts_waitcnt lgkmcnt(0)\n\ts_barrier" ::: "memory");
  sfor<7 * 12>(LAM {
    constexpr int fo = ic_ / 12, f = (fo + 2) % 7, i = 13 + (ic_ % 12) / 4, t = ic_ % 4, m = ic_ % 12;
    if constexpr (t < 2) acc[f][t] = mfma_bf(Z[i][f], WR[t * 16 + i], acc[f][t]);
    else acc[f][t] = mfma_bf(Z[i][f], WL[i & 3][t - 2], acc[f][t]);
    if constexpr (m % 4 == 0 && 3 * fo + m / 4 < NWO) WO_LD(3 * fo + m / 4);
    if constexpr (fo >= 1 && fo <= 5 && m % 3 == 2) { constexpr int pf = fo + 1, pt = m / 3; *(f32x4*)(smem + ((rof[pf] + l16) ^ (pt << 10))) = acc[pf][pt]; }
    SB();
  });
  if (w == 3) {
#pragma unroll
    for (int t = 0; t < 4; ++t) *(f32x4*)(smem + ((rof[1] + l16) ^ (t << 10))) = acc[1][t];
  }
  f32x4 o0[4], o1[4];
#pragma unroll
  for (int t = 0; t < 4; ++t) {
    const float bias = bf2f(((const u16*)(smem + L_DUMMY + 3072))[8 * (i16 >> 2) + (i16 & 3) + 32 * ((t ^ wx) >> 1) + 4 * ((t ^ wx) & 1)]);
    o0[t] = acc[0][t] + bias; o1[t] = acc[1][t] + bias; PIN(o0[t]); PIN(o1[t]);
  }
  __syncthreads();
  v4u PO[4];
  {
    f32x4 pa[4][3], pb[4][3];
#pragma unroll
    for (int s = 0; s < 3; ++s) {
      const int s0 = (2 * w * 3 + s) * 4096, s1 = ((w < 3 ? 2 * w + 1 : 6) * 3 + s) * 4096;
      const unsigned char* r0 = smem + (s0 < 65536 ? L_WL + s0 : s0) + l16;
      const unsigned char* r1 = smem + (s1 < 65536 ? L_WL + s1 : s1) + l16;
#pragma unroll
      for (int t = 0; t < 4; ++t) { pa[t][s] = *(const f32x4*)(r0 + t * 1024); pb[t][s] = *(const f32x4*)(r1 + t * 1024); }
    }
    SB();
#pragma unroll
    for (int t = 0; t < 4; ++t) {
      const f32x4 x0 = (o0[t] + pa[t][0]) + (pa[t][1] + pa[t][2]), x1 = (o1[t] + pb[t][0]) + (pb[t][1] + pb[t][2]);
      const f32x2 qa = unp(z1p[0][t][0]) * f32x2{x0[0], x0[1]}, qb = unp(z1p[0][t][1]) * f32x2{x0[2], x0[3]};
      const f32x2 qc = unp(z1p[1][t][0]) * f32x2{x1[0], x1[1]}, qd = unp(z1p[1][t][1]) * f32x2{x1[2], x1[3]};
      PO[t] = v4u{min(pk_bf16(qa[0], qa[1]), SENT - 1u), min(pk_bf16(qb[0], qb[1]), SENT - 1u), min(pk_bf16(qc[0], qc[1]), SENT - 1u), min(pk_bf16(qd[0], qd[1]), SENT - 1u)};
      bst1(rs, pu16 + (t ^ wx) * (27 * 1024) + l16, PO[t]);
      bst1_64(rs, pu8 + (t ^ wx) * (27 * 1024) + l16, v2u{PO[t][0], PO[t][1]});
    }
  }
  SB();
  if (w == 3) {
#pragma unroll
    for (int t = 0; t < 4; ++t) PO[t] = v4u{0u, 0u, 0u, 0u};
  }
  if (j & 1) { const v4u a = PO[0], c = PO[2]; PO[0] = PO[1]; PO[1] = a; PO[2] = PO[3]; PO[3] = c; }
  if (j & 2) { const v4u a = PO[0], c = PO[1]; PO[0] = PO[2]; PO[2] = a; PO[1] = PO[3]; PO[3] = c; }
  f32x4 e0[5][4], e[5][4];
#pragma unroll
  for (int t = 0; t < 4; ++t)
#pragma unroll
    for (int d = 0; d < 5; ++d) e0[d][t] = mfma_bf(WO[d], PO[t], f32x4{0.f, 0.f, 0.f, 0.f});
  const int trow = 64 * panel + 8 * (i16 >> 2) + (i16 & 3) + 32 * (w >> 1) + 4 * (w & 1);
  int pof[4], eof[4];
#pragma unroll
  for (int q = 0; q < 4; ++q) { pof[q] = __builtin_amdgcn_readlane(otab, 24 + q); eof[q] = __builtin_amdgcn_readlane(otab, 28 + q); }
  const int ppo = lane < 48 ? O_P + (4 * panel + ((lane >> 1) & 3)) * (27 * 1024) + __builtin_amdgcn_ds_bpermute((17 + (lane >> 3)) * 4, otab) + 1012 + (lane & 1) * 8 : OOB;
  v4u gb0[5], gb1[5];
#pragma unroll
  for (int d = 0; d < 5; ++d) { gb0[d] = *(const v4u*)(smem + L_DUMMY + 2048 + (16 * d + 4 * g) * 8); gb1[d] = *(const v4u*)(smem + L_DUMMY + 2048 + (16 * d + 4 * g) * 8 + 16); }
  SB();
#pragma unroll
  for (int q = NWO; q < 35; ++q) WO_LD(q);
  SB();
  v4u P[24];
#define P_LD(q) P[q] = bld1(rs, l16, pof[((q) / 6 + 1) & 3] + kof[1 + (q) % 6])
  constexpr int PDN = PDNV;
#pragma unroll 1
  for (unsigned tries = 0; tries < 4u; ++tries) {
#pragma unroll 1
    for (unsigned s = 0; s < SPIN; ++s) {
      asm volatile("" ::: "memory");
      const unsigned m = __builtin_amdgcn_raw_buffer_load_b32(rs, ppo, 0, AUX_SC1);
#pragma unroll
      for (int q = 0; q < PDN; ++q) P_LD(q);
      if (!__any(m == SENT)) break;
    }
    asm volatile("" ::: "memory");
    unsigned pm = 0u;
    sfor<24 * 5>(LAM {
      constexpr int q = ic_ / 5, d = ic_ % 5, lo = q / 6, lt = (lo + 1) & 3, kk = 1 + q % 6;
      e[d][lt] = mfma_bf(WO[5 * kk + d], P[q], kk == 1 ? e0[d][lt] : e[d][lt]);
      if constexpr (d == 0 && q + PDN < 24) P_LD(q + PDN);
      if constexpr (d == 1) { pm = mx4(pm, P[q]); PIN(pm); }
      if constexpr (lo >= 1 && d == 3 && q % 6 < 5) *(f32x4*)(smem + eof[lo] + (q % 6) * 1024 + l16) = e[q % 6][lo];
      SB();
    });
    if (__builtin_expect(!__any(pm == SENT), 1)) break;
  }
  __syncthreads();
  u16* orow = p.out + (size_t)trow * D + 80 * j + 4 * g;
  f32x4 ep[5][3];
#pragma unroll
  for (int d = 0; d < 5; ++d)
#pragma unroll
    for (int s = 0; s < 3; ++s) ep[d][s] = *(const f32x4*)(smem + ((w * 3 + s) * 5 + d) * 1024 + l16);
  SB();
#pragma unroll
  for (int d = 0; d < 5; ++d) {
    const f32x4 eo = (e[d][0] + ep[d][0]) + (ep[d][1] + ep[d][2]);
    const float r0 = bflo(x1v[d][0]) + u2f(gb0[d][0]) * eo[0], r1 = bfhi(x1v[d][0]) + u2f(gb0[d][2]) * eo[1];
    const float r2 = bflo(x1v[d][1]) + u2f(gb1[d][0]) * eo[2], r3 = bfhi(x1v[d][1]) + u2f(gb1[d][2]) * eo[3];
    *(v2u*)(orow + 16 * d) = v2u{pk_bf16(r0, r1), pk_bf16(r2, r3)};
  }
  if (blockIdx.x == 0 && tid == 0) __hip_atomic_store((GAS unsigned*)(p.ws + O_EPOCH), tag, RLX_AGENT);
  asm volatile(".Lkf_end:");
}

extern "C" void kernel_launch(void* const* d_in, const int* in_sizes, int n_in, void* d_out, int out_size, void* d_ws,
                              size_t ws_size, hipStream_t stream) {
  Params p;
  p.u = (const u16*)d_in[0]; p.a_re = (const u16*)d_in[1]; p.a_im = (const u16*)d_in[2];
  p.b_re = (const u16*)d_in[3]; p.b_im = (const u16*)d_in[4]; p.c_re = (const u16*)d_in[5]; p.c_im = (const u16*)d_in[6];
  p.log_dt = (const u16*)d_in[7]; p.dskip = (const u16*)d_in[8];
  p.w_in = (const u16*)d_in[10]; p.w_sp = (const u16*)d_in[11]; p.b_sp = (const u16*)d_in[12]; p.w_out = (const u16*)d_in[13];
  p.cvec = (const u16*)d_in[14]; p.w_mod = (const u16*)d_in[15];
  p.out = (u16*)d_out; p.ws = (unsigned char*)d_ws;
  (void)hipFuncSetAttribute((const void*)k_fused, hipFuncAttributeMaxDynamicSharedMemorySize, L_TOTAL);
  k_fused<<<256, 256, L_TOTAL, stream>>>(p);
}
```
